```python
import torch

def _softmax(x, axis=-1):
    return torch.softmax(x, dim=axis)
def _gelu(x):
    return torch.nn.functional.gelu(x, approximate="tanh")
def _silu(x):
    return torch.nn.functional.silu(x)
def _sigmoid(x):
    return torch.sigmoid(x)
def _layernorm(x, axis=-1, eps=1e-5):
    m = x.mean(dim=axis, keepdim=True)
    v = x.var(dim=axis, keepdim=True, unbiased=False)
    return (x - m) / torch.sqrt(v + eps)
def _scatter_add(tgt, idx, src, axis=0):
    return tgt.clone().index_add_(axis, idx, src)
def _segment_sum(src, seg, n):
    out = torch.zeros((n,) + src.shape[1:], dtype=src.dtype)
    return out.index_add_(0, seg, src)
def _pad(x, pw):
    flat = tuple(v for pair in reversed(pw) for v in pair)
    return torch.nn.functional.pad(x, flat)

kernel_name = 'concat_heads__mha_block_cached__gqa_block_cached__decode'

def setup_inputs():

    _w = (lambda a: (lambda t: t.to(torch.bfloat16) if t.is_floating_point() else t)(torch.as_tensor(a)))
    def _A_setup():
        B, Sq, D = 8, 1, 512
        H, Sk, Dh = 32, 4096, 128
        return {
            "x": _w((lambda *s: torch.randn(*s, dtype=torch.bfloat16) * 0.8)(B, Sq, D)),
            "k_cache": _w((lambda *s: torch.randn(*s, dtype=torch.bfloat16) * 0.8)(B, H, Sk, Dh)),
            "v_cache": _w((lambda *s: torch.randn(*s, dtype=torch.bfloat16) * 0.05)(B, H, Sk, Dh)),
            "wq": _w((lambda *s: torch.randn(*s, dtype=torch.bfloat16) * 0.8)(D, H * Dh) * (D ** -0.5)),
            "wo": _w((lambda *s: torch.randn(*s, dtype=torch.bfloat16) * 0.05)(H * Dh, D) * ((H * Dh) ** -0.5)),
        }
    _A_in = _A_setup()

    def _B_setup():
        B, Sq, D = 8, 1, 512
        Hq, Hkv, Sk, Dh = 32, 8, 4096, 128
        assert Hq % Hkv == 0, (Hq, Hkv)
        return {
            "x": _w((lambda *s: torch.randn(*s, dtype=torch.bfloat16) * 0.8)(B, Sq, D)),
            "k_cache": _w((lambda *s: torch.randn(*s, dtype=torch.bfloat16) * 0.8)(B, Hkv, Sk, Dh)),
            "v_cache": _w((lambda *s: torch.randn(*s, dtype=torch.bfloat16) * 0.05)(B, Hkv, Sk, Dh)),
            "wq": _w((lambda *s: torch.randn(*s, dtype=torch.bfloat16) * 0.8)(D, Hq * Dh) * (D ** -0.5)),
            "wo": _w((lambda *s: torch.randn(*s, dtype=torch.bfloat16) * 0.05)(Hq * Dh, D) * ((Hq * Dh) ** -0.5)),
        }
    _B_in = _B_setup()

    return {
        "A_x": _A_in["x"],
        "A_k_cache": _A_in["k_cache"],
        "A_v_cache": _A_in["v_cache"],
        "A_wq": _A_in["wq"],
        "A_wo": _A_in["wo"],
        "B_x": _B_in["x"],
        "B_k_cache": _B_in["k_cache"],
        "B_v_cache": _B_in["v_cache"],
        "B_wq": _B_in["wq"],
        "B_wo": _B_in["wo"],
    }

def reference(A_x, A_k_cache, A_v_cache, A_wq, A_wo, B_x, B_k_cache, B_v_cache, B_wq, B_wo):
    def _A_ref(x, k_cache, v_cache, wq, wo):
        B, Sq, D = x.shape
        H, Dh = 32, 128
        q = (lambda x, *d: x.permute(*d))((x @ wq).reshape(B, Sq, H, Dh), 0, 2, 1, 3)
        scores = torch.einsum("bhqd,bhkd->bhqk", q, k_cache) * (Dh ** -0.5)
        p = _softmax(scores, axis=-1)
        o = torch.einsum("bhqk,bhkd->bhqd", p, v_cache)
        return (lambda x, *d: x.permute(*d))(o, 0, 2, 1, 3).reshape(B, Sq, H * Dh) @ wo

    def _B_ref(x, k_cache, v_cache, wq, wo):
        B, Sq, D = x.shape
        Hq, Dh = 32, 128
        Hkv = k_cache.shape[1]
        rep = Hq // Hkv
        q = (lambda x, *d: x.permute(*d))((x @ wq).reshape(B, Sq, Hq, Dh), 0, 2, 1, 3)
        k = (lambda x, n, axis: torch.repeat_interleave(x, n, dim=axis))(k_cache, rep, axis=1)
        v = (lambda x, n, axis: torch.repeat_interleave(x, n, dim=axis))(v_cache, rep, axis=1)
        scores = torch.einsum("bhqd,bhkd->bhqk", q, k) * (Dh ** -0.5)
        p = _softmax(scores, axis=-1)
        o = torch.einsum("bhqk,bhkd->bhqd", p, v)
        return (lambda x, *d: x.permute(*d))(o, 0, 2, 1, 3).reshape(B, Sq, Hq * Dh) @ wo

    a = _A_ref(A_x, A_k_cache, A_v_cache, A_wq, A_wo)
    b = _B_ref(B_x, B_k_cache, B_v_cache, B_wq, B_wo)
    return torch.concatenate([a, b], axis=-1)
```

```cpp
#include <hip/hip_runtime.h>
#include <hip/hip_bf16.h>
#include <stdint.h>

typedef unsigned v4u __attribute__((ext_vector_type(4)));
typedef unsigned v2u __attribute__((ext_vector_type(2)));
typedef float f32x4 __attribute__((ext_vector_type(4)));
typedef short s16x8 __attribute__((ext_vector_type(8)));
typedef short s16x4 __attribute__((ext_vector_type(4)));
typedef __bf16 bf16x8 __attribute__((ext_vector_type(8)));
typedef unsigned short u16;

constexpr int NB = 8, DM = 512, NH = 32, SK = 4096, DH = 128, HKV = 8;
constexpr float SC2 = 0.088388347648318440f * 1.4426950408889634f;

__device__ __forceinline__ float bf2f(u16 v) { return __builtin_bit_cast(float, (unsigned)v << 16); }
typedef __bf16 bf16x2 __attribute__((ext_vector_type(2)));
typedef float f32x2 __attribute__((ext_vector_type(2)));
__device__ __forceinline__ unsigned cvtpk(float lo, float hi) { return __builtin_bit_cast(unsigned, __builtin_convertvector(f32x2{lo, hi}, bf16x2)); }
__device__ __forceinline__ __amdgpu_buffer_rsrc_t mk(const void* p, unsigned bytes) { return __builtin_amdgcn_make_buffer_rsrc((void*)p, 0, bytes, 0x00020000); }
__device__ __forceinline__ f32x4 mfma32(v4u a, v4u b, f32x4 c) { return __builtin_amdgcn_mfma_f32_16x16x32_bf16(__builtin_bit_cast(bf16x8, a), __builtin_bit_cast(bf16x8, b), c, 0, 0, 0); }
__device__ __forceinline__ f32x4 mfma16(v2u a, v2u b, f32x4 c) { return __builtin_amdgcn_mfma_f32_16x16x16bf16_1k(__builtin_bit_cast(s16x4, a), __builtin_bit_cast(s16x4, b), c, 0, 0, 0); }
__device__ __forceinline__ float xg_max(float x) {
  auto s = __builtin_amdgcn_permlane16_swap(__float_as_uint(x), __float_as_uint(x), false, false);
  x = fmaxf(__uint_as_float(s[0]), __uint_as_float(s[1]));
  auto t = __builtin_amdgcn_permlane32_swap(__float_as_uint(x), __float_as_uint(x), false, false);
  return fmaxf(__uint_as_float(t[0]), __uint_as_float(t[1]));
}
__device__ __forceinline__ float xg_sum(float x) {
  auto s = __builtin_amdgcn_permlane16_swap(__float_as_uint(x), __float_as_uint(x), false, false);
  x = __uint_as_float(s[0]) + __uint_as_float(s[1]);
  auto t = __builtin_amdgcn_permlane32_swap(__float_as_uint(x), __float_as_uint(x), false, false);
  return __uint_as_float(t[0]) + __uint_as_float(t[1]);
}

__global__ void __launch_bounds__(256) k_qproj(const u16* xa, const u16* wqa, const u16* xb, const u16* wqb, u16* q) {
  __shared__ float xs[NB][DM];
  __shared__ float red[8][NB][32];
  const int p = blockIdx.x, mat = p >> 7, cb = p & 127, tid = threadIdx.x;
  const u16* x = mat ? xb : xa; const u16* w = mat ? wqb : wqa;
  for (int i = tid; i < NB * DM; i += 256) xs[i >> 9][i & 511] = bf2f(x[i]);
  __syncthreads();
  const int col = tid & 31, ks = tid >> 5;
  float acc[NB] = {0, 0, 0, 0, 0, 0, 0, 0};
  for (int k = ks * 64; k < ks * 64 + 64; ++k) { const float wv = bf2f(w[(size_t)k * 4096 + cb * 32 + col]);
#pragma unroll
    for (int b = 0; b < NB; ++b) acc[b] += xs[b][k] * wv; }
#pragma unroll
  for (int b = 0; b < NB; ++b) red[ks][b][col] = acc[b];
  __syncthreads();
  { const int b = tid >> 5; float s = 0; for (int i = 0; i < 8; ++i) s += red[i][b][col];
    q[(size_t)(mat * NB + b) * 4096 + cb * 32 + col] = (u16)(cvtpk(s, s) & 0xffff); }
}

struct St { float m, l; f32x4 a0, a1; };
__device__ __forceinline__ void tile_math(const v4u (&K)[4], const v4u (&V)[4], const v4u (&q)[4], St& s, unsigned sl0, unsigned sl1, unsigned sh0, unsigned sh1) {
  f32x4 sc = {0.f, 0.f, 0.f, 0.f};
#pragma unroll
  for (int m = 0; m < 4; ++m) sc = mfma32(K[m], q[m], sc);
  const float tmax = fmaxf(fmaxf(sc[0], sc[1]), fmaxf(sc[2], sc[3]));
  if (__builtin_amdgcn_ballot_w64(tmax > s.m) != 0) {
    const float mn = fmaxf(s.m, xg_max(tmax));
    const float al = __builtin_amdgcn_exp2f((s.m - mn) * SC2);
    s.a0 *= al; s.a1 *= al; s.l *= al; s.m = mn;
  }
  const float mc = -s.m * SC2;
  float p[4];
#pragma unroll
  for (int i = 0; i < 4; ++i) p[i] = __builtin_amdgcn_exp2f(fmaf(sc[i], SC2, mc));
  s.l += (p[0] + p[1]) + (p[2] + p[3]);
  const unsigned pk01 = cvtpk(p[0], p[1]), pk23 = cvtpk(p[2], p[3]);
#pragma unroll
  for (int i = 0; i < 4; ++i) {
    const unsigned pk = i < 2 ? pk01 : pk23;
    v2u bf = {__builtin_amdgcn_perm(pk, pk, (i & 1) ? sh0 : sl0), __builtin_amdgcn_perm(pk, pk, (i & 1) ? sh1 : sl1)};
    s.a0 = mfma16(v2u{V[i][0], V[i][1]}, bf, s.a0);
    s.a1 = mfma16(v2u{V[i][2], V[i][3]}, bf, s.a1);
  }
}

constexpr int W = 4, DEPTH = 4;
template <int NT>
__device__ __forceinline__ void run_seg(__amdgpu_buffer_rsrc_t rk, __amdgpu_buffer_rsrc_t rv, int wave, int koff, int voff, const v4u (&q)[4], St& s, unsigned sl0, unsigned sl1, unsigned sh0, unsigned sh1) {
  v4u K[DEPTH][4], V[DEPTH][4];
#define ISSUE(t_, d_) do { const int so_ = ((t_) * W + wave) * 4096; \
    _Pragma("unroll") for (int l = 0; l < 4; ++l) K[d_][l] = __builtin_amdgcn_raw_buffer_load_b128(rk, koff + l * 64, so_, 2); \
    _Pragma("unroll") for (int l = 0; l < 4; ++l) V[d_][l] = __builtin_amdgcn_raw_buffer_load_b128(rv, voff + l * 256, so_, 2); \
    __builtin_amdgcn_sched_barrier(0); } while (0)
#pragma unroll
  for (int d = 0; d < DEPTH; ++d) ISSUE(d, d);
#pragma unroll 1
  for (int t = 0; t < NT; t += DEPTH) {
#pragma unroll
    for (int d = 0; d < DEPTH; ++d) {
      tile_math(K[d], V[d], q, s, sl0, sl1, sh0, sh1);
      __builtin_amdgcn_sched_barrier(0);
      ISSUE(t + d + DEPTH, d);
    }
  }
#undef ISSUE
}

__global__ void __launch_bounds__(W * 64) k_attn(const u16* ak, const u16* av, const u16* bk, const u16* bv, const u16* q, u16* oa, float* pb) {
  extern __shared__ __attribute__((aligned(16))) float lds[];
  const int wg = blockIdx.x, lane = threadIdx.x & 63, wave = __builtin_amdgcn_readfirstlane(threadIdx.x >> 6);
  const int xcd = wg & 7, j = wg >> 3, b = j >> 2, sp = j & 3, h = 4 * xcd + sp;
  const int g = lane >> 4, n = lane & 15, jj = n >> 2;
  const int koff = n * 256 + g * 16, voff = g * 1024 + n * 16;
  const unsigned Z = 0x0C0C0C0Cu;
  const unsigned sl0 = jj == 0 ? 0x0C0C0100u : jj == 1 ? 0x01000C0Cu : Z, sl1 = jj == 2 ? 0x0C0C0100u : jj == 3 ? 0x01000C0Cu : Z;
  const unsigned sh0 = jj == 0 ? 0x0C0C0302u : jj == 1 ? 0x03020C0Cu : Z, sh1 = jj == 2 ? 0x0C0C0302u : jj == 3 ? 0x03020C0Cu : Z;
  float* rec = lds;
  for (int seg = 0; seg < 2; ++seg) {
    v4u qf[4];
    const u16* qp = seg == 0 ? q + (size_t)(NB + b) * 4096 + (xcd * 4 + (n & 3)) * 128 : q + (size_t)b * 4096 + h * 128;
#pragma unroll
    for (int m = 0; m < 4; ++m) qf[m] = *(const v4u*)(qp + 32 * m + 8 * g);
    St s; s.m = -3.0e38f; s.l = 0.f; s.a0 = f32x4{0, 0, 0, 0}; s.a1 = f32x4{0, 0, 0, 0};
    if (seg == 0) run_seg<1024 / 16 / W>(mk(bk + ((size_t)(b * HKV + xcd) * SK + sp * 1024) * DH, 1024 * 256), mk(bv + ((size_t)(b * HKV + xcd) * SK + sp * 1024) * DH, 1024 * 256), wave, koff, voff, qf, s, sl0, sl1, sh0, sh1);
    else run_seg<4096 / 16 / W>(mk(ak + (size_t)(b * NH + h) * SK * DH, SK * 256), mk(av + (size_t)(b * NH + h) * SK * DH, SK * 256), wave, koff, voff, qf, s, sl0, sl1, sh0, sh1);
    const float lt = xg_sum(s.l);
    float* r = rec + (wave * 4 + (n & 3)) * 132;
#pragma unroll
    for (int i = 0; i < 4; ++i) { r[4 + 32 * g + 8 * i + jj] = s.a0[i]; r[4 + 32 * g + 8 * i + 4 + jj] = s.a1[i]; }
    if (lane < 4) { r[0] = s.m; r[1] = lt; }
    __syncthreads();
    const int hh = wave; float M = -3.0e38f;
    for (int w = 0; w < W; ++w) M = fmaxf(M, rec[(w * 4 + hh) * 132]);
    float L = 0, o0 = 0, o1 = 0;
    for (int w = 0; w < W; ++w) { const float* rr = rec + (w * 4 + hh) * 132; const float f = __builtin_amdgcn_exp2f((rr[0] - M) * SC2);
      L += f * rr[1]; o0 += f * rr[4 + 2 * lane]; o1 += f * rr[5 + 2 * lane]; }
    if (seg == 0) { float* o = pb + ((size_t)(b * NH + xcd * 4 + hh) * 4 + sp) * 130; if (lane == 0) { o[0] = M; o[1] = L; } o[2 + 2 * lane] = o0; o[3 + 2 * lane] = o1; }
    else if (wave == 0) { const float inv = 1.f / L; *(unsigned*)(oa + (size_t)b * 4096 + h * 128 + 2 * lane) = cvtpk(o0 * inv, o1 * inv); }
    __syncthreads();
  }
}

__global__ void __launch_bounds__(256) k_out(const u16* oa, const float* pb, const u16* woa, const u16* wob, u16* out) {
  __shared__ float os[4096];
  __shared__ float red[16][16];
  const int p = blockIdx.x, mat = p >> 8, b = (p >> 5) & 7, cb = p & 31, tid = threadIdx.x;
  if (mat == 0) { for (int i = tid; i < 4096; i += 256) os[i] = bf2f(oa[(size_t)b * 4096 + i]); }
  else { for (int i = tid; i < 4096; i += 256) { const int h = i >> 7, d = i & 127; const float* r = pb + (size_t)(b * NH + h) * 4 * 130;
      const float M = fmaxf(fmaxf(r[0], r[130]), fmaxf(r[260], r[390])); float L = 0, o = 0;
      for (int s = 0; s < 4; ++s) { const float f = __builtin_amdgcn_exp2f((r[s * 130] - M) * SC2); L += f * r[s * 130 + 1]; o += f * r[s * 130 + 2 + d]; }
      o /= L; os[i] = bf2f((u16)(cvtpk(o, o) & 0xffff)); } }
  __syncthreads();
  const u16* w = mat ? wob : woa;
  const int col = tid & 15, ks = tid >> 4; float acc = 0;
  for (int k = ks; k < 4096; k += 16) acc += os[k] * bf2f(w[(size_t)k * 512 + cb * 16 + col]);
  red[ks][col] = acc;
  __syncthreads();
  if (tid < 16) { float s = 0; for (int i = 0; i < 16; ++i) s += red[i][tid]; out[(size_t)b * 1024 + mat * 512 + cb * 16 + tid] = (u16)(cvtpk(s, s) & 0xffff); }
}

extern "C" void kernel_launch(void* const* d_in, const int* in_sizes, int n_in, void* d_out, int out_size, void* d_ws, size_t ws_size, hipStream_t stream) {
  const u16* xa = (const u16*)d_in[0]; const u16* ak = (const u16*)d_in[1]; const u16* av = (const u16*)d_in[2]; const u16* wqa = (const u16*)d_in[3]; const u16* woa = (const u16*)d_in[4];
  const u16* xb = (const u16*)d_in[5]; const u16* bk = (const u16*)d_in[6]; const u16* bv = (const u16*)d_in[7]; const u16* wqb = (const u16*)d_in[8]; const u16* wob = (const u16*)d_in[9];
  u16* q = (u16*)d_ws; u16* oa = q + 2 * 8 * 4096; float* pb = (float*)(oa + 8 * 4096);
  hipLaunchKernelGGL(k_qproj, dim3(256), dim3(256), 0, stream, xa, wqa, xb, wqb, q);
  hipLaunchKernelGGL(k_attn, dim3(256), dim3(W * 64), 16384, stream, ak, av, bk, bv, q, oa, pb);
  hipLaunchKernelGGL(k_out, dim3(512), dim3(256), 0, stream, oa, pb, woa, wob, (u16*)d_out);
}
```

```cpp
#include <hip/hip_runtime.h>
#include <hip/hip_bf16.h>
#include <stdint.h>

typedef unsigned v4u __attribute__((ext_vector_type(4)));
typedef unsigned v2u __attribute__((ext_vector_type(2)));
typedef int v4i __attribute__((ext_vector_type(4)));
typedef float f32x4 __attribute__((ext_vector_type(4)));
typedef float f32x2 __attribute__((ext_vector_type(2)));
typedef short s16x4 __attribute__((ext_vector_type(4)));
typedef __bf16 bf16x8 __attribute__((ext_vector_type(8)));
typedef __bf16 bf16x2 __attribute__((ext_vector_type(2)));
typedef unsigned short u16;
typedef unsigned long long u64;
#define GAS __attribute__((address_space(1)))
#define LAS __attribute__((address_space(3)))
#define RLX_AGENT __ATOMIC_RELAXED, __HIP_MEMORY_SCOPE_AGENT

struct CfgDefault {
  static constexpr int NFL = 20;
  static constexpr int N1 = 64;
  static constexpr int WO_AUX = 0;
  static constexpr int WO_SLEEP = 12;
  static constexpr int WQ_LOADER = 1;
  static constexpr int PRE = 4;
  static constexpr int DBG = 0;
};

constexpr int SK = 4096, DH = 128, NH = 32, HKV = 8;
constexpr float SC2 = 0.088388347648318440f * 1.4426950408889634f;
constexpr int NWAVE = 10, NTHR = NWAVE * 64;
constexpr int NTILE = 320, NTB = 64;
constexpr int RING = 15, SLOT = 8192;
constexpr int L_MISC = 0;
constexpr int L_WQ = 8192;
constexpr int L_RING = L_WQ + 32768;
constexpr int L_FULL = L_MISC, L_QA = L_MISC + 4, L_QB = L_MISC + 8, L_BCNT = L_MISC + 12, L_DONE = L_MISC + 16, L_ACNT = L_MISC + 32, L_OB = L_MISC + 36, L_TAG = L_MISC + 40, L_WQF = L_MISC + 44;
constexpr int L_QIMG = L_MISC + 64;
constexpr int L_OIMG = L_QIMG + 1280;
constexpr int REC = 528;
constexpr int L_AREC = L_OIMG + 1280;
constexpr int LDS_BYTES = 160 * 1024;
static_assert(L_AREC + 4 * REC <= L_WQ && 16 * REC <= 32768 && L_RING + RING * SLOT <= LDS_BYTES, "lds map");
constexpr size_t W_EPOCH = 0;
constexpr size_t W_FLAG = 4096;
constexpr size_t W_QG = 65536;
constexpr size_t W_PB = W_QG + 2 * 8 * 2048 * 8;
constexpr size_t W_YG = W_PB + 256 * 4 * 136 * 4;
constexpr size_t W_END = W_YG + (size_t)2 * 8 * 32 * 512 * 8;
constexpr size_t W_STAMP = 64u << 20;
constexpr unsigned SPIN_CAP = 1u << 21;

__device__ __forceinline__ unsigned cvtpk(float lo, float hi) { return __builtin_bit_cast(unsigned, __builtin_convertvector(f32x2{lo, hi}, bf16x2)); }
__device__ __forceinline__ __amdgpu_buffer_rsrc_t mk(const void* p, unsigned bytes) { return __builtin_amdgcn_make_buffer_rsrc((void*)p, 0, bytes, 0x00020000); }
__device__ __forceinline__ v4i mksrd(const void* p, unsigned bytes) { return v4i{(int)(uintptr_t)p, (int)((uintptr_t)p >> 32), (int)bytes, 0x00020000}; }
__device__ __forceinline__ f32x4 mfma32(v4u a, v4u b, f32x4 c) { return __builtin_amdgcn_mfma_f32_16x16x32_bf16(__builtin_bit_cast(bf16x8, a), __builtin_bit_cast(bf16x8, b), c, 0, 0, 0); }
__device__ __forceinline__ f32x4 mfma16(v2u a, v2u b, f32x4 c) { return __builtin_amdgcn_mfma_f32_16x16x16bf16_1k(__builtin_bit_cast(s16x4, a), __builtin_bit_cast(s16x4, b), c, 0, 0, 0); }
__device__ __forceinline__ float ex2(float x) { return __builtin_amdgcn_exp2f(x); }
__device__ __forceinline__ float xg_max(float x) {
  auto s = __builtin_amdgcn_permlane16_swap(__float_as_uint(x), __float_as_uint(x), false, false);
  x = fmaxf(__uint_as_float(s[0]), __uint_as_float(s[1]));
  auto t = __builtin_amdgcn_permlane32_swap(__float_as_uint(x), __float_as_uint(x), false, false);
  return fmaxf(__uint_as_float(t[0]), __uint_as_float(t[1]));
}
__device__ __forceinline__ float xg_sum(float x) {
  auto s = __builtin_amdgcn_permlane16_swap(__float_as_uint(x), __float_as_uint(x), false, false);
  x = __uint_as_float(s[0]) + __uint_as_float(s[1]);
  auto t = __builtin_amdgcn_permlane32_swap(__float_as_uint(x), __float_as_uint(x), false, false);
  return __uint_as_float(t[0]) + __uint_as_float(t[1]);
}
#define CBAR() asm volatile("" ::: "memory")
#define LDSP(T, off) ((LAS T*)(lds + (off)))
__device__ __forceinline__ unsigned lds_ld(LAS unsigned char* lds, int off) { return __builtin_amdgcn_readfirstlane(*(volatile LAS unsigned*)(lds + off)); }
__device__ __forceinline__ void lds_st(LAS unsigned char* lds, int off, unsigned v) { *(volatile LAS unsigned*)(lds + off) = v; }
__device__ __forceinline__ unsigned lds_wait_ge(LAS unsigned char* lds, int off, unsigned want) {
  unsigned v = lds_ld(lds, off);
#pragma unroll 1
  for (unsigned sp = 0; v < want && sp < SPIN_CAP * 8; ++sp) { __builtin_amdgcn_s_sleep(1); v = lds_ld(lds, off); }
  CBAR();
  return v;
}
#define STAMP(i) do {} while (0)

template <int NFL> __device__ __forceinline__ void dma4(unsigned m0v, unsigned v0, unsigned v1, unsigned v2, unsigned v3, v4i srd, unsigned so) {
  asm volatile("s_mov_b32 m0, %0\n\t"
               "s_waitcnt vmcnt(%7)\n\tbuffer_load_dwordx4 %1, %5, %6 offen nt lds\n\t"
               "s_waitcnt vmcnt(%7)\n\tbuffer_load_dwordx4 %2, %5, %6 offen offset:1024 nt lds\n\t"
               "s_waitcnt vmcnt(%7)\n\tbuffer_load_dwordx4 %3, %5, %6 offen offset:2048 nt lds\n\t"
               "s_waitcnt vmcnt(%7)\n\tbuffer_load_dwordx4 %4, %5, %6 offen offset:3072 nt lds"
               :: "s"(m0v), "v"(v0), "v"(v1), "v"(v2), "v"(v3), "s"(srd), "s"(so), "i"(NFL - 1) : "memory");
}

struct St { float m, l; f32x4 a0, a1; };
struct Sel { unsigned l0, l1, h0, h1; };
__device__ __forceinline__ void tile_math(const v4u (&K)[4], const v4u (&V)[4], const v4u (&q)[4], St& s, const Sel& e) {
  f32x4 sc = {0.f, 0.f, 0.f, 0.f};
#pragma unroll
  for (int m = 0; m < 4; ++m) sc = mfma32(K[m], q[m], sc);
  const float tmax = fmaxf(fmaxf(sc[0], sc[1]), fmaxf(sc[2], sc[3]));
  if (__builtin_amdgcn_ballot_w64(tmax > s.m) != 0) {
    const float mn = fmaxf(s.m, xg_max(tmax));
    const float al = ex2((s.m - mn) * SC2);
    s.a0 *= al; s.a1 *= al; s.l *= al; s.m = mn;
  }
  const float mc = -s.m * SC2;
  float p[4];
#pragma unroll
  for (int i = 0; i < 4; ++i) p[i] = ex2(fmaf(sc[i], SC2, mc));
  s.l += (p[0] + p[1]) + (p[2] + p[3]);
  const unsigned pk01 = cvtpk(p[0], p[1]), pk23 = cvtpk(p[2], p[3]);
#pragma unroll
  for (int i = 0; i < 4; ++i) {
    const unsigned pk = i < 2 ? pk01 : pk23;
    const v2u bf = {__builtin_amdgcn_perm(pk, pk, (i & 1) ? e.h0 : e.l0), __builtin_amdgcn_perm(pk, pk, (i & 1) ? e.h1 : e.l1)};
    s.a0 = mfma16(v2u{V[i][0], V[i][1]}, bf, s.a0);
    s.a1 = mfma16(v2u{V[i][2], V[i][3]}, bf, s.a1);
  }
}

template <class C>
__global__ void __launch_bounds__(NTHR, 1) fused_decode(const u16* xa, const u16* ak, const u16* av, const u16* wqa, const u16* woa,
                                                        const u16* xb, const u16* bk, const u16* bv, const u16* wqb, const u16* wob,
                                                        u16* out, unsigned char* ws) {
  constexpr int NFL = C::NFL, N1 = C::N1, WO_AUX = C::WO_AUX, WO_SLEEP = C::WO_SLEEP, PRE = C::PRE;
  extern __shared__ __attribute__((aligned(16))) unsigned char lds_raw[];
  LAS unsigned char* lds = (LAS unsigned char*)lds_raw;
  const int wg = blockIdx.x, lane = threadIdx.x & 63, wave = __builtin_amdgcn_readfirstlane(threadIdx.x >> 6);
  const int xcd = wg & 7, jw = wg >> 3, b = jw >> 2, sp = jw & 3, h = 4 * xcd + sp;
  const int g = lane >> 4, n = lane & 15;
  const unsigned lds0 = (unsigned)(uintptr_t)lds;
  const int mat = jw >> 4, cb = xcd * 16 + (jw & 15);
#define WQ_ISSUE() do { \
      const v4i rw = mksrd((mat ? wqb : wqa) + cb * 32, 512 * 8192 - cb * 64); \
      const unsigned vo = (lane >> 2) * 8192 + (lane & 3) * 16; \
      _Pragma("unroll") for (int i = 0; i < 32; ++i) \
        asm volatile("s_mov_b32 m0, %0\n\ts_nop 0\n\tbuffer_load_dwordx4 %1, %2, %3 offen nt lds" :: "s"(lds0 + L_WQ + i * 1024), "v"(vo), "s"(rw), "s"(i * 16 * 8192) : "memory"); \
    } while (0)
  if (threadIdx.x < 16) *(volatile LAS unsigned*)(lds + L_MISC + 4 * lane) = 0u;
  asm volatile("s_waitcnt lgkmcnt(0)" ::: "memory");
  __builtin_amdgcn_s_barrier();
  CBAR();

  if (wave == 1) {
    __builtin_amdgcn_s_setprio(3);
    const v4i rak = mksrd(ak + (size_t)(b * NH + h) * SK * DH, SK * 256), rav = mksrd(av + (size_t)(b * NH + h) * SK * DH, SK * 256);
    const v4i rbk = mksrd(bk + ((size_t)(b * HKV + xcd) * SK + sp * 1024) * DH, 1024 * 256), rbv = mksrd(bv + ((size_t)(b * HKV + xcd) * SK + sp * 1024) * DH, 1024 * 256);
    const unsigned k0 = g * 256 + ((n ^ g) * 16), k1 = k0 ^ 64, k2 = k0 ^ 128, k3 = k0 ^ 192, vv = lane * 16;
    unsigned slot = lds0 + L_RING;
    int limit = RING;
    constexpr int LAGK = (NFL + 4 + 7) / 8, LAGV = (NFL + 7) / 8;
    static_assert(NFL >= 17 && NFL <= 32, "publish lag");
    STAMP(0);
    if (C::WQ_LOADER) WQ_ISSUE();
    int t = 0;
#pragma unroll 1
    for (int seg = 0; seg < 3; ++seg) {
      const v4i rk = seg == 1 ? rbk : rak, rv = seg == 1 ? rbv : rav;
      const int tend = seg == 0 ? N1 : seg == 1 ? N1 + NTB : NTILE;
      unsigned so = seg == 2 ? N1 * 4096u : 0u;
#pragma unroll 1
      for (; t < tend; ++t) {
        if (t >= limit) {
#pragma unroll 1
          for (unsigned spn = 0; spn < SPIN_CAP * 8; ++spn) {
            const v4u d = *(volatile LAS v4u*)(lds + L_DONE);
            const int c = min(min(4 * (int)d[0], 4 * (int)d[1] + 1), min(4 * (int)d[2] + 2, 4 * (int)d[3] + 3));
            limit = __builtin_amdgcn_readfirstlane(c) + RING;
            if (t < limit) break;
            __builtin_amdgcn_s_sleep(1);
          }
        }
        dma4<NFL>(slot, k0, k1, k2, k3, rk, so);
        if (LAGK == LAGV && t >= LAGK) lds_st(lds, L_FULL, (unsigned)(t - LAGK + 1));
        dma4<NFL>(slot + 4096, vv, vv, vv, vv, rv, so);
        if (LAGK != LAGV && t >= LAGV) lds_st(lds, L_FULL, (unsigned)(t - LAGV + 1));
        if (C::WQ_LOADER && t == LAGV - 1) lds_st(lds, L_WQF, 1u);
        slot += SLOT; if (slot >= lds0 + L_RING + RING * SLOT) slot = lds0 + L_RING;
        so += 4096u;
      }
    }
    asm volatile("s_waitcnt vmcnt(24)" ::: "memory"); lds_st(lds, L_FULL, NTILE - 3);
    asm volatile("s_waitcnt vmcnt(16)" ::: "memory"); lds_st(lds, L_FULL, NTILE - 2);
    asm volatile("s_waitcnt vmcnt(8)" ::: "memory");  lds_st(lds, L_FULL, NTILE - 1);
    asm volatile("s_waitcnt vmcnt(0)" ::: "memory");  lds_st(lds, L_FULL, NTILE);
    STAMP(1);
  } else if (wave >= 2 && wave < 6) {
    const int w = wave - 2, jj = n >> 2;
    const unsigned Z = 0x0C0C0C0Cu;
    Sel e;
    e.l0 = jj == 0 ? 0x0C0C0100u : jj == 1 ? 0x01000C0Cu : Z; e.l1 = jj == 2 ? 0x0C0C0100u : jj == 3 ? 0x01000C0Cu : Z;
    e.h0 = jj == 0 ? 0x0C0C0302u : jj == 1 ? 0x03020C0Cu : Z; e.h1 = jj == 2 ? 0x0C0C0302u : jj == 3 ? 0x03020C0Cu : Z;
    const int kb = n * 256 + ((g ^ n) * 16), vb = 4096 + g * 1024 + n * 16;
    int slot = L_RING + w * SLOT; unsigned full = 0, cnt = 0; int t = w;
    St sa; sa.m = -3.0e38f; sa.l = 0.f; sa.a0 = f32x4{0, 0, 0, 0}; sa.a1 = f32x4{0, 0, 0, 0};
#define TILE_READ(K_, V_) do { \
        if (full <= (unsigned)t) full = lds_wait_ge(lds, L_FULL, (unsigned)t + 1u); \
        CBAR(); \
        _Pragma("unroll") for (int m = 0; m < 4; ++m) K_[m] = *LDSP(v4u, (slot + kb) ^ (64 * m)); \
        _Pragma("unroll") for (int i_ = 0; i_ < 4; ++i_) V_[i_] = *LDSP(v4u, slot + vb + 256 * i_); \
        CBAR(); \
        ++cnt; lds_st(lds, L_DONE + 4 * w, cnt);     \
        CBAR(); \
        slot += 4 * SLOT; if (slot >= L_RING + RING * SLOT) slot -= RING * SLOT; \
        t += 4; } while (0)
    if (PRE > 0) {
      static_assert(4 * PRE <= N1, "parked tiles belong to the first A segment");
      v4u KP[PRE > 0 ? PRE : 1][4], VP[PRE > 0 ? PRE : 1][4];
#pragma unroll
      for (int i = 0; i < PRE; ++i) TILE_READ(KP[i], VP[i]);
      lds_wait_ge(lds, L_QA, 1u);
      v4u q[4];
#pragma unroll
      for (int m = 0; m < 4; ++m) q[m] = *LDSP(v4u, L_QIMG + g * 16 + 64 * m);
#pragma unroll
      for (int i = 0; i < PRE; ++i) tile_math(KP[i], VP[i], q, sa, e);
    }
#pragma unroll 1
    for (int seg = 0; seg < 3; ++seg) {
      const int tend = seg == 0 ? N1 : seg == 1 ? N1 + NTB : NTILE;
      lds_wait_ge(lds, seg == 1 ? L_QB : L_QA, 1u);
      v4u q[4];
      const int qo = L_QIMG + (seg == 1 ? 256 + (n & 3) * 256 : 0) + g * 16;
#pragma unroll
      for (int m = 0; m < 4; ++m) q[m] = *LDSP(v4u, qo + 64 * m);
      St s = sa;
      if (seg == 1) { s.m = -3.0e38f; s.l = 0.f; s.a0 = f32x4{0, 0, 0, 0}; s.a1 = f32x4{0, 0, 0, 0}; }
      if (seg == 0 && w == 0) STAMP(2);
#pragma unroll 1
      while (t < tend) {
        v4u K[4], V[4];
        TILE_READ(K, V);
        tile_math(K, V, q, s, e);
      }
      if (seg == 0) { sa = s; continue; }
      const float lt = xg_sum(s.l);
      const int ro = seg == 1 ? L_WQ + (w * 4 + (n & 3)) * REC : L_AREC + w * REC;
      if (seg == 1 || (n & 3) == 0) {
#pragma unroll
        for (int i = 0; i < 4; ++i) { *LDSP(float, ro + 16 + 4 * (32 * g + 8 * i + jj)) = s.a0[i]; *LDSP(float, ro + 16 + 4 * (32 * g + 8 * i + 4 + jj)) = s.a1[i]; }
        if (lane < 4) *LDSP(f32x2, ro) = f32x2{s.m, lt};
      }
      CBAR();
      if (lane == 0) __hip_atomic_fetch_add(LDSP(unsigned, seg == 1 ? L_BCNT : L_ACNT), 1u, __ATOMIC_RELAXED, __HIP_MEMORY_SCOPE_WORKGROUP);
      CBAR();
    }
    if (w == 0) STAMP(3);
  } else if (wave >= 6) {
    const int w = wave - 6;
    const __amdgpu_buffer_rsrc_t ry = mk(ws + W_YG, 2 * 8 * 32 * 512 * 8);
    const int yo = ((b * 32 + h) * 512 + 128 * w + 8 * n + 4 * (g & 1) + 2 * (g >> 1)) * 8;
    __builtin_amdgcn_raw_buffer_store_b128(v4u{0, 0, 0, 0}, ry, yo, 0, 16);
    __builtin_amdgcn_raw_buffer_store_b128(v4u{0, 0, 0, 0}, ry, yo, 8 * 32 * 512 * 8, 16);
    unsigned sl[4], sh[4];
#pragma unroll
    for (int d = 0; d < 4; ++d) { const bool on = ((n & 7) >> 1) == d; sl[d] = !on ? 0x0C0C0C0Cu : (n & 1) ? 0x01000C0Cu : 0x0C0C0100u; sh[d] = !on ? 0x0C0C0C0Cu : (n & 1) ? 0x03020C0Cu : 0x0C0C0302u; }
#pragma unroll 1
    for (int pass = 0; pass < 2; ++pass) {
      const __amdgpu_buffer_rsrc_t rw = mk((pass == 0 ? wob : woa) + (size_t)h * 128 * 512, (C::DBG & 1) ? 0 : 128 * 1024);
      v4u wo[32];
      __builtin_amdgcn_s_sleep(100);
#pragma unroll
      for (int i = 0; i < 32; ++i) {
        wo[i] = __builtin_amdgcn_raw_buffer_load_b128(rw, g * 32768 + w * 256 + n * 16, i * 1024, WO_AUX);
        __builtin_amdgcn_sched_barrier(0);
        __builtin_amdgcn_s_sleep(WO_SLEEP);
        __builtin_amdgcn_sched_barrier(0);
      }
      int oi;
      if (pass == 0) { lds_wait_ge(lds, L_OB, 1u); oi = L_OIMG; }
      else {
        asm volatile("s_waitcnt vmcnt(0)" ::: "memory");
        lds_wait_ge(lds, L_ACNT, 4u);
        f32x2 ml[4], ac[4];
#pragma unroll
        for (int u = 0; u < 4; ++u) { ml[u] = *LDSP(f32x2, L_AREC + u * REC); ac[u] = *LDSP(f32x2, L_AREC + u * REC + 16 + 8 * lane); }
        const float M = fmaxf(fmaxf(ml[0][0], ml[1][0]), fmaxf(ml[2][0], ml[3][0]));
        float L = 0.f, o0 = 0.f, o1 = 0.f;
#pragma unroll
        for (int u = 0; u < 4; ++u) { const float f = ex2((ml[u][0] - M) * SC2); L = fmaf(f, ml[u][1], L); o0 = fmaf(f, ac[u][0], o0); o1 = fmaf(f, ac[u][1], o1); }
        const float inv = 1.f / L;
        oi = L_OIMG + 256 + w * 256;
        *LDSP(unsigned, oi + 4 * lane) = cvtpk(o0 * inv, o1 * inv);
        CBAR();
      }
      v4u of[4];
#pragma unroll
      for (int u = 0; u < 4; ++u) of[u] = *LDSP(v4u, oi + 64 * g + 16 * u);
      const unsigned tag = lds_ld(lds, L_TAG);
      f32x4 y0 = {0, 0, 0, 0}, y1 = {0, 0, 0, 0};
#pragma unroll
      for (int i = 0; i < 32; i += 2) {
        const unsigned pk = of[i >> 3][(i >> 1) & 3];
        v4u a0, a1;
#pragma unroll
        for (int d = 0; d < 4; ++d) { a0[d] = __builtin_amdgcn_perm(pk, pk, sl[d]); a1[d] = __builtin_amdgcn_perm(pk, pk, sh[d]); }
        y0 = mfma32(a0, wo[i], y0); y1 = mfma32(a1, wo[i + 1], y1);
      }
      y0 += y1;
      const float va = g < 2 ? y0[0] : y0[2], vb = g < 2 ? y0[1] : y0[3];
      __builtin_amdgcn_raw_buffer_store_b128(v4u{__float_as_uint(va), tag, __float_as_uint(vb), tag}, ry, yo, pass == 0 ? 8 * 32 * 512 * 8 : 0, 16);
      if (w == 0) STAMP(4 + pass);
    }
  } else {
    unsigned G = __hip_atomic_load((GAS unsigned*)(ws + W_EPOCH), RLX_AGENT);
    const __amdgpu_buffer_rsrc_t rq = mk(ws + W_QG, 2 * 8 * 2048 * 8);
    const int qso = ((mat * 8 + (n & 7)) * 2048 + cb * 16 + (n >> 3) * 8 + 2 * g) * 8;
    __builtin_amdgcn_raw_buffer_store_b128(v4u{0, 0, 0, 0}, rq, qso, 0, 16);
    if (!C::WQ_LOADER) WQ_ISSUE();
    v4u xf[16];
    {
      const u16* xp = (mat ? xb : xa) + (n & 7) * 512 + 8 * g;
#pragma unroll
      for (int s = 0; s < 16; ++s) xf[s] = *(const GAS v4u*)(xp + 32 * s);
    }
    asm volatile("s_waitcnt vmcnt(0)" : "+v"(G) :: "memory");
    unsigned tag = __builtin_amdgcn_readfirstlane(G) + 1u; if (tag == 0u) tag = 1u;
    lds_st(lds, L_TAG, tag);
    STAMP(12);
    if (C::WQ_LOADER) lds_wait_ge(lds, L_WQF, 1u);
    STAMP(6);
    f32x4 d0 = {0, 0, 0, 0}, d1 = {0, 0, 0, 0};
    {
      const int ta = L_WQ + (8 * g + (n >> 2)) * 64 + (n & 3) * 8;
#pragma unroll
      for (int s4 = 0; s4 < 16; s4 += 8) {
        v2u t[8][4];
#pragma unroll
        for (int s = 0; s < 8; ++s)
#pragma unroll
          for (int u = 0; u < 4; ++u) t[s][u] = __builtin_bit_cast(v2u, __builtin_amdgcn_ds_read_tr16_b64_v4i16(LDSP(s16x4, ta + (s4 + s) * 2048 + (u & 1) * 256 + (u >> 1) * 32)));
        __builtin_amdgcn_sched_barrier(0);
#pragma unroll
        for (int s = 0; s < 8; ++s) {
          d0 = mfma32(v4u{t[s][0][0], t[s][0][1], t[s][1][0], t[s][1][1]}, xf[s4 + s], d0);
          d1 = mfma32(v4u{t[s][2][0], t[s][2][1], t[s][3][0], t[s][3][1]}, xf[s4 + s], d1);
        }
        __builtin_amdgcn_sched_barrier(0);
      }
    }
    const f32x4 dd = n < 8 ? d0 : d1;
    __builtin_amdgcn_raw_buffer_store_b128(v4u{cvtpk(dd[0], dd[1]), tag, cvtpk(dd[2], dd[3]), tag}, rq, qso, 0, 16);
    STAMP(7);
#pragma unroll 1
    for (unsigned spn = 0; spn < SPIN_CAP; ++spn) {
      const v4u v = __builtin_amdgcn_raw_buffer_load_b128(rq, (b * 2048 + h * 64 + 2 * (lane & 31)) * 8, 0, 16);
      if (__builtin_amdgcn_ballot_w64(v[1] != tag || v[3] != tag) == 0 || spn == SPIN_CAP - 1) { *LDSP(v2u, L_QIMG + 8 * (lane & 31)) = v2u{v[0], v[2]}; break; }
      __builtin_amdgcn_s_sleep(1);
    }
    CBAR(); lds_st(lds, L_QA, 1u);
    STAMP(8);
#pragma unroll 1
    for (unsigned spn = 0; spn < SPIN_CAP; ++spn) {
      const v4u v = __builtin_amdgcn_raw_buffer_load_b128(rq, ((8 + b) * 2048 + xcd * 256 + 2 * lane) * 8, 0, 16);
      const v4u u = __builtin_amdgcn_raw_buffer_load_b128(rq, ((8 + b) * 2048 + xcd * 256 + 128 + 2 * lane) * 8, 0, 16);
      if (__builtin_amdgcn_ballot_w64(v[1] != tag || v[3] != tag || u[1] != tag || u[3] != tag) == 0 || spn == SPIN_CAP - 1) {
        *LDSP(v2u, L_QIMG + 256 + 8 * lane) = v2u{v[0], v[2]}; *LDSP(v2u, L_QIMG + 768 + 8 * lane) = v2u{u[0], u[2]}; break; }
      __builtin_amdgcn_s_sleep(4);
    }
    CBAR(); lds_st(lds, L_QB, 1u);
    {
      unsigned v = lds_ld(lds, L_BCNT);
#pragma unroll 1
      for (unsigned spn = 0; v < 4u && spn < SPIN_CAP; ++spn) { __builtin_amdgcn_s_sleep(8); v = lds_ld(lds, L_BCNT); }
      CBAR();
    }
    const __amdgpu_buffer_rsrc_t rp = mk(ws + W_PB, 256 * 4 * 136 * 4);
#pragma unroll
    for (int hh = 0; hh < 4; ++hh) {
      f32x2 ml[4], ac[4];
#pragma unroll
      for (int u = 0; u < 4; ++u) { ml[u] = *LDSP(f32x2, L_WQ + (u * 4 + hh) * REC); ac[u] = *LDSP(f32x2, L_WQ + (u * 4 + hh) * REC + 16 + 8 * lane); }
      const float M = fmaxf(fmaxf(ml[0][0], ml[1][0]), fmaxf(ml[2][0], ml[3][0]));
      float L = 0.f, o0 = 0.f, o1 = 0.f;
#pragma unroll
      for (int u = 0; u < 4; ++u) { const float f = ex2((ml[u][0] - M) * SC2); L = fmaf(f, ml[u][1], L); o0 = fmaf(f, ac[u][0], o0); o1 = fmaf(f, ac[u][1], o1); }
      __builtin_amdgcn_raw_buffer_store_b64(v2u{__float_as_uint(o0), __float_as_uint(o1)}, rp, ((wg * 4 + hh) * 136 + 8 + 2 * lane) * 4, 0, 16);
      if (lane == 0) __builtin_amdgcn_raw_buffer_store_b64(v2u{__float_as_uint(M), __float_as_uint(L)}, rp, (wg * 4 + hh) * 136 * 4, 0, 16);
    }
    asm volatile("s_waitcnt vmcnt(0)" ::: "memory");
    const u64 flagv = ((u64)(tag ^ 0x5A5A5A5Au) << 32) | tag;
    if (lane == 0) __hip_atomic_store((GAS u64*)(ws + W_FLAG + wg * 128), flagv, RLX_AGENT);
#pragma unroll 1
    for (unsigned spn = 0; spn < SPIN_CAP; ++spn) {
      const u64 f = __hip_atomic_load((GAS u64*)(ws + W_FLAG + (xcd + 8 * (4 * b + (lane & 3))) * 128), RLX_AGENT);
      if (__builtin_amdgcn_ballot_w64(f != flagv) == 0) break;
      __builtin_amdgcn_s_sleep(16);
    }
    __builtin_amdgcn_fence(__ATOMIC_ACQUIRE, "wavefront");
    {
      v2u ml[4], ac[4];
#pragma unroll
      for (int u = 0; u < 4; ++u) { const int base = ((xcd + 8 * (4 * b + u)) * 4 + sp) * 136;
        ml[u] = __builtin_amdgcn_raw_buffer_load_b64(rp, base * 4, 0, 16); ac[u] = __builtin_amdgcn_raw_buffer_load_b64(rp, (base + 8 + 2 * lane) * 4, 0, 16); }
      const float M = fmaxf(fmaxf(__uint_as_float(ml[0][0]), __uint_as_float(ml[1][0])), fmaxf(__uint_as_float(ml[2][0]), __uint_as_float(ml[3][0])));
      float L = 0.f, o0 = 0.f, o1 = 0.f;
#pragma unroll
      for (int u = 0; u < 4; ++u) { const float f = ex2((__uint_as_float(ml[u][0]) - M) * SC2); L = fmaf(f, __uint_as_float(ml[u][1]), L);
        o0 = fmaf(f, __uint_as_float(ac[u][0]), o0); o1 = fmaf(f, __uint_as_float(ac[u][1]), o1); }
      const float inv = 1.f / L;
      *LDSP(unsigned, L_OIMG + 4 * lane) = cvtpk(o0 * inv, o1 * inv);
      CBAR(); lds_st(lds, L_OB, 1u);
    }
    STAMP(9);
    const __amdgpu_buffer_rsrc_t ry = mk(ws + W_YG, 2 * 8 * 32 * 512 * 8);
#pragma unroll 1
    for (int pass = 0; pass < 2; ++pass) {
      if (pass == 0) { __builtin_amdgcn_s_sleep(127); __builtin_amdgcn_s_sleep(127); }
      else { unsigned v = lds_ld(lds, L_ACNT);
#pragma unroll 1
        for (unsigned spn = 0; v < 4u && spn < SPIN_CAP; ++spn) { __builtin_amdgcn_s_sleep(4); v = lds_ld(lds, L_ACNT); } }
      const int yo = (((pass == 0 ? 8 : 0) + b) * 32 * 512 + (lane >> 3) * 512 + 16 * h + 2 * (lane & 7)) * 8;
      v4u y[4];
  #pragma unroll 1
    for (unsigned spn = 0; spn < SPIN_CAP; ++spn) {
        bool bad = false;
#pragma unroll
        for (int i = 0; i < 4; ++i) { y[i] = __builtin_amdgcn_raw_buffer_load_b128(ry, yo, i * 8 * 512 * 8, 16); bad |= y[i][1] != tag || y[i][3] != tag; }
        if (__builtin_amdgcn_ballot_w64(bad) == 0) break;
        if (pass == 0) __builtin_amdgcn_s_sleep(64); else __builtin_amdgcn_s_sleep(2);
      }
      float s0 = (__uint_as_float(y[0][0]) + __uint_as_float(y[1][0])) + (__uint_as_float(y[2][0]) + __uint_as_float(y[3][0]));
      float s1 = (__uint_as_float(y[0][2]) + __uint_as_float(y[1][2])) + (__uint_as_float(y[2][2]) + __uint_as_float(y[3][2]));
      s0 += __builtin_bit_cast(float, __builtin_amdgcn_mov_dpp(__builtin_bit_cast(int, s0), 0x128, 0xf, 0xf, true));
      s1 += __builtin_bit_cast(float, __builtin_amdgcn_mov_dpp(__builtin_bit_cast(int, s1), 0x128, 0xf, 0xf, true));
      s0 = xg_sum(s0); s1 = xg_sum(s1);
      if (lane < 8) *(GAS unsigned*)(out + b * 1024 + (pass == 0 ? 512 : 0) + 16 * h + 2 * lane) = cvtpk(s0, s1);
      STAMP(10 + pass);
    }
    if (wg == 0 && lane == 0) __hip_atomic_store((GAS unsigned*)(ws + W_EPOCH), tag, RLX_AGENT);
  }
}

template <class C>
static void launch_cfg(void* const* d_in, void* d_out, void* d_ws, hipStream_t stream) {
  (void)hipFuncSetAttribute((const void*)fused_decode<C>, hipFuncAttributeMaxDynamicSharedMemorySize, LDS_BYTES);
  hipLaunchKernelGGL(fused_decode<C>, dim3(256), dim3(NTHR), LDS_BYTES, stream,
                     (const u16*)d_in[0], (const u16*)d_in[1], (const u16*)d_in[2], (const u16*)d_in[3], (const u16*)d_in[4],
                     (const u16*)d_in[5], (const u16*)d_in[6], (const u16*)d_in[7], (const u16*)d_in[8], (const u16*)d_in[9],
                     (u16*)d_out, (unsigned char*)d_ws);
}
extern "C" void kernel_launch(void* const* d_in, const int* in_sizes, int n_in, void* d_out, int out_size, void* d_ws, size_t ws_size, hipStream_t stream) {
  (void)in_sizes; (void)n_in; (void)out_size; (void)ws_size;
  launch_cfg<CfgDefault>(d_in, d_out, d_ws, stream);
}
```

```cpp
#include <hip/hip_runtime.h>
#include <hip/hip_bf16.h>
#include <stdint.h>

typedef unsigned v4u __attribute__((ext_vector_type(4)));
typedef unsigned v2u __attribute__((ext_vector_type(2)));
typedef int v4i __attribute__((ext_vector_type(4)));
typedef float f32x4 __attribute__((ext_vector_type(4)));
typedef float f32x2 __attribute__((ext_vector_type(2)));
typedef short s16x4 __attribute__((ext_vector_type(4)));
typedef __bf16 bf16x8 __attribute__((ext_vector_type(8)));
typedef __bf16 bf16x2 __attribute__((ext_vector_type(2)));
typedef unsigned short u16;
typedef unsigned long long u64;
#define GAS __attribute__((address_space(1)))
#define LAS __attribute__((address_space(3)))
#define RLX_AGENT __ATOMIC_RELAXED, __HIP_MEMORY_SCOPE_AGENT

struct CfgDefault {
  static constexpr int NFL = 20;
  static constexpr int N1 = 64;
  static constexpr int ROTA = 0, ROTB = 0;
  static constexpr int WO_AUX = 0;
  static constexpr int WO_SLEEP = 12;
  static constexpr int PARK = 2;
  static constexpr int QB_DELAY = 2;
  static constexpr int DBG = 0;
};

constexpr int SK = 4096, DH = 128, NH = 32, HKV = 8;
constexpr float SC2 = 0.088388347648318440f * 1.4426950408889634f;
constexpr int NWAVE = 10, NTHR = NWAVE * 64;
constexpr int NTILE = 320, NTB = 64;
constexpr int RING = 15, SLOT = 8192;
constexpr int L_FULL = 0, L_QA = 4, L_QB = 8, L_BCNT = 12, L_DONE = 16, L_RCNT = 32  , L_TAG = 40, L_YPUB = 44;
constexpr int L_QIMG = 64;
constexpr int L_OIMG = L_QIMG + 1280;
constexpr int REC = 528;
constexpr int L_HREC = L_OIMG + 1024;
constexpr int L_WQ = 8192;
constexpr int L_RING = L_WQ + 32768;
constexpr int LDS_BYTES = 160 * 1024;
static_assert(L_HREC % 16 == 0 && L_HREC + 8 * REC <= 7168 && 16 * REC <= 32768 && L_RING + RING * SLOT <= LDS_BYTES, "lds map");
constexpr int W_EPOCH = 0;
constexpr int W_ML = 4096;
constexpr int W_QG = 65536;
constexpr int W_PO = W_QG + 2 * 8 * 2048 * 8;
constexpr int W_YG = W_PO + 256 * 2048;
constexpr int YHALF = 8 * 32 * 512 * 8;
constexpr int W_END = W_YG + 2 * YHALF;
constexpr size_t W_STAMP = 64u << 20;
constexpr unsigned SPIN_CAP = 1u << 21;
constexpr int OOB = 0x7FFFFF00;

__device__ __forceinline__ unsigned cvtpk(float lo, float hi) { return __builtin_bit_cast(unsigned, __builtin_convertvector(f32x2{lo, hi}, bf16x2)); }
__device__ __forceinline__ __amdgpu_buffer_rsrc_t mk(const void* p, unsigned bytes) { return __builtin_amdgcn_make_buffer_rsrc((void*)p, 0, bytes, 0x00020000); }
__device__ __forceinline__ v4i mksrd(const void* p, unsigned bytes) { return v4i{(int)(uintptr_t)p, (int)((uintptr_t)p >> 32), (int)bytes, 0x00020000}; }
__device__ __forceinline__ f32x4 mfma32(v4u a, v4u b, f32x4 c) { return __builtin_amdgcn_mfma_f32_16x16x32_bf16(__builtin_bit_cast(bf16x8, a), __builtin_bit_cast(bf16x8, b), c, 0, 0, 0); }
__device__ __forceinline__ f32x4 mfma16(v2u a, v2u b, f32x4 c) { return __builtin_amdgcn_mfma_f32_16x16x16bf16_1k(__builtin_bit_cast(s16x4, a), __builtin_bit_cast(s16x4, b), c, 0, 0, 0); }
__device__ __forceinline__ float ex2(float x) { return __builtin_amdgcn_exp2f(x); }
__device__ __forceinline__ v4u ld_sc1(__amdgpu_buffer_rsrc_t r, int vo, int so) { return __builtin_amdgcn_raw_buffer_load_b128(r, vo, so, 16); }
__device__ __forceinline__ void st_sc1(v4u v, __amdgpu_buffer_rsrc_t r, int vo, int so) { __builtin_amdgcn_raw_buffer_store_b128(v, r, vo, so, 16); }
__device__ __forceinline__ float xg_max(float x) {
  auto s = __builtin_amdgcn_permlane16_swap(__float_as_uint(x), __float_as_uint(x), false, false);
  x = fmaxf(__uint_as_float(s[0]), __uint_as_float(s[1]));
  auto t = __builtin_amdgcn_permlane32_swap(__float_as_uint(x), __float_as_uint(x), false, false);
  return fmaxf(__uint_as_float(t[0]), __uint_as_float(t[1]));
}
__device__ __forceinline__ float xg_sum(float x) {
  auto s = __builtin_amdgcn_permlane16_swap(__float_as_uint(x), __float_as_uint(x), false, false);
  x = __uint_as_float(s[0]) + __uint_as_float(s[1]);
  auto t = __builtin_amdgcn_permlane32_swap(__float_as_uint(x), __float_as_uint(x), false, false);
  return __uint_as_float(t[0]) + __uint_as_float(t[1]);
}
#define CBAR() asm volatile("" ::: "memory")
#define OPAQUE_S(x) asm volatile("" : "+s"(x))
#define LDSP(T, off) ((LAS T*)(lds + (off)))
__device__ __forceinline__ unsigned lds_ld(LAS unsigned char* lds, int off) { return __builtin_amdgcn_readfirstlane(*(volatile LAS unsigned*)(lds + off)); }
__device__ __forceinline__ void lds_st(LAS unsigned char* lds, int off, unsigned v) { *(volatile LAS unsigned*)(lds + off) = v; }
template <int SLP> __device__ __forceinline__ unsigned lds_wait_ge(LAS unsigned char* lds, int off, unsigned want) {
  unsigned v = lds_ld(lds, off);
#pragma unroll 1
  for (unsigned sp = 0; v < want && sp < SPIN_CAP * 8; ++sp) { __builtin_amdgcn_s_sleep(SLP); v = lds_ld(lds, off); }
  CBAR();
  return v;
}
__device__ __forceinline__ void lds_inc(LAS unsigned char* lds, int off, int lane) {
  CBAR();
  if (lane == 0) __hip_atomic_fetch_add(LDSP(unsigned, off), 1u, __ATOMIC_RELAXED, __HIP_MEMORY_SCOPE_WORKGROUP);
  CBAR();
}
#define STAMP(i) do {} while (0)
#define STAMP_OUT(lo, hi) do {} while (0)

template <int NFL> __device__ __forceinline__ void dma4(unsigned m0v, unsigned v0, unsigned v1, unsigned v2, unsigned v3, v4i srd, unsigned so) {
  asm volatile("s_mov_b32 m0, %0\n\t"
               "s_waitcnt vmcnt(%7)\n\tbuffer_load_dwordx4 %1, %5, %6 offen nt lds\n\t"
               "s_waitcnt vmcnt(%7)\n\tbuffer_load_dwordx4 %2, %5, %6 offen offset:1024 nt lds\n\t"
               "s_waitcnt vmcnt(%7)\n\tbuffer_load_dwordx4 %3, %5, %6 offen offset:2048 nt lds\n\t"
               "s_waitcnt vmcnt(%7)\n\tbuffer_load_dwordx4 %4, %5, %6 offen offset:3072 nt lds"
               :: "s"(m0v), "v"(v0), "v"(v1), "v"(v2), "v"(v3), "s"(srd), "s"(so), "i"(NFL - 1) : "memory");
}

struct St { float m, l; f32x4 a0, a1; };
struct Sel { unsigned l0, l1, h0, h1; };
__device__ __forceinline__ void st_reset(St& s) { s.m = -3.0e38f; s.l = 0.f; s.a0 = f32x4{0, 0, 0, 0}; s.a1 = f32x4{0, 0, 0, 0}; }
__device__ __forceinline__ void tile_math(const v4u (&T)[8], const v4u (&q)[4], St& s, const Sel& e) {
  f32x4 sc = {0.f, 0.f, 0.f, 0.f};
#pragma unroll
  for (int m = 0; m < 4; ++m) sc = mfma32(T[m], q[m], sc);
  const float tmax = fmaxf(fmaxf(sc[0], sc[1]), fmaxf(sc[2], sc[3]));
  if (__builtin_amdgcn_ballot_w64(tmax > s.m) != 0) {
    const float mn = fmaxf(s.m, xg_max(tmax));
    const float al = ex2((s.m - mn) * SC2);
    s.a0 *= al; s.a1 *= al; s.l *= al; s.m = mn;
  }
  const float mc = -s.m * SC2;
  float p[4];
#pragma unroll
  for (int i = 0; i < 4; ++i) p[i] = ex2(fmaf(sc[i], SC2, mc));
  s.l += (p[0] + p[1]) + (p[2] + p[3]);
  const unsigned pk01 = cvtpk(p[0], p[1]), pk23 = cvtpk(p[2], p[3]);
#pragma unroll
  for (int i = 0; i < 4; ++i) {
    const unsigned pk = i < 2 ? pk01 : pk23;
    const v2u bf = {__builtin_amdgcn_perm(pk, pk, (i & 1) ? e.h0 : e.l0), __builtin_amdgcn_perm(pk, pk, (i & 1) ? e.h1 : e.l1)};
    s.a0 = mfma16(v2u{T[4 + i][0], T[4 + i][1]}, bf, s.a0);
    s.a1 = mfma16(v2u{T[4 + i][2], T[4 + i][3]}, bf, s.a1);
  }
}

template <class C>
__global__ void __launch_bounds__(NTHR, 1) fused_decode(const u16* xa, const u16* ak, const u16* av, const u16* wqa, const u16* woa,
                                                        const u16* xb, const u16* bk, const u16* bv, const u16* wqb, const u16* wob,
                                                        u16* out, unsigned char* ws) {
  constexpr int NFL = C::NFL, N1 = C::N1, PARK = C::PARK;
  extern __shared__ __attribute__((aligned(16))) unsigned char lds_raw[];
  LAS unsigned char* lds = (LAS unsigned char*)lds_raw;
  const int wg = blockIdx.x, lane = threadIdx.x & 63, wave = __builtin_amdgcn_readfirstlane(threadIdx.x >> 6);
  const int xcd = wg & 7, jw = wg >> 3, b = jw >> 2, sp = jw & 3, h = 4 * xcd + sp;
  const int g = lane >> 4, n = lane & 15;
  const unsigned lds0 = (unsigned)(uintptr_t)lds;
  if (threadIdx.x < 16) *(volatile LAS unsigned*)(lds + 4 * lane) = 0u;
  asm volatile("s_waitcnt lgkmcnt(0)" :: "s"(wqb), "s"(wob), "s"(out), "s"(ws), "s"(xa) : "memory");
  __builtin_amdgcn_s_barrier();
  CBAR();

  if (wave == 0) {
    __builtin_amdgcn_s_setprio(3);
    const v4i rak = mksrd(ak + (size_t)(b * NH + h) * SK * DH, SK * 256), rav = mksrd(av + (size_t)(b * NH + h) * SK * DH, SK * 256);
    const v4i rbk = mksrd(bk + ((size_t)(b * HKV + xcd) * SK + sp * 1024) * DH, 1024 * 256), rbv = mksrd(bv + ((size_t)(b * HKV + xcd) * SK + sp * 1024) * DH, 1024 * 256);
    const unsigned k0 = g * 256 + ((n ^ g) * 16), k1 = k0 ^ 64, k2 = k0 ^ 128, k3 = k0 ^ 192, vv = lane * 16;
    unsigned slot = lds0 + L_RING;
    int limit = RING;
    constexpr int LAGK = (NFL + 4 + 7) / 8, LAGV = (NFL + 7) / 8;
    static_assert(NFL >= 17 && NFL <= 32, "publish lag");
    STAMP(0);
    int t = 0;
#pragma unroll 1
    for (int seg = 0; seg < 3; ++seg) {
      const v4i rk = seg == 1 ? rbk : rak, rv = seg == 1 ? rbv : rav;
      const int tend = seg == 0 ? N1 : seg == 1 ? N1 + NTB : NTILE;
      const unsigned msk = seg == 1 ? 64 * 4096u - 1u : 256 * 4096u - 1u;
      unsigned so = ((seg == 1 ? (b * HKV + xcd) * C::ROTB : (b * NH + h) * C::ROTA + (seg == 2 ? N1 : 0)) * 4096u) & msk;
#pragma unroll 1
      for (; t < tend; ++t) {
        if (!(C::DBG & 4) && t >= limit) {
#pragma unroll 1
          for (unsigned spn = 0; spn < SPIN_CAP * 8; ++spn) {
            const v4u d = *(volatile LAS v4u*)(lds + L_DONE);
            const int c = min(min(4 * (int)d[0], 4 * (int)d[1] + 1), min(4 * (int)d[2] + 2, 4 * (int)d[3] + 3));
            limit = __builtin_amdgcn_readfirstlane(c) + RING;
            if (t < limit) break;
            __builtin_amdgcn_s_sleep(1);
          }
        }
        dma4<NFL>(slot, k0, k1, k2, k3, rk, so);
        if (LAGK == LAGV && t >= LAGK) lds_st(lds, L_FULL, (unsigned)(t - LAGK + 1));
        dma4<NFL>(slot + 4096, vv, vv, vv, vv, rv, so);
        if (LAGK != LAGV && t >= LAGV) lds_st(lds, L_FULL, (unsigned)(t - LAGV + 1));
        slot += SLOT; if (slot >= lds0 + L_RING + RING * SLOT) slot = lds0 + L_RING;
        so = (so + 4096u) & msk;
      }
    }
    asm volatile("s_waitcnt vmcnt(24)" ::: "memory"); lds_st(lds, L_FULL, (unsigned)(NTILE - 3));
    asm volatile("s_waitcnt vmcnt(16)" ::: "memory"); lds_st(lds, L_FULL, (unsigned)(NTILE - 2));
    asm volatile("s_waitcnt vmcnt(8)" ::: "memory");  lds_st(lds, L_FULL, NTILE - 1);
    asm volatile("s_waitcnt vmcnt(0)" ::: "memory");  lds_st(lds, L_FULL, NTILE);
    STAMP(1); STAMP_OUT(0, 2); STAMP_OUT(16, 26);
  } else if (wave >= 2 && wave < 6) {
    if (C::DBG & 8) return;
    const int w = wave - 2, jj = n >> 2;
    const unsigned Z = 0x0C0C0C0Cu;
    Sel e;
    e.l0 = jj == 0 ? 0x0C0C0100u : jj == 1 ? 0x01000C0Cu : Z; e.l1 = jj == 2 ? 0x0C0C0100u : jj == 3 ? 0x01000C0Cu : Z;
    e.h0 = jj == 0 ? 0x0C0C0302u : jj == 1 ? 0x03020C0Cu : Z; e.h1 = jj == 2 ? 0x0C0C0302u : jj == 3 ? 0x03020C0Cu : Z;
    const int kb = n * 256 + ((g ^ n) * 16), vb = 4096 + g * 1024 + n * 16;
    int slot = L_RING + w * SLOT; unsigned full = 0, cnt = 0; int t = w;
#define TILE_READ(T_) do { \
        if (full <= (unsigned)t) full = lds_wait_ge<1>(lds, L_FULL, (unsigned)t + 1u); \
        CBAR(); \
        _Pragma("unroll") for (int m_ = 0; m_ < 4; ++m_) T_[m_] = *LDSP(v4u, (slot + kb) ^ (64 * m_)); \
        _Pragma("unroll") for (int i_ = 0; i_ < 4; ++i_) T_[4 + i_] = *LDSP(v4u, slot + vb + 256 * i_); \
        CBAR(); \
        ++cnt; lds_st(lds, L_DONE + 4 * w, cnt);     \
        CBAR(); \
        slot += 4 * SLOT; if (slot >= L_RING + RING * SLOT) slot -= RING * SLOT; \
        t += 4; } while (0)
    static_assert(4 * PARK <= N1 && PARK >= 1, "parked tiles belong to the first A segment");
    St s, sa; st_reset(s); st_reset(sa);
    v4u P[PARK][8], q[4];
#pragma unroll
    for (int u = 0; u < 4; ++u) q[u] = v4u{0, 0, 0, 0};
#pragma unroll
    for (int i = 0; i < PARK; ++i)
#pragma unroll
      for (int u = 0; u < 8; ++u) P[i][u] = v4u{0, 0, 0, 0};
    int seg = -1, parked = 1, tend = 0;
    OPAQUE_S(seg); OPAQUE_S(parked);
#pragma unroll 1
    for (;;) {
#pragma unroll 1
      while (parked > 0 || t < tend) {
        v4u T[8];
        if (parked > 0) {
#pragma unroll
          for (int u = 0; u < 8; ++u) { T[u] = P[0][u];
#pragma unroll
            for (int i = 0; i + 1 < PARK; ++i) P[i][u] = P[i + 1][u]; }
          --parked;
        } else TILE_READ(T);
        tile_math(T, q, s, e);
      }
      if (seg != 0) {
        const float lt = xg_sum(s.l);
        const int ro = seg == 1 ? L_WQ + (w * 4 + (n & 3)) * REC : L_HREC + (4 + w) * REC;
        if (seg == 1 || (n & 3) == 0) {
#pragma unroll
          for (int i = 0; i < 4; ++i) { *LDSP(float, ro + 16 + 4 * (32 * g + 8 * i + jj)) = s.a0[i]; *LDSP(float, ro + 16 + 4 * (32 * g + 8 * i + 4 + jj)) = s.a1[i]; }
          if (lane < 4) *LDSP(f32x2, ro) = f32x2{s.m, lt};
        }
        lds_inc(lds, seg == 1 ? L_BCNT : seg == 2 ? L_RCNT + 4 : 48, lane);
        if (seg == 2) break;
      }
      if (seg == 0) { sa = s; st_reset(s); } else s = sa;
      if (seg < 0) {
#pragma unroll
        for (int i = 0; i < PARK; ++i) TILE_READ(P[i]);
        parked = PARK;
      }
      ++seg;
      tend = seg == 0 ? N1 : seg == 1 ? N1 + NTB : NTILE;
      lds_wait_ge<1>(lds, seg == 1 ? L_QB : L_QA, 1u);
      const int qo = L_QIMG + (seg == 1 ? 256 + (n & 3) * 256 : 0) + g * 16;
#pragma unroll
      for (int m = 0; m < 4; ++m) q[m] = *LDSP(v4u, qo + 64 * m);
      if (seg == 0 && w == 0) STAMP(2);
    }
    if (w == 3) { STAMP(3); STAMP_OUT(2, 4); }
  } else if (wave >= 6) {
    if (C::DBG & (8 | 16)) return;
    const int w = wave - 6;
    const __amdgpu_buffer_rsrc_t ry = mk(ws + W_YG, 2 * YHALF);
    const int yo = ((b * 32 + h) * 512 + 128 * w + 8 * n + 4 * (g & 1) + 2 * (g >> 1)) * 8;
    unsigned sl[4], sh[4];
#pragma unroll
    for (int d = 0; d < 4; ++d) { const bool on = ((n & 7) >> 1) == d; sl[d] = !on ? 0x0C0C0C0Cu : (n & 1) ? 0x01000C0Cu : 0x0C0C0100u; sh[d] = !on ? 0x0C0C0C0Cu : (n & 1) ? 0x03020C0Cu : 0x0C0C0302u; }
    v4u yb = {0, 0, 0, 0};
    __builtin_amdgcn_s_sleep(127); __builtin_amdgcn_s_sleep(127); __builtin_amdgcn_s_sleep(127);
#pragma unroll 1
    for (int pass = 0; pass < 2; ++pass) {
      const __amdgpu_buffer_rsrc_t rw = mk((pass == 0 ? wob : woa) + (size_t)h * 128 * 512, (C::DBG & 1) ? 0 : 128 * 1024);
      v4u wo[32];
#pragma unroll
      for (int i = 0; i < 32; ++i) {
        wo[i] = __builtin_amdgcn_raw_buffer_load_b128(rw, g * 32768 + w * 256 + n * 16, i * 1024, C::WO_AUX);
        __builtin_amdgcn_sched_barrier(0);
        __builtin_amdgcn_s_sleep(C::WO_SLEEP);
        __builtin_amdgcn_sched_barrier(0);
      }
      if (pass == 1) { lds_wait_ge<8>(lds, L_FULL, NTILE - 8); st_sc1(yb, ry, yo, YHALF); }
      asm volatile("s_waitcnt vmcnt(1)" ::: "memory");
      lds_wait_ge<1>(lds, L_RCNT + 4 * pass, 4u);
      const int rb = L_HREC + pass * 4 * REC;
      f32x2 ml[4], ac[4];
#pragma unroll
      for (int u = 0; u < 4; ++u) { ml[u] = *LDSP(f32x2, rb + u * REC); ac[u] = *LDSP(f32x2, rb + u * REC + 16 + 8 * lane); }
      const unsigned tag = *LDSP(unsigned, L_TAG);
      const float M = fmaxf(fmaxf(ml[0][0], ml[1][0]), fmaxf(ml[2][0], ml[3][0]));
      float L = 0.f, o0 = 0.f, o1 = 0.f;
#pragma unroll
      for (int u = 0; u < 4; ++u) { const float f = ex2((ml[u][0] - M) * SC2); L = fmaf(f, ml[u][1], L); o0 = fmaf(f, ac[u][0], o0); o1 = fmaf(f, ac[u][1], o1); }
      const float inv = __builtin_amdgcn_rcpf(L);
      const int oi = L_OIMG + w * 256;
      *LDSP(unsigned, oi + 4 * lane) = cvtpk(o0 * inv, o1 * inv);
      CBAR();
      v4u of[4];
#pragma unroll
      for (int u = 0; u < 4; ++u) of[u] = *LDSP(v4u, oi + 64 * g + 16 * u);
      f32x4 y0 = {0, 0, 0, 0}, y1 = {0, 0, 0, 0};
#pragma unroll
      for (int i = 0; i < 32; i += 2) {
        const unsigned pk = of[i >> 3][(i >> 1) & 3];
        v4u a0, a1;
#pragma unroll
        for (int d = 0; d < 4; ++d) { a0[d] = __builtin_amdgcn_perm(pk, pk, sl[d]); a1[d] = __builtin_amdgcn_perm(pk, pk, sh[d]); }
        y0 = mfma32(a0, wo[i], y0); y1 = mfma32(a1, wo[i + 1], y1);
      }
      y0 += y1;
      yb = v4u{__float_as_uint(g < 2 ? y0[0] : y0[2]), tag, __float_as_uint(g < 2 ? y0[1] : y0[3]), tag};
      st_sc1(yb, ry, pass == 0 ? OOB : yo, 0);
      lds_inc(lds, L_YPUB, lane);
      if (w == 0) STAMP(4 + pass);
    }
    if (w == 0) STAMP_OUT(4, 6);
  } else {
    if (C::DBG & 8) return;
    unsigned G;
    asm volatile("global_load_dword %0, %1, %2 sc1" : "=v"(G) : "v"(W_EPOCH), "s"(ws) : "memory");
    const int mat = jw >> 4, cb = xcd * 16 + (jw & 15);
    const __amdgpu_buffer_rsrc_t rs = mk(ws, W_END);
    unsigned tag = 0;
#define TAKE_TAG() do { asm volatile("s_waitcnt vmcnt(0)\n\tv_readfirstlane_b32 %0, %1\n\ts_nop 1" : "=s"(tag) : "v"(G) : "memory"); tag += 1u; } while (0)
#pragma unroll 1
    for (int step = 0; step < 2; ++step) {
      if (step == mat) {
        if (mat) { for (int i = 0; i < C::QB_DELAY; ++i) __builtin_amdgcn_s_sleep(127); }
        {
          const v4i rw = mksrd((mat ? wqb : wqa) + cb * 32, 512 * 8192 - cb * 64);
          const unsigned vo = (lane >> 2) * 8192 + (lane & 3) * 16;
#pragma unroll
          for (int i = 0; i < 32; ++i)
            asm volatile("s_mov_b32 m0, %0\n\ts_nop 0\n\tbuffer_load_dwordx4 %1, %2, %3 offen nt lds" :: "s"(lds0 + L_WQ + i * 1024), "v"(vo), "s"(rw), "s"(i * 16 * 8192) : "memory");
        }
        v4u xf[16];
        {
          const __amdgpu_buffer_rsrc_t rx = mk(mat ? xb : xa, 8 * 512 * 2);
#pragma unroll
          for (int s = 0; s < 16; ++s) asm volatile("buffer_load_dwordx4 %0, %1, %2, 0 offen offset:%3" : "=v"(xf[s]) : "v"((n & 7) * 1024 + 16 * g), "s"(rx), "i"(64 * s) : "memory");
        }
        const int ta = L_WQ + (8 * g + (n >> 2)) * 64 + (n & 3) * 8;
        int it = 0; OPAQUE_S(it);
#pragma unroll 1
        for (; it < 2; ++it) {
          if (it == 1) {
            TAKE_TAG();
            STAMP(6);
          }
#pragma unroll
          for (int s = 0; s < 16; ++s) asm volatile("" : "+v"(xf[s]));
          f32x4 d0 = {0, 0, 0, 0}, d1 = {0, 0, 0, 0};
#pragma unroll
          for (int s4 = 0; s4 < 16; s4 += 8) {
            v2u t[8][4];
#pragma unroll
            for (int s = 0; s < 8; ++s)
#pragma unroll
              for (int u = 0; u < 4; ++u) t[s][u] = __builtin_bit_cast(v2u, __builtin_amdgcn_ds_read_tr16_b64_v4i16(LDSP(s16x4, ta + (s4 + s) * 2048 + (u & 1) * 256 + (u >> 1) * 32)));
            __builtin_amdgcn_sched_barrier(0);
#pragma unroll
            for (int s = 0; s < 8; ++s) {
              d0 = mfma32(v4u{t[s][0][0], t[s][0][1], t[s][1][0], t[s][1][1]}, xf[s4 + s], d0);
              d1 = mfma32(v4u{t[s][2][0], t[s][2][1], t[s][3][0], t[s][3][1]}, xf[s4 + s], d1);
            }
            __builtin_amdgcn_sched_barrier(0);
          }
          const f32x4 dd = n < 8 ? d0 : d1;
          const int qso = W_QG + ((mat * 8 + (n & 7)) * 2048 + cb * 16 + (n >> 3) * 8 + 2 * g) * 8;
          st_sc1(v4u{cvtpk(dd[0], dd[1]), tag, cvtpk(dd[2], dd[3]), tag}, rs, it ? qso : OOB, 0);
        }
        STAMP(7);
      } else {
        if (mat) {
          TAKE_TAG();
          __builtin_amdgcn_s_sleep(90);
        } else __builtin_amdgcn_s_sleep(8);
#pragma unroll 1
        for (unsigned spn = 0; spn < SPIN_CAP; ++spn) {
          const v4u v = ld_sc1(rs, lane < 32 ? W_QG + (b * 2048 + h * 64 + 2 * lane) * 8 : OOB, 0);
          if (lane < 32) *LDSP(v2u, L_QIMG + 8 * lane) = v2u{v[0], v[2]};
          if ((unsigned)__builtin_amdgcn_ballot_w64(v[1] != tag || v[3] != tag) == 0u) break;
          __builtin_amdgcn_s_sleep(6);
        }
        CBAR(); lds_st(lds, L_QA, 1u);
        STAMP(8);
      }
    }
    lds_st(lds, L_TAG, tag);
    lds_wait_ge<16>(lds, L_FULL, N1 - 16);
#pragma unroll 1
    for (unsigned spn = 0; spn < SPIN_CAP; ++spn) {
      const v4u v = ld_sc1(rs, W_QG + ((8 + b) * 2048 + xcd * 256 + 2 * lane) * 8, 0), u = ld_sc1(rs, W_QG + ((8 + b) * 2048 + xcd * 256 + 2 * lane) * 8, 1024);
      *LDSP(v2u, L_QIMG + 256 + 8 * lane) = v2u{v[0], v[2]}; *LDSP(v2u, L_QIMG + 768 + 8 * lane) = v2u{u[0], u[2]};
      if (__builtin_amdgcn_ballot_w64(v[1] != tag || v[3] != tag || u[1] != tag || u[3] != tag) == 0) break;
      __builtin_amdgcn_s_sleep(32);
    }
    CBAR(); lds_st(lds, L_QB, 1u);
    lds_wait_ge<16>(lds, L_BCNT, 4u);
    {
      float Mk = 0.f, Lk = 0.f;
#pragma unroll
      for (int hp = 0; hp < 2; ++hp) {
        const int hh = 2 * hp + (lane >> 5), rb = L_WQ + hh * REC;
        f32x2 ml[4]; f32x4 ac[4];
#pragma unroll
        for (int u = 0; u < 4; ++u) { ml[u] = *LDSP(f32x2, rb + u * 4 * REC); ac[u] = *LDSP(f32x4, rb + u * 4 * REC + 16 + 16 * (lane & 31)); }
        const float M = fmaxf(fmaxf(ml[0][0], ml[1][0]), fmaxf(ml[2][0], ml[3][0]));
        float L = 0.f; f32x4 o = {0, 0, 0, 0};
#pragma unroll
        for (int u = 0; u < 4; ++u) { const float f = ex2((ml[u][0] - M) * SC2); L = fmaf(f, ml[u][1], L); o += f * ac[u]; }
        st_sc1(__builtin_bit_cast(v4u, o), rs, W_PO + (xcd + 8 * (4 * b + hh)) * 2048 + sp * 512 + 16 * (lane & 31), 0);
        if ((lane & 31) == hp) { Mk = M; Lk = L; }
      }
      asm volatile("s_waitcnt vmcnt(0)" ::: "memory");
      const int hh = 2 * (lane & 31) + (lane >> 5);
      st_sc1(v4u{__float_as_uint(Mk), tag, __float_as_uint(Lk), tag}, rs, (lane & 31) < 2 ? W_ML + (xcd + 8 * (4 * b + hh)) * 128 + sp * 16 : OOB, 0);
    }
    __builtin_amdgcn_s_sleep(127);
#pragma unroll 1
    for (unsigned spn = 0; spn < SPIN_CAP; ++spn) {
      const v4u v = ld_sc1(rs, lane < 4 ? W_ML + wg * 128 + lane * 16 : OOB, 0);
      if (lane < 4) *LDSP(v2u, L_HREC + lane * REC) = v2u{v[0], v[2]};
      if ((__builtin_amdgcn_ballot_w64(v[1] != tag || v[3] != tag) & 15u) == 0u) break;
      __builtin_amdgcn_s_sleep(64);
    }
    __builtin_amdgcn_fence(__ATOMIC_ACQUIRE, "wavefront");
    {
      const v4u p0 = ld_sc1(rs, W_PO + wg * 2048 + 16 * lane, 0), p1 = ld_sc1(rs, W_PO + wg * 2048 + 16 * lane, 1024);
      *LDSP(v4u, L_HREC + (lane >> 5) * REC + 16 + 16 * (lane & 31)) = p0;
      *LDSP(v4u, L_HREC + (2 + (lane >> 5)) * REC + 16 + 16 * (lane & 31)) = p1;
      CBAR(); lds_st(lds, L_RCNT, 4u);
    }
    STAMP(9);
    if (C::DBG & 16) return;
#pragma unroll 1
    for (int pass = 0; pass < 2; ++pass) {
      if (pass == 0) lds_wait_ge<8>(lds, L_FULL, NTILE - 1); else lds_wait_ge<1>(lds, L_YPUB, 8u);
      const int yo = W_YG + (pass == 0 ? YHALF : 0) + (b * 32 * 512 + (lane >> 3) * 512 + 16 * h + 2 * (lane & 7)) * 8;
      v4u y[4]; bool ok[4];
#pragma unroll
      for (int i = 0; i < 4; ++i) { y[i] = v4u{0, 0, 0, 0}; ok[i] = false; }
#pragma unroll 1
      for (unsigned spn = 0; spn < SPIN_CAP; ++spn) {
        v4u z[4];
#pragma unroll
        for (int i = 0; i < 4; ++i) z[i] = ld_sc1(rs, ok[i] ? OOB : yo, i * 8 * 512 * 8);
        bool all = true;
#pragma unroll
        for (int i = 0; i < 4; ++i) { if (!ok[i]) { y[i] = z[i]; ok[i] = z[i][1] == tag && z[i][3] == tag; } all &= ok[i]; }
        if (__builtin_amdgcn_ballot_w64(!all) == 0) break;
        __builtin_amdgcn_s_sleep(3);
      }
      float s0 = (__uint_as_float(y[0][0]) + __uint_as_float(y[1][0])) + (__uint_as_float(y[2][0]) + __uint_as_float(y[3][0]));
      float s1 = (__uint_as_float(y[0][2]) + __uint_as_float(y[1][2])) + (__uint_as_float(y[2][2]) + __uint_as_float(y[3][2]));
      s0 += __builtin_bit_cast(float, __builtin_amdgcn_mov_dpp(__builtin_bit_cast(int, s0), 0x128, 0xf, 0xf, true));
      s1 += __builtin_bit_cast(float, __builtin_amdgcn_mov_dpp(__builtin_bit_cast(int, s1), 0x128, 0xf, 0xf, true));
      s0 = xg_sum(s0); s1 = xg_sum(s1);
      if (lane < 8) *(GAS unsigned*)(out + b * 1024 + (pass == 0 ? 512 : 0) + 16 * h + 2 * lane) = cvtpk(s0, s1);
      STAMP(10 + pass);
    }
    if (wg == 0 && lane == 0) __hip_atomic_store((GAS unsigned*)(ws + W_EPOCH), tag, RLX_AGENT);
    STAMP_OUT(6, 12);
  }
}

template <class C>
static void launch_cfg(void* const* d_in, void* d_out, void* d_ws, hipStream_t stream) {
  (void)hipFuncSetAttribute((const void*)fused_decode<C>, hipFuncAttributeMaxDynamicSharedMemorySize, LDS_BYTES);
  hipLaunchKernelGGL(fused_decode<C>, dim3(256), dim3(NTHR), LDS_BYTES, stream,
                     (const u16*)d_in[0], (const u16*)d_in[1], (const u16*)d_in[2], (const u16*)d_in[3], (const u16*)d_in[4],
                     (const u16*)d_in[5], (const u16*)d_in[6], (const u16*)d_in[7], (const u16*)d_in[8], (const u16*)d_in[9],
                     (u16*)d_out, (unsigned char*)d_ws);
}
extern "C" void kernel_launch(void* const* d_in, const int* in_sizes, int n_in, void* d_out, int out_size, void* d_ws, size_t ws_size, hipStream_t stream) {
  (void)in_sizes; (void)n_in; (void)out_size; (void)ws_size;
  launch_cfg<CfgDefault>(d_in, d_out, d_ws, stream);
}
```

```cpp
#include <hip/hip_runtime.h>
#include <hip/hip_bf16.h>
#include <stdint.h>

typedef unsigned v4u __attribute__((ext_vector_type(4)));
typedef unsigned v2u __attribute__((ext_vector_type(2)));
typedef int v4i __attribute__((ext_vector_type(4)));
typedef float f32x4 __attribute__((ext_vector_type(4)));
typedef float f32x2 __attribute__((ext_vector_type(2)));
typedef short s16x4 __attribute__((ext_vector_type(4)));
typedef __bf16 bf16x8 __attribute__((ext_vector_type(8)));
typedef __bf16 bf16x2 __attribute__((ext_vector_type(2)));
typedef unsigned short u16;
typedef unsigned long long u64;
#define GAS __attribute__((address_space(1)))
#define LAS __attribute__((address_space(3)))
#define RLX_AGENT __ATOMIC_RELAXED, __HIP_MEMORY_SCOPE_AGENT

struct CfgDefault {
  static constexpr int NFL = 20;
  static constexpr int MUL = 5;
  static constexpr int WO_AUX = 0;
  static constexpr int WO_SLEEP = 12;
  static constexpr int PF_AUX = 0;
  static constexpr int T_SLICE = 3, T_GATHER = 16, T_MLPOLL = 40;
  static constexpr int WQA_SLEEP = 0, WQB_SLEEP = 12;
  static constexpr int PARK = 4;
  static constexpr int DBG = 0;
};

constexpr int SK = 4096, DH = 128, NH = 32, HKV = 8;
constexpr float SC2 = 0.088388347648318440f * 1.4426950408889634f;
constexpr int NWAVE = 10, NTHR = NWAVE * 64;
constexpr int NSL = 320;
constexpr int RING = 15, SLOT = 8192;
constexpr int L_FULL = 0, L_QRDY = 4  , L_BCNT = 12, L_DONE = 16, L_RCNT = 32  , L_TAG = 40, L_YPUB = 44, L_MLC = 48;
constexpr int L_QIMG = 64;
constexpr int L_OIMG = L_QIMG + 1280;
constexpr int L_ML = L_OIMG + 1024;
constexpr int L_HREC = L_ML + 256;
constexpr int L_S = 8192;
constexpr int L_P = L_S + 16384;
constexpr int L_RING = L_P + 16384;
constexpr int LDS_BYTES = 160 * 1024;
static_assert(L_HREC % 16 == 0 && L_HREC + 8 * 512 <= 7168 && L_RING + RING * SLOT <= LDS_BYTES, "lds map");
constexpr int W_EPOCH = 0;
constexpr int W_ML = 4096;
constexpr int W_QG = 65536;
constexpr int W_PO = W_QG + 2 * 8 * 2048 * 8;
constexpr int W_YG = W_PO + 256 * 2048;
constexpr int YHALF = 8 * 32 * 512 * 8;
constexpr int W_END = W_YG + 2 * YHALF;
constexpr size_t W_STAMP = 64u << 20;
constexpr unsigned SPIN_CAP = 1u << 21;
constexpr int OOB = 0x7FFFFF00;

__device__ __forceinline__ unsigned cvtpk(float lo, float hi) { return __builtin_bit_cast(unsigned, __builtin_convertvector(f32x2{lo, hi}, bf16x2)); }
__device__ __forceinline__ __amdgpu_buffer_rsrc_t mk(const void* p, unsigned bytes) { return __builtin_amdgcn_make_buffer_rsrc((void*)p, 0, bytes, 0x00020000); }
__device__ __forceinline__ v4i mksrd(const void* p, unsigned bytes) { return v4i{(int)(uintptr_t)p, (int)((uintptr_t)p >> 32), (int)bytes, 0x00020000}; }
__device__ __forceinline__ f32x4 mfma32(v4u a, v4u b, f32x4 c) { return __builtin_amdgcn_mfma_f32_16x16x32_bf16(__builtin_bit_cast(bf16x8, a), __builtin_bit_cast(bf16x8, b), c, 0, 0, 0); }
__device__ __forceinline__ f32x4 mfma16(v2u a, v2u b, f32x4 c) { return __builtin_amdgcn_mfma_f32_16x16x16bf16_1k(__builtin_bit_cast(s16x4, a), __builtin_bit_cast(s16x4, b), c, 0, 0, 0); }
__device__ __forceinline__ float ex2(float x) { return __builtin_amdgcn_exp2f(x); }
__device__ __forceinline__ v4u ld_sc1(__amdgpu_buffer_rsrc_t r, int vo, int so) { return __builtin_amdgcn_raw_buffer_load_b128(r, vo, so, 16); }
__device__ __forceinline__ void st_sc1(v4u v, __amdgpu_buffer_rsrc_t r, int vo, int so) { __builtin_amdgcn_raw_buffer_store_b128(v, r, vo, so, 16); }
template <int CTRL> __device__ __forceinline__ float dpp(float x) { return __builtin_bit_cast(float, __builtin_amdgcn_mov_dpp(__builtin_bit_cast(int, x), CTRL, 0xf, 0xf, true)); }
constexpr int XOR1 = 0xB1, XOR2 = 0x4E, HMIR = 0x141, ROR8 = 0x128;
__device__ __forceinline__ float x16_sum(float x) { auto s = __builtin_amdgcn_permlane16_swap(__float_as_uint(x), __float_as_uint(x), false, false); return __uint_as_float(s[0]) + __uint_as_float(s[1]); }
__device__ __forceinline__ float x32_sum(float x) { auto s = __builtin_amdgcn_permlane32_swap(__float_as_uint(x), __float_as_uint(x), false, false); return __uint_as_float(s[0]) + __uint_as_float(s[1]); }
__device__ __forceinline__ float xg_max(float x) {
  auto s = __builtin_amdgcn_permlane16_swap(__float_as_uint(x), __float_as_uint(x), false, false);
  x = fmaxf(__uint_as_float(s[0]), __uint_as_float(s[1]));
  auto t = __builtin_amdgcn_permlane32_swap(__float_as_uint(x), __float_as_uint(x), false, false);
  return fmaxf(__uint_as_float(t[0]), __uint_as_float(t[1]));
}
#define CBAR() asm volatile("" ::: "memory")
#define OPAQUE_S(x) asm volatile("" : "+s"(x))
#define LDSP(T, off) ((LAS T*)(lds + (off)))
__device__ __forceinline__ unsigned lds_ld(LAS unsigned char* lds, int off) { return __builtin_amdgcn_readfirstlane(*(volatile LAS unsigned*)(lds + off)); }
__device__ __forceinline__ void lds_st(LAS unsigned char* lds, int off, unsigned v) { *(volatile LAS unsigned*)(lds + off) = v; }
template <int SLP> __device__ __forceinline__ unsigned lds_wait_ge(LAS unsigned char* lds, int off, unsigned want) {
  unsigned v = lds_ld(lds, off);
#pragma unroll 1
  for (unsigned sp = 0; v < want && sp < SPIN_CAP * 8; ++sp) { __builtin_amdgcn_s_sleep(SLP); v = lds_ld(lds, off); }
  CBAR();
  return v;
}
__device__ __forceinline__ void lds_inc(LAS unsigned char* lds, int off, int lane) {
  CBAR();
  if (lane == 0) __hip_atomic_fetch_add(LDSP(unsigned, off), 1u, __ATOMIC_RELAXED, __HIP_MEMORY_SCOPE_WORKGROUP);
  CBAR();
}
#define STAMP(i) do {} while (0)
#define STAMP_OUT(lo, hi) do {} while (0)

template <int NFL> __device__ __forceinline__ void dma4(unsigned m0v, unsigned v0, unsigned v1, unsigned v2, unsigned v3, v4i srd, unsigned so) {
  asm volatile("s_mov_b32 m0, %0\n\t"
               "s_waitcnt vmcnt(%7)\n\tbuffer_load_dwordx4 %1, %5, %6 offen nt lds\n\t"
               "s_waitcnt vmcnt(%7)\n\tbuffer_load_dwordx4 %2, %5, %6 offen offset:1024 nt lds\n\t"
               "s_waitcnt vmcnt(%7)\n\tbuffer_load_dwordx4 %3, %5, %6 offen offset:2048 nt lds\n\t"
               "s_waitcnt vmcnt(%7)\n\tbuffer_load_dwordx4 %4, %5, %6 offen offset:3072 nt lds"
               :: "s"(m0v), "v"(v0), "v"(v1), "v"(v2), "v"(v3), "s"(srd), "s"(so), "i"(NFL - 1) : "memory");
}

template <class C>
__global__ void __launch_bounds__(NTHR, 1) fused_decode(const u16* xa, const u16* ak, const u16* av, const u16* wqa, const u16* woa,
                                                        const u16* xb, const u16* bk, const u16* bv, const u16* wqb, const u16* wob,
                                                        u16* out, unsigned char* ws) {
  constexpr int NFL = C::NFL, PARK = C::PARK;
  extern __shared__ __attribute__((aligned(16))) unsigned char lds_raw[];
  LAS unsigned char* lds = (LAS unsigned char*)lds_raw;
  const int wg = blockIdx.x, lane = threadIdx.x & 63, wave = __builtin_amdgcn_readfirstlane(threadIdx.x >> 6);
  const int xcd = wg & 7, jw = wg >> 3, b = jw >> 2, sp = jw & 3, h = 4 * xcd + sp;
  const int g = lane >> 4, n = lane & 15;
  const unsigned lds0 = (unsigned)(uintptr_t)lds;
  if (threadIdx.x < 16) *(volatile LAS unsigned*)(lds + 4 * lane) = 0u;
  asm volatile("s_waitcnt lgkmcnt(0)" :: "s"(wqb), "s"(wob), "s"(out), "s"(ws), "s"(xa) : "memory");
  __builtin_amdgcn_s_barrier();
  CBAR();

  if (wave == 0) {
    __builtin_amdgcn_s_setprio(3);
    const size_t ua = (size_t)(b * NH + h) * SK * DH, ub = (size_t)(b * HKV + xcd) * SK * DH;
    const unsigned k0 = g * 256 + ((n ^ g) * 16), vv = lane * 16;
    unsigned slot = lds0 + L_RING;
    int limit = RING;
    constexpr int LAG0 = (NFL + 4 + 7) / 8, LAG1 = (NFL + 7) / 8;
    static_assert(NFL >= 17 && NFL <= 32, "publish lag");
    STAMP(0);
    int t = 0;
#pragma unroll 1
    for (int ph = 0; ph < 4; ++ph) {
      const v4i rs = mksrd(ph == 0 ? ak + ua : ph == 1 ? bk + ub : ph == 2 ? bv + ub : av + ua, SK * 256);
      const int tend = ph == 0 ? 128 : ph == 1 ? 160 : ph == 2 ? 192 : NSL;
      const unsigned v0 = ph < 2 ? k0 : vv, v1 = ph < 2 ? k0 ^ 64 : vv, v2 = ph < 2 ? k0 ^ 128 : vv, v3 = ph < 2 ? k0 ^ 192 : vv;
      unsigned r = (ph == 1 || ph == 2) ? (sp * 32 * C::MUL) & 127 : 0u;
#pragma unroll 1
      for (; t < tend; ++t) {
        if (!(C::DBG & 4) && t >= limit) {
#pragma unroll 1
          for (unsigned spn = 0; spn < SPIN_CAP * 8; ++spn) {
            const v4u d = *(volatile LAS v4u*)(lds + L_DONE);
            const int c = min(min(4 * (int)d[0], 4 * (int)d[1] + 1), min(4 * (int)d[2] + 2, 4 * (int)d[3] + 3));
            limit = __builtin_amdgcn_readfirstlane(c) + RING;
            if (t < limit) break;
            __builtin_amdgcn_s_sleep(1);
          }
        }
        const unsigned so = (((r & 0x70u) << 1) | (r & 15u)) << 12;
        dma4<NFL>(slot, v0, v1, v2, v3, rs, so);
        if (LAG0 == LAG1 && t >= LAG0) lds_st(lds, L_FULL, (unsigned)(t - LAG0 + 1));
        dma4<NFL>(slot + 4096, v0, v1, v2, v3, rs, so + 65536u);
        if (LAG0 != LAG1 && t >= LAG1) lds_st(lds, L_FULL, (unsigned)(t - LAG1 + 1));
        slot += SLOT; if (slot >= lds0 + L_RING + RING * SLOT) slot = lds0 + L_RING;
        r = (r + C::MUL) & 127u;
      }
    }
    asm volatile("s_waitcnt vmcnt(24)" ::: "memory"); lds_st(lds, L_FULL, NSL - 3);
    asm volatile("s_waitcnt vmcnt(16)" ::: "memory"); lds_st(lds, L_FULL, NSL - 2);
    asm volatile("s_waitcnt vmcnt(8)" ::: "memory");  lds_st(lds, L_FULL, NSL - 1);
    asm volatile("s_waitcnt vmcnt(0)" ::: "memory");  lds_st(lds, L_FULL, NSL);
    STAMP(1); STAMP_OUT(0, 2); STAMP_OUT(16, 26);
  } else if (wave >= 2 && wave < 6) {
    if (C::DBG & 8) return;
    const int w = wave - 2, jj = n >> 2;
    int slot = L_RING + w * SLOT; unsigned full = 0, cnt = 0; int t = w;
#define SLOT_WAIT() do { if (full <= (unsigned)t) full = lds_wait_ge<1>(lds, L_FULL, (unsigned)t + 1u); CBAR(); } while (0)
#define SLOT_DONE() do { CBAR(); ++cnt; lds_st(lds, L_DONE + 4 * w, cnt);     \
        CBAR(); slot += 4 * SLOT; if (slot >= L_RING + RING * SLOT) slot -= RING * SLOT; t += 4; } while (0)
#define K_READ(T_) do { SLOT_WAIT(); _Pragma("unroll") for (int m_ = 0; m_ < 8; ++m_) T_[m_] = *LDSP(v4u, ((slot + kb) ^ (64 * (m_ & 3))) + (m_ >> 2) * 4096); SLOT_DONE(); } while (0)
    const int sw = L_S + w * 4096;
    {
      const int kb = n * 256 + ((g ^ n) * 16);
      v4u q[4];
      float mx = -3.0e38f;
      const int sa0 = sw + ((n & 3) * 8 + g * 2 + (jj & 1)) * 16;
      int sa = sa0;
#define K_MATH(T_, act_, step_) do { f32x4 s0 = {0.f, 0.f, 0.f, 0.f}, s1 = {0.f, 0.f, 0.f, 0.f}; \
        _Pragma("unroll") for (int m = 0; m < 4; ++m) { s0 = mfma32(T_[m], q[m], s0); s1 = mfma32(T_[4 + m], q[m], s1); }       \
        mx = fmaxf(fmaxf(fmaxf(fmaxf(s0[0], s0[1]), fmaxf(s0[2], s0[3])), fmaxf(fmaxf(s1[0], s1[1]), fmaxf(s1[2], s1[3]))), mx); \
        if (act_) *LDSP(f32x4, sa) = (jj & 1) ? s1 : s0; \
        sa += (step_); } while (0)
      {
        v4u P[PARK][8];
#pragma unroll
        for (int i = 0; i < PARK; ++i) K_READ(P[i]);
        lds_wait_ge<1>(lds, L_QRDY, 1u);
#pragma unroll
        for (int m = 0; m < 4; ++m) q[m] = *LDSP(v4u, L_QIMG + g * 16 + 64 * m);
        if (w == 0) STAMP(2);
#pragma unroll
        for (int i = 0; i < PARK; ++i) K_MATH(P[i], (n & 11) == 0, 128);
      }
#pragma unroll 1
      for (int ph = 0; ph < 2; ++ph) {
        const bool act = ph == 1 ? n < 8 : (n & 11) == 0;
        const int sstep = ph == 1 ? 512 : 128, tend = ph == 0 ? 128 : 160;
#pragma unroll 1
        while (t < tend) { v4u T[8]; K_READ(T); K_MATH(T, act, sstep); }
        {
          lds_wait_ge<1>(lds, L_QRDY + 4, 1u);
          const float Mh = xg_max(mx);
          const float M = __builtin_bit_cast(float, __builtin_amdgcn_ds_bpermute(((lane >> 3) & 3) * 4, __builtin_bit_cast(int, Mh)));
          const float mc = -M * SC2;
          float l = 0.f;
          CBAR();
#pragma unroll
          for (int j = 0; j < 4; ++j) {
            const f32x4 s = *LDSP(f32x4, sw + 16 * lane + 1024 * j);
            const float p0 = ex2(fmaf(s[0], SC2, mc)), p1 = ex2(fmaf(s[1], SC2, mc)), p2 = ex2(fmaf(s[2], SC2, mc)), p3 = ex2(fmaf(s[3], SC2, mc));
            l += (p0 + p1) + (p2 + p3);
            *LDSP(v2u, L_P + ph * 8192 + w * 2048 + 8 * lane + 512 * j) = v2u{cvtpk(p0, p1), cvtpk(p2, p3)};
          }
          l += dpp<XOR1>(l); l += dpp<XOR2>(l); l += dpp<HMIR>(l); l = x32_sum(l);
          const float la = x16_sum(l + dpp<ROR8>(l));
          if ((lane & 39) == 0) *LDSP(f32x2, L_ML + ph * 128 + w * 32 + lane) = f32x2{M, ph == 1 ? l : la};
          lds_inc(lds, L_MLC, lane);
        }
        mx = -3.0e38f; sa = sa0;
#pragma unroll
        for (int m = 0; m < 4; ++m) q[m] = *LDSP(v4u, L_QIMG + 256 + (n & 3) * 256 + g * 16 + 64 * m);
      }
    }
    {
      const unsigned Z = 0x0C0C0C0Cu;
      const unsigned el0 = jj == 0 ? 0x0C0C0100u : jj == 1 ? 0x01000C0Cu : Z, el1 = jj == 2 ? 0x0C0C0100u : jj == 3 ? 0x01000C0Cu : Z;
      const unsigned eh0 = jj == 0 ? 0x0C0C0302u : jj == 1 ? 0x03020C0Cu : Z, eh1 = jj == 2 ? 0x0C0C0302u : jj == 3 ? 0x03020C0Cu : Z;
      const int vb = g * 1024 + n * 16;
#pragma unroll 1
      for (int ph = 1; ph >= 0; --ph) {
        float f = 1.f;
        if (ph == 0) {
          lds_wait_ge<1>(lds, L_MLC, 8u);
          f32x2 ml[4];
#pragma unroll
          for (int u = 0; u < 4; ++u) ml[u] = *LDSP(f32x2, L_ML + u * 32);
          const float M = fmaxf(fmaxf(ml[0][0], ml[1][0]), fmaxf(ml[2][0], ml[3][0]));
          float L = 0.f;
#pragma unroll
          for (int u = 0; u < 4; ++u) L = fmaf(ex2((ml[u][0] - M) * SC2), ml[u][1], L);
          f = ex2(((*LDSP(f32x2, L_ML + w * 32))[0] - M) * SC2) / L;
        }
        const int tend = ph == 1 ? 192 : NSL, pstep = ph == 1 ? 256 : 64;
        int pa = L_P + ph * 8192 + w * 2048 + (ph == 1 ? (n & 3) * 64 : 0) + g * 16;
        f32x4 a0 = {0, 0, 0, 0}, a1 = {0, 0, 0, 0};
#pragma unroll 1
        while (t < tend) {
          const v4u pp = *LDSP(v4u, pa); pa += pstep;
          v2u bf[8];
#pragma unroll
          for (int i = 0; i < 8; ++i) { const unsigned pk = pp[i >> 1]; bf[i] = v2u{__builtin_amdgcn_perm(pk, pk, (i & 1) ? eh0 : el0), __builtin_amdgcn_perm(pk, pk, (i & 1) ? eh1 : el1)}; }
#pragma unroll
          for (int i = 0; i < 8; ++i) asm volatile("" : "+v"(bf[i]));
          SLOT_WAIT();
          v4u T[8];
#pragma unroll
          for (int i = 0; i < 8; ++i) T[i] = *LDSP(v4u, slot + vb + (i >> 2) * 4096 + (i & 3) * 256);
          SLOT_DONE();
#pragma unroll
          for (int i = 0; i < 8; ++i) { a0 = mfma16(v2u{T[i][0], T[i][1]}, bf[i], a0); a1 = mfma16(v2u{T[i][2], T[i][3]}, bf[i], a1); }
        }
        a0 *= f; a1 *= f;
        const int ro = (ph == 1 ? sw + (n & 3) * 512 : L_HREC + 2048 + w * 512) + jj * 128 + g * 32;
        if (ph == 1 || (n & 3) == 0) {
          *LDSP(f32x4, ro) = f32x4{a0[0], a1[0], a0[1], a1[1]};
          *LDSP(f32x4, ro + 16) = f32x4{a0[2], a1[2], a0[3], a1[3]};
        }
        lds_inc(lds, ph == 1 ? L_BCNT : L_RCNT + 4, lane);
      }
    }
    if (w == 3) { STAMP(3); STAMP_OUT(2, 4); }
  } else if (wave >= 6) {
    if (C::DBG & 8) return;
    const int w = wave - 6;
    if (C::PF_AUX >= 0) {
#pragma unroll 1
      for (int mat = 0; mat < 2; ++mat) {
        if (mat) lds_wait_ge<16>(lds, L_FULL, 32u);
        const __amdgpu_buffer_rsrc_t rw = mk((mat ? wqb : wqa) + (size_t)(16 * jw + w) * 4096 + xcd * 512, 13 * 8192);
        v4u d[4];
#pragma unroll
        for (int i = 0; i < 4; ++i) d[i] = __builtin_amdgcn_raw_buffer_load_b128(rw, lane * 16, ((i & 1) * 8 + (i >> 1) * 4) * 8192, C::PF_AUX < 0 ? 0 : C::PF_AUX);
#pragma unroll
        for (int i = 0; i < 4; ++i) asm volatile("" :: "v"(d[i]));

      }
    }
    if (C::DBG & 16) return;
    const __amdgpu_buffer_rsrc_t ry = mk(ws + W_YG, 2 * YHALF);
    const int yo = ((b * 32 + h) * 512 + 128 * w + 8 * n + 4 * (g & 1) + 2 * (g >> 1)) * 8;
    unsigned sl[4], sh[4];
#pragma unroll
    for (int d = 0; d < 4; ++d) { const bool on = ((n & 7) >> 1) == d; sl[d] = !on ? 0x0C0C0C0Cu : (n & 1) ? 0x01000C0Cu : 0x0C0C0100u; sh[d] = !on ? 0x0C0C0C0Cu : (n & 1) ? 0x03020C0Cu : 0x0C0C0302u; }
    v4u yb = {0, 0, 0, 0};
    lds_wait_ge<16>(lds, L_FULL, 72u);
#pragma unroll 1
    for (int pass = 0; pass < 2; ++pass) {
      const __amdgpu_buffer_rsrc_t rw = mk((pass == 0 ? wob : woa) + (size_t)h * 128 * 512, (C::DBG & 1) ? 0 : 128 * 1024);
      v4u wo[32];
#pragma unroll
      for (int i = 0; i < 32; ++i) {
        wo[i] = __builtin_amdgcn_raw_buffer_load_b128(rw, g * 1024 + w * 256 + n * 16, i * 4096, C::WO_AUX);
        __builtin_amdgcn_sched_barrier(0);
        __builtin_amdgcn_s_sleep(C::WO_SLEEP);
        __builtin_amdgcn_sched_barrier(0);
      }
      if (pass == 1) { lds_wait_ge<8>(lds, L_FULL, NSL - 8); st_sc1(yb, ry, yo, YHALF); }
      asm volatile("s_waitcnt vmcnt(1)" ::: "memory");
      lds_wait_ge<1>(lds, L_RCNT + 4 * pass, 4u);
      const unsigned tag = *LDSP(unsigned, L_TAG);
      const int rb = L_HREC + pass * 2048 + 8 * lane;
      const f32x2 o = (*LDSP(f32x2, rb) + *LDSP(f32x2, rb + 512)) + (*LDSP(f32x2, rb + 1024) + *LDSP(f32x2, rb + 1536));
      const int oi = L_OIMG + w * 256;
      *LDSP(unsigned, oi + 4 * lane) = cvtpk(o[0], o[1]);
      CBAR();
      v4u of[4];
#pragma unroll
      for (int u = 0; u < 4; ++u) of[u] = *LDSP(v4u, oi + 64 * g + 16 * u);
      f32x4 y0 = {0, 0, 0, 0}, y1 = {0, 0, 0, 0};
#pragma unroll
      for (int i = 0; i < 32; i += 2) {
        const unsigned pk = of[i >> 3][(i >> 1) & 3];
        v4u a0, a1;
#pragma unroll
        for (int d = 0; d < 4; ++d) { a0[d] = __builtin_amdgcn_perm(pk, pk, sl[d]); a1[d] = __builtin_amdgcn_perm(pk, pk, sh[d]); }
        y0 = mfma32(a0, wo[i], y0); y1 = mfma32(a1, wo[i + 1], y1);
      }
      y0 += y1;
      yb = v4u{__float_as_uint(g < 2 ? y0[0] : y0[2]), tag, __float_as_uint(g < 2 ? y0[1] : y0[3]), tag};
      st_sc1(yb, ry, pass == 0 ? OOB : yo, 0);
      lds_inc(lds, L_YPUB, lane);
      if (w == 0) STAMP(4 + pass);
    }
    if (w == 0) STAMP_OUT(4, 6);
  } else {
    if (C::DBG & 8) return;
    unsigned G;
    asm volatile("global_load_dword %0, %1, %2 sc1" : "=v"(G) : "v"(W_EPOCH), "s"(ws) : "memory");
    const int cb = xcd * 32 + jw;
    const __amdgpu_buffer_rsrc_t rs = mk(ws, W_END);
    unsigned tag = 0;
    const int ta = L_P + (8 * g + (n >> 2)) * 32 + (n & 3) * 8;
    int it = -1; OPAQUE_S(it);
    v4u xf[16];
#pragma unroll 1
    for (; it < 2; ++it) {
      if (it != 0) {
        if (it == 1) lds_wait_ge<16>(lds, L_FULL, 52u);
        const v4i rx = mksrd(it == 1 ? xb : xa, (C::DBG & 128) ? 0 : 8 * 512 * 2);
#pragma unroll
        for (int s = 0; s < 16; ++s) asm volatile("buffer_load_dwordx4 %0, %1, %2, 0 offen offset:%3" : "+v"(xf[s]) : "v"(n * 1024 + 16 * g), "s"(rx), "i"(64 * s) : "memory");
      }
      if (it >= 0) {
        if (it == 0) lds_wait_ge<1>(lds, L_FULL, C::T_SLICE);
        const v4i rw = mksrd((it ? wqb : wqa) + cb * 16, (C::DBG & 32) ? 0 : 512 * 8192 - cb * 32);
        const unsigned vo = (lane >> 1) * 8192 + (lane & 1) * 16;
#pragma unroll
        for (int i = 0; i < 16; ++i) {
          asm volatile("s_mov_b32 m0, %0\n\ts_nop 0\n\tbuffer_load_dwordx4 %1, %2, %3 offen nt lds" :: "s"(lds0 + L_P + i * 1024), "v"(vo), "s"(rw), "s"(i * 32 * 8192) : "memory");
          if (it) __builtin_amdgcn_s_sleep(C::WQB_SLEEP); else if (C::WQA_SLEEP) __builtin_amdgcn_s_sleep(C::WQA_SLEEP);
        }
        asm volatile("s_waitcnt vmcnt(0)\n\tv_readfirstlane_b32 %0, %1\n\ts_nop 1" : "=s"(tag) : "v"(G) : "memory");
        tag += 1u;
        if (it == 0) STAMP(6);
      }
#pragma unroll
      for (int s = 0; s < 16; ++s) asm volatile("" : "+v"(xf[s]));
      f32x4 d0 = {0, 0, 0, 0}, d1 = {0, 0, 0, 0};
      {
        v2u t[16][2];
#pragma unroll
        for (int s = 0; s < 16; ++s)
#pragma unroll
          for (int u = 0; u < 2; ++u) t[s][u] = __builtin_bit_cast(v2u, __builtin_amdgcn_ds_read_tr16_b64_v4i16(LDSP(s16x4, ta + s * 1024 + u * 128)));
        __builtin_amdgcn_sched_barrier(0);
#pragma unroll
        for (int s = 0; s < 16; s += 2) {
          d0 = mfma32(v4u{t[s][0][0], t[s][0][1], t[s][1][0], t[s][1][1]}, xf[s], d0);
          d1 = mfma32(v4u{t[s + 1][0][0], t[s + 1][0][1], t[s + 1][1][0], t[s + 1][1][1]}, xf[s + 1], d1);
        }
      }
      d0 += d1;
      st_sc1(v4u{cvtpk(d0[0], d0[1]), tag, cvtpk(d0[2], d0[3]), tag}, rs, (it >= 0 && n < 8 && !(C::DBG & 64)) ? W_QG + ((it * 8 + n) * 2048 + cb * 8 + 2 * g) * 8 : OOB, 0);
      if (it == 0) STAMP(7);
      if (it < 0) continue;
      lds_wait_ge<4>(lds, L_FULL, it ? 104u : C::T_GATHER);
      const int go = (C::DBG & (64 | 256)) ? OOB : it == 0 ? (lane < 32 ? W_QG + (b * 2048 + h * 64 + 2 * lane) * 8 : OOB) : W_QG + ((8 + b) * 2048 + xcd * 256 + 2 * lane) * 8;
      const u64 need = (C::DBG & (64 | 256)) ? 0ull : it == 0 ? 0xFFFFFFFFull : ~0ull;
#pragma unroll 1
      for (unsigned spn = 0; spn < SPIN_CAP; ++spn) {
        const v4u v = ld_sc1(rs, go, 0), u = ld_sc1(rs, it == 1 ? go : OOB, 1024);
        if (it == 1 || lane < 32) *LDSP(v2u, L_QIMG + (it == 1 ? 256 : 0) + 8 * lane) = v2u{v[0], v[2]};
        if (it == 1) *LDSP(v2u, L_QIMG + 768 + 8 * lane) = v2u{u[0], u[2]};
        if ((__builtin_amdgcn_ballot_w64(v[1] != tag || v[3] != tag || (it == 1 && (u[1] != tag || u[3] != tag))) & need) == 0) break;
        __builtin_amdgcn_s_sleep(24);
      }
      CBAR();
      lds_st(lds, L_QRDY + 4 * it, 1u);
      if (it == 0) STAMP(8);
    }
    lds_st(lds, L_TAG, tag);
    lds_wait_ge<16>(lds, L_BCNT, 4u);
    {
      float Mk = 0.f, Lk = 0.f;
#pragma unroll
      for (int hp = 0; hp < 2; ++hp) {
        const int hh = 2 * hp + (lane >> 5);
        f32x2 ml[4]; f32x4 ac[4];
#pragma unroll
        for (int u = 0; u < 4; ++u) { ml[u] = *LDSP(f32x2, L_ML + 128 + u * 32 + hh * 8); ac[u] = *LDSP(f32x4, L_S + u * 4096 + hh * 512 + 16 * (lane & 31)); }
        const float M = fmaxf(fmaxf(ml[0][0], ml[1][0]), fmaxf(ml[2][0], ml[3][0]));
        float L = 0.f; f32x4 o = {0, 0, 0, 0};
#pragma unroll
        for (int u = 0; u < 4; ++u) { const float f = ex2((ml[u][0] - M) * SC2); L = fmaf(f, ml[u][1], L); o += f * ac[u]; }
        st_sc1(__builtin_bit_cast(v4u, o), rs, W_PO + (xcd + 8 * (4 * b + hh)) * 2048 + sp * 512 + 16 * (lane & 31), 0);
        if ((lane & 31) == hp) { Mk = M; Lk = L; }
      }
      asm volatile("s_waitcnt vmcnt(0)" ::: "memory");
      const int hh = 2 * (lane & 31) + (lane >> 5);
      st_sc1(v4u{__float_as_uint(Mk), tag, __float_as_uint(Lk), tag}, rs, (lane & 31) < 2 ? W_ML + (xcd + 8 * (4 * b + hh)) * 128 + sp * 16 : OOB, 0);
    }
    lds_wait_ge<16>(lds, L_FULL, 192u + C::T_MLPOLL);
    v4u mlv = {0, 0, 0, 0};
#pragma unroll 1
    for (unsigned spn = 0; spn < SPIN_CAP; ++spn) {
      mlv = ld_sc1(rs, lane < 4 ? W_ML + wg * 128 + lane * 16 : OOB, 0);
      if ((__builtin_amdgcn_ballot_w64(mlv[1] != tag || mlv[3] != tag) & 15u) == 0u) break;
      __builtin_amdgcn_s_sleep(64);
    }
    __builtin_amdgcn_fence(__ATOMIC_ACQUIRE, "wavefront");
    {
      const v4u p0 = ld_sc1(rs, W_PO + wg * 2048 + 16 * lane, 0), p1 = ld_sc1(rs, W_PO + wg * 2048 + 16 * lane, 1024);
      const float m = __uint_as_float(mlv[0]);
      float M = fmaxf(m, dpp<XOR1>(m)); M = fmaxf(M, dpp<XOR2>(M));
      const float e = ex2((m - M) * SC2);
      float L = e * __uint_as_float(mlv[2]); L += dpp<XOR1>(L); L += dpp<XOR2>(L);
      const int fi = __builtin_bit_cast(int, e / L);
      const float f0 = __builtin_bit_cast(float, lane < 32 ? __builtin_amdgcn_readlane(fi, 0) : __builtin_amdgcn_readlane(fi, 1));
      const float f1 = __builtin_bit_cast(float, lane < 32 ? __builtin_amdgcn_readlane(fi, 2) : __builtin_amdgcn_readlane(fi, 3));
      *LDSP(f32x4, L_HREC + 16 * lane) = f0 * __builtin_bit_cast(f32x4, p0);
      *LDSP(f32x4, L_HREC + 1024 + 16 * lane) = f1 * __builtin_bit_cast(f32x4, p1);
      CBAR(); lds_st(lds, L_RCNT, 4u);
    }
    STAMP(9);
    if (C::DBG & 16) return;
#pragma unroll 1
    for (int pass = 0; pass < 2; ++pass) {
      if (pass == 0) lds_wait_ge<8>(lds, L_FULL, NSL - 1); else lds_wait_ge<1>(lds, L_YPUB, 8u);
      const int yo = W_YG + (pass == 0 ? YHALF : 0) + (b * 32 * 512 + (lane >> 3) * 512 + 16 * h + 2 * (lane & 7)) * 8;
      v4u y[4]; bool ok[4];
#pragma unroll
      for (int i = 0; i < 4; ++i) { y[i] = v4u{0, 0, 0, 0}; ok[i] = false; }
#pragma unroll 1
      for (unsigned spn = 0; spn < SPIN_CAP; ++spn) {
        v4u z[4];
#pragma unroll
        for (int i = 0; i < 4; ++i) z[i] = ld_sc1(rs, ok[i] ? OOB : yo, i * 8 * 512 * 8);
        bool all = true;
#pragma unroll
        for (int i = 0; i < 4; ++i) { if (!ok[i]) { y[i] = z[i]; ok[i] = z[i][1] == tag && z[i][3] == tag; } all &= ok[i]; }
        if (__builtin_amdgcn_ballot_w64(!all) == 0) break;
        __builtin_amdgcn_s_sleep(3);
      }
      float s0 = (__uint_as_float(y[0][0]) + __uint_as_float(y[1][0])) + (__uint_as_float(y[2][0]) + __uint_as_float(y[3][0]));
      float s1 = (__uint_as_float(y[0][2]) + __uint_as_float(y[1][2])) + (__uint_as_float(y[2][2]) + __uint_as_float(y[3][2]));
      s0 += dpp<ROR8>(s0); s1 += dpp<ROR8>(s1);
      s0 = x32_sum(x16_sum(s0)); s1 = x32_sum(x16_sum(s1));
      if (lane < 8) *(GAS unsigned*)(out + b * 1024 + (pass == 0 ? 512 : 0) + 16 * h + 2 * lane) = cvtpk(s0, s1);
      STAMP(10 + pass);
    }
    if (wg == 0 && lane == 0) __hip_atomic_store((GAS unsigned*)(ws + W_EPOCH), tag, RLX_AGENT);
    STAMP_OUT(6, 12);
  }
}

template <class C>
static void launch_cfg(void* const* d_in, void* d_out, void* d_ws, hipStream_t stream) {
  (void)hipFuncSetAttribute((const void*)fused_decode<C>, hipFuncAttributeMaxDynamicSharedMemorySize, LDS_BYTES);
  hipLaunchKernelGGL(fused_decode<C>, dim3(256), dim3(NTHR), LDS_BYTES, stream,
                     (const u16*)d_in[0], (const u16*)d_in[1], (const u16*)d_in[2], (const u16*)d_in[3], (const u16*)d_in[4],
                     (const u16*)d_in[5], (const u16*)d_in[6], (const u16*)d_in[7], (const u16*)d_in[8], (const u16*)d_in[9],
                     (u16*)d_out, (unsigned char*)d_ws);
}
extern "C" void kernel_launch(void* const* d_in, const int* in_sizes, int n_in, void* d_out, int out_size, void* d_ws, size_t ws_size, hipStream_t stream) {
  (void)in_sizes; (void)n_in; (void)out_size; (void)ws_size;
  launch_cfg<CfgDefault>(d_in, d_out, d_ws, stream);
}
```

```cpp
#include <hip/hip_runtime.h>
#include <hip/hip_bf16.h>
#include <stdint.h>

typedef unsigned v4u __attribute__((ext_vector_type(4)));
typedef unsigned v2u __attribute__((ext_vector_type(2)));
typedef int v4i __attribute__((ext_vector_type(4)));
typedef float f32x4 __attribute__((ext_vector_type(4)));
typedef float f32x2 __attribute__((ext_vector_type(2)));
typedef short s16x4 __attribute__((ext_vector_type(4)));
typedef __bf16 bf16x8 __attribute__((ext_vector_type(8)));
typedef __bf16 bf16x2 __attribute__((ext_vector_type(2)));
typedef unsigned short u16;
typedef unsigned long long u64;
#define GAS __attribute__((address_space(1)))
#define LAS __attribute__((address_space(3)))
#define RLX_AGENT __ATOMIC_RELAXED, __HIP_MEMORY_SCOPE_AGENT

struct CfgDefault {
  static constexpr int NFL = 20;
  static constexpr int MUL = 5;
  static constexpr int WO_AUX = 0;
  static constexpr int WO_SLEEP = 12;
  static constexpr int PF_AUX = 0;
  static constexpr int T_SLICE = 3, T_GATHER = 16, T_MLPOLL = 40;
  static constexpr int WQA_SLEEP = 0, WQB_SLEEP = 12;
  static constexpr int PARK = 4;
  static constexpr int DBG = 0;
};

constexpr int SK = 4096, DH = 128, NH = 32, HKV = 8;
constexpr float SC2 = 0.088388347648318440f * 1.4426950408889634f;
constexpr int NWAVE = 10, NTHR = NWAVE * 64;
constexpr int NSL = 320;
constexpr int RING = 15, SLOT = 8192;
constexpr int L_FULL = 0, L_QRDY = 4  , L_BCNT = 12, L_DONE = 16, L_RCNT = 32  , L_TAG = 40, L_YPUB = 44, L_MLC = 48;
constexpr int L_QIMG = 64;
constexpr int L_OIMG = L_QIMG + 1280;
constexpr int L_ML = L_OIMG + 1024;
constexpr int L_HREC = L_ML + 256;
constexpr int L_S = 8192;
constexpr int L_P = L_S + 16384;
constexpr int L_RING = L_P + 16384;
constexpr int LDS_BYTES = 160 * 1024;
static_assert(L_HREC % 16 == 0 && L_HREC + 8 * 512 <= 7168 && L_RING + RING * SLOT <= LDS_BYTES, "lds map");
constexpr int W_EPOCH = 0;
constexpr int W_ML = 4096;
constexpr int W_QG = 65536;
constexpr int W_PO = W_QG + 2 * 8 * 2048 * 8;
constexpr int W_YG = W_PO + 256 * 2048;
constexpr int YHALF = 8 * 32 * 512 * 8;
constexpr int W_END = W_YG + 2 * YHALF;
constexpr size_t W_STAMP = 64u << 20;
constexpr unsigned SPIN_CAP = 1u << 21;
constexpr int OOB = 0x7FFFFF00;

__device__ __forceinline__ unsigned cvtpk(float lo, float hi) { return __builtin_bit_cast(unsigned, __builtin_convertvector(f32x2{lo, hi}, bf16x2)); }
__device__ __forceinline__ __amdgpu_buffer_rsrc_t mk(const void* p, unsigned bytes) { return __builtin_amdgcn_make_buffer_rsrc((void*)p, 0, bytes, 0x00020000); }
__device__ __forceinline__ v4i mksrd(const void* p, unsigned bytes) { return v4i{(int)(uintptr_t)p, (int)((uintptr_t)p >> 32), (int)bytes, 0x00020000}; }
__device__ __forceinline__ f32x4 mfma32(v4u a, v4u b, f32x4 c) { return __builtin_amdgcn_mfma_f32_16x16x32_bf16(__builtin_bit_cast(bf16x8, a), __builtin_bit_cast(bf16x8, b), c, 0, 0, 0); }
__device__ __forceinline__ f32x4 mfma16(v2u a, v2u b, f32x4 c) { return __builtin_amdgcn_mfma_f32_16x16x16bf16_1k(__builtin_bit_cast(s16x4, a), __builtin_bit_cast(s16x4, b), c, 0, 0, 0); }
__device__ __forceinline__ float ex2(float x) { return __builtin_amdgcn_exp2f(x); }
__device__ __forceinline__ v4u ld_sc1(__amdgpu_buffer_rsrc_t r, int vo, int so) { return __builtin_amdgcn_raw_buffer_load_b128(r, vo, so, 16); }
__device__ __forceinline__ void st_sc1(v4u v, __amdgpu_buffer_rsrc_t r, int vo, int so) { __builtin_amdgcn_raw_buffer_store_b128(v, r, vo, so, 16); }
template <int CTRL> __device__ __forceinline__ float dpp(float x) { return __builtin_bit_cast(float, __builtin_amdgcn_mov_dpp(__builtin_bit_cast(int, x), CTRL, 0xf, 0xf, true)); }
constexpr int XOR1 = 0xB1, XOR2 = 0x4E, HMIR = 0x141, ROR8 = 0x128;
__device__ __forceinline__ float x16_sum(float x) { auto s = __builtin_amdgcn_permlane16_swap(__float_as_uint(x), __float_as_uint(x), false, false); return __uint_as_float(s[0]) + __uint_as_float(s[1]); }
__device__ __forceinline__ float x32_sum(float x) { auto s = __builtin_amdgcn_permlane32_swap(__float_as_uint(x), __float_as_uint(x), false, false); return __uint_as_float(s[0]) + __uint_as_float(s[1]); }
__device__ __forceinline__ float xg_max(float x) {
  auto s = __builtin_amdgcn_permlane16_swap(__float_as_uint(x), __float_as_uint(x), false, false);
  x = fmaxf(__uint_as_float(s[0]), __uint_as_float(s[1]));
  auto t = __builtin_amdgcn_permlane32_swap(__float_as_uint(x), __float_as_uint(x), false, false);
  return fmaxf(__uint_as_float(t[0]), __uint_as_float(t[1]));
}
#define CBAR() asm volatile("" ::: "memory")
#define OPAQUE_S(x) asm volatile("" : "+s"(x))
#define LDSP(T, off) ((LAS T*)(lds + (off)))
__device__ __forceinline__ unsigned lds_ld(LAS unsigned char* lds, int off) { return __builtin_amdgcn_readfirstlane(*(volatile LAS unsigned*)(lds + off)); }
__device__ __forceinline__ void lds_st(LAS unsigned char* lds, int off, unsigned v) { *(volatile LAS unsigned*)(lds + off) = v; }
template <int SLP> __device__ __forceinline__ unsigned lds_wait_ge(LAS unsigned char* lds, int off, unsigned want) {
  unsigned v = lds_ld(lds, off);
#pragma unroll 1
  for (unsigned sp = 0; v < want && sp < SPIN_CAP * 8; ++sp) { __builtin_amdgcn_s_sleep(SLP); v = lds_ld(lds, off); }
  CBAR();
  return v;
}
__device__ __forceinline__ void lds_inc(LAS unsigned char* lds, int off, int lane) {
  CBAR();
  if (lane == 0) __hip_atomic_fetch_add(LDSP(unsigned, off), 1u, __ATOMIC_RELAXED, __HIP_MEMORY_SCOPE_WORKGROUP);
  CBAR();
}
#define STAMP(i) do {} while (0)
#define STAMP_OUT(lo, hi) do {} while (0)

template <int NFL> __device__ __forceinline__ void dma4(unsigned m0v, unsigned v0, unsigned v1, unsigned v2, unsigned v3, v4i srd, unsigned so) {
  asm volatile("s_mov_b32 m0, %0\n\t"
               "s_waitcnt vmcnt(%7)\n\tbuffer_load_dwordx4 %1, %5, %6 offen nt lds\n\t"
               "s_waitcnt vmcnt(%7)\n\tbuffer_load_dwordx4 %2, %5, %6 offen offset:1024 nt lds\n\t"
               "s_waitcnt vmcnt(%7)\n\tbuffer_load_dwordx4 %3, %5, %6 offen offset:2048 nt lds\n\t"
               "s_waitcnt vmcnt(%7)\n\tbuffer_load_dwordx4 %4, %5, %6 offen offset:3072 nt lds"
               :: "s"(m0v), "v"(v0), "v"(v1), "v"(v2), "v"(v3), "s"(srd), "s"(so), "i"(NFL - 1) : "memory");
}

template <class C>
__global__ void __launch_bounds__(NTHR, 1) fused_decode(const u16* xa, const u16* ak, const u16* av, const u16* wqa, const u16* woa,
                                                        const u16* xb, const u16* bk, const u16* bv, const u16* wqb, const u16* wob,
                                                        u16* out, unsigned char* ws) {
  constexpr int NFL = C::NFL, PARK = C::PARK;
  extern __shared__ __attribute__((aligned(16))) unsigned char lds_raw[];
  LAS unsigned char* lds = (LAS unsigned char*)lds_raw;
  const int wg = blockIdx.x, lane = threadIdx.x & 63, wave = __builtin_amdgcn_readfirstlane(threadIdx.x >> 6);
  const int xcd = wg & 7, jw = wg >> 3, b = jw >> 2, sp = jw & 3, h = 4 * xcd + sp;
  const int g = lane >> 4, n = lane & 15;
  const unsigned lds0 = (unsigned)(uintptr_t)lds;
  if (threadIdx.x < 16) *(volatile LAS unsigned*)(lds + 4 * lane) = 0u;
  asm volatile("s_waitcnt lgkmcnt(0)" :: "s"(wqb), "s"(wob), "s"(out), "s"(ws), "s"(xa) : "memory");
  __builtin_amdgcn_s_barrier();
  CBAR();

  if (wave == 0) {
    __builtin_amdgcn_s_setprio(3);
    const size_t ua = (size_t)(b * NH + h) * SK * DH, ub = (size_t)(b * HKV + xcd) * SK * DH;
    const unsigned k0 = g * 256 + ((n ^ g) * 16), vv = lane * 16;
    unsigned slot = lds0 + L_RING;
    int limit = RING;
    constexpr int LAG0 = (NFL + 4 + 7) / 8, LAG1 = (NFL + 7) / 8;
    static_assert(NFL >= 17 && NFL <= 32, "publish lag");
    STAMP(0);
    int t = 0;
#pragma unroll 1
    for (int ph = 0; ph < 4; ++ph) {
      const v4i rs = mksrd(ph == 0 ? ak + ua : ph == 1 ? bk + ub : ph == 2 ? bv + ub : av + ua, SK * 256);
      const int tend = ph == 0 ? 128 : ph == 1 ? 160 : ph == 2 ? 192 : NSL;
      const unsigned v0 = ph < 2 ? k0 : vv, v1 = ph < 2 ? k0 ^ 64 : vv, v2 = ph < 2 ? k0 ^ 128 : vv, v3 = ph < 2 ? k0 ^ 192 : vv;
      unsigned r = (ph == 1 || ph == 2) ? (sp * 32 * C::MUL) & 127 : 0u;
#pragma unroll 1
      for (; t < tend; ++t) {
        if (!(C::DBG & 4) && t >= limit) {
#pragma unroll 1
          for (unsigned spn = 0; spn < SPIN_CAP * 8; ++spn) {
            const v4u d = *(volatile LAS v4u*)(lds + L_DONE);
            const int c = min(min(4 * (int)d[0], 4 * (int)d[1] + 1), min(4 * (int)d[2] + 2, 4 * (int)d[3] + 3));
            limit = __builtin_amdgcn_readfirstlane(c) + RING;
            if (t < limit) break;
            __builtin_amdgcn_s_sleep(1);
          }
        }
        const unsigned so = (((r & 0x70u) << 1) | (r & 15u)) << 12;
        dma4<NFL>(slot, v0, v1, v2, v3, rs, so);
        if (LAG0 == LAG1 && t >= LAG0) lds_st(lds, L_FULL, (unsigned)(t - LAG0 + 1));
        dma4<NFL>(slot + 4096, v0, v1, v2, v3, rs, so + 65536u);
        if (LAG0 != LAG1 && t >= LAG1) lds_st(lds, L_FULL, (unsigned)(t - LAG1 + 1));
        slot += SLOT; if (slot >= lds0 + L_RING + RING * SLOT) slot = lds0 + L_RING;
        r = (r + C::MUL) & 127u;
      }
    }
    asm volatile("s_waitcnt vmcnt(24)" ::: "memory"); lds_st(lds, L_FULL, NSL - 3);
    asm volatile("s_waitcnt vmcnt(16)" ::: "memory"); lds_st(lds, L_FULL, NSL - 2);
    asm volatile("s_waitcnt vmcnt(8)" ::: "memory");  lds_st(lds, L_FULL, NSL - 1);
    asm volatile("s_waitcnt vmcnt(0)" ::: "memory");  lds_st(lds, L_FULL, NSL);
    STAMP(1); STAMP_OUT(0, 2); STAMP_OUT(16, 26);
  } else if (wave >= 2 && wave < 6) {
    if (C::DBG & 8) return;
    const int w = wave - 2, jj = n >> 2;
    int slot = L_RING + w * SLOT; unsigned full = 0, cnt = 0; int t = w;
#define SLOT_WAIT() do { if (full <= (unsigned)t) full = lds_wait_ge<1>(lds, L_FULL, (unsigned)t + 1u); CBAR(); } while (0)
#define SLOT_DONE() do { CBAR(); ++cnt; lds_st(lds, L_DONE + 4 * w, cnt);     \
        CBAR(); slot += 4 * SLOT; if (slot >= L_RING + RING * SLOT) slot -= RING * SLOT; t += 4; } while (0)
#define K_READ(T_) do { SLOT_WAIT(); _Pragma("unroll") for (int m_ = 0; m_ < 8; ++m_) T_[m_] = *LDSP(v4u, ((slot + kb) ^ (64 * (m_ & 3))) + (m_ >> 2) * 4096); SLOT_DONE(); } while (0)
    const int sw = L_S + w * 4096;
    {
      const int kb = n * 256 + ((g ^ n) * 16);
      v4u q[4];
      float mx = -3.0e38f;
      const int sa0 = sw + ((n & 3) * 8 + g * 2 + (jj & 1)) * 16;
      int sa = sa0;
#define K_MATH(T_, act_, step_) do { f32x4 s0 = {0.f, 0.f, 0.f, 0.f}, s1 = {0.f, 0.f, 0.f, 0.f}; \
        _Pragma("unroll") for (int m = 0; m < 4; ++m) { s0 = mfma32(T_[m], q[m], s0); s1 = mfma32(T_[4 + m], q[m], s1); }       \
        mx = fmaxf(fmaxf(fmaxf(fmaxf(s0[0], s0[1]), fmaxf(s0[2], s0[3])), fmaxf(fmaxf(s1[0], s1[1]), fmaxf(s1[2], s1[3]))), mx); \
        if (act_) *LDSP(f32x4, sa) = (jj & 1) ? s1 : s0; \
        sa += (step_); } while (0)
      {
        v4u P[PARK][8];
#pragma unroll
        for (int i = 0; i < PARK; ++i) K_READ(P[i]);
        lds_wait_ge<1>(lds, L_QRDY, 1u);
#pragma unroll
        for (int m = 0; m < 4; ++m) q[m] = *LDSP(v4u, L_QIMG + g * 16 + 64 * m);
        if (w == 0) STAMP(2);
#pragma unroll
        for (int i = 0; i < PARK; ++i) K_MATH(P[i], (n & 11) == 0, 128);
      }
#pragma unroll 1
      for (int ph = 0; ph < 2; ++ph) {
        const bool act = ph == 1 ? n < 8 : (n & 11) == 0;
        const int sstep = ph == 1 ? 512 : 128, tend = ph == 0 ? 128 : 160;
#pragma unroll 1
        while (t < tend) { v4u T[8]; K_READ(T); K_MATH(T, act, sstep); }
        {
          lds_wait_ge<1>(lds, L_QRDY + 4, 1u);
          const float Mh = xg_max(mx);
          const float M = __builtin_bit_cast(float, __builtin_amdgcn_ds_bpermute(((lane >> 3) & 3) * 4, __builtin_bit_cast(int, Mh)));
          const float mc = -M * SC2;
          float l = 0.f;
          CBAR();
#pragma unroll
          for (int j = 0; j < 4; ++j) {
            const f32x4 s = *LDSP(f32x4, sw + 16 * lane + 1024 * j);
            const float p0 = ex2(fmaf(s[0], SC2, mc)), p1 = ex2(fmaf(s[1], SC2, mc)), p2 = ex2(fmaf(s[2], SC2, mc)), p3 = ex2(fmaf(s[3], SC2, mc));
            l += (p0 + p1) + (p2 + p3);
            *LDSP(v2u, L_P + ph * 8192 + w * 2048 + 8 * lane + 512 * j) = v2u{cvtpk(p0, p1), cvtpk(p2, p3)};
          }
          l += dpp<XOR1>(l); l += dpp<XOR2>(l); l += dpp<HMIR>(l); l = x32_sum(l);
          const float la = x16_sum(l + dpp<ROR8>(l));
          if ((lane & 39) == 0) *LDSP(f32x2, L_ML + ph * 128 + w * 32 + lane) = f32x2{M, ph == 1 ? l : la};
          lds_inc(lds, L_MLC, lane);
        }
        mx = -3.0e38f; sa = sa0;
#pragma unroll
        for (int m = 0; m < 4; ++m) q[m] = *LDSP(v4u, L_QIMG + 256 + (n & 3) * 256 + g * 16 + 64 * m);
      }
    }
    {
      const unsigned Z = 0x0C0C0C0Cu;
      const unsigned el0 = jj == 0 ? 0x0C0C0100u : jj == 1 ? 0x01000C0Cu : Z, el1 = jj == 2 ? 0x0C0C0100u : jj == 3 ? 0x01000C0Cu : Z;
      const unsigned eh0 = jj == 0 ? 0x0C0C0302u : jj == 1 ? 0x03020C0Cu : Z, eh1 = jj == 2 ? 0x0C0C0302u : jj == 3 ? 0x03020C0Cu : Z;
      const int vb = g * 1024 + n * 16;
#pragma unroll 1
      for (int ph = 1; ph >= 0; --ph) {
        float f = 1.f;
        if (ph == 0) {
          lds_wait_ge<1>(lds, L_MLC, 8u);
          f32x2 ml[4];
#pragma unroll
          for (int u = 0; u < 4; ++u) ml[u] = *LDSP(f32x2, L_ML + u * 32);
          const float M = fmaxf(fmaxf(ml[0][0], ml[1][0]), fmaxf(ml[2][0], ml[3][0]));
          float L = 0.f;
#pragma unroll
          for (int u = 0; u < 4; ++u) L = fmaf(ex2((ml[u][0] - M) * SC2), ml[u][1], L);
          f = ex2(((*LDSP(f32x2, L_ML + w * 32))[0] - M) * SC2) / L;
        }
        const int tend = ph == 1 ? 192 : NSL, pstep = ph == 1 ? 256 : 64;
        int pa = L_P + ph * 8192 + w * 2048 + (ph == 1 ? (n & 3) * 64 : 0) + g * 16;
        f32x4 a0 = {0, 0, 0, 0}, a1 = {0, 0, 0, 0};
#pragma unroll 1
        while (t < tend) {
          const v4u pp = *LDSP(v4u, pa); pa += pstep;
          v2u bf[8];
#pragma unroll
          for (int i = 0; i < 8; ++i) { const unsigned pk = pp[i >> 1]; bf[i] = v2u{__builtin_amdgcn_perm(pk, pk, (i & 1) ? eh0 : el0), __builtin_amdgcn_perm(pk, pk, (i & 1) ? eh1 : el1)}; }
#pragma unroll
          for (int i = 0; i < 8; ++i) asm volatile("" : "+v"(bf[i]));
          SLOT_WAIT();
          v4u T[8];
#pragma unroll
          for (int i = 0; i < 8; ++i) T[i] = *LDSP(v4u, slot + vb + (i >> 2) * 4096 + (i & 3) * 256);
          SLOT_DONE();
#pragma unroll
          for (int i = 0; i < 8; ++i) { a0 = mfma16(v2u{T[i][0], T[i][1]}, bf[i], a0); a1 = mfma16(v2u{T[i][2], T[i][3]}, bf[i], a1); }
        }
        a0 *= f; a1 *= f;
        const int ro = (ph == 1 ? sw + (n & 3) * 512 : L_HREC + 2048 + w * 512) + jj * 128 + g * 32;
        if (ph == 1 || (n & 3) == 0) {
          *LDSP(f32x4, ro) = f32x4{a0[0], a1[0], a0[1], a1[1]};
          *LDSP(f32x4, ro + 16) = f32x4{a0[2], a1[2], a0[3], a1[3]};
        }
        lds_inc(lds, ph == 1 ? L_BCNT : L_RCNT + 4, lane);
      }
    }
    if (w == 3) { STAMP(3); STAMP_OUT(2, 4); }
  } else if (wave >= 6) {
    if (C::DBG & 8) return;
    const int w = wave - 6;
    if (C::PF_AUX >= 0) {
#pragma unroll 1
      for (int mat = 0; mat < 2; ++mat) {
        if (mat) lds_wait_ge<16>(lds, L_FULL, 32u);
        const __amdgpu_buffer_rsrc_t rw = mk((mat ? wqb : wqa) + (size_t)(16 * jw + w) * 4096 + xcd * 512, 13 * 8192);
        v4u d[4];
#pragma unroll
        for (int i = 0; i < 4; ++i) d[i] = __builtin_amdgcn_raw_buffer_load_b128(rw, lane * 16, ((i & 1) * 8 + (i >> 1) * 4) * 8192, C::PF_AUX < 0 ? 0 : C::PF_AUX);
#pragma unroll
        for (int i = 0; i < 4; ++i) asm volatile("" :: "v"(d[i]));

      }
    }
    if (C::DBG & 16) return;
    const __amdgpu_buffer_rsrc_t ry = mk(ws + W_YG, 2 * YHALF);
    const int yo = ((b * 32 + h) * 512 + 128 * w + 8 * n + 4 * (g & 1) + 2 * (g >> 1)) * 8;
    unsigned sl[4], sh[4];
#pragma unroll
    for (int d = 0; d < 4; ++d) { const bool on = ((n & 7) >> 1) == d; sl[d] = !on ? 0x0C0C0C0Cu : (n & 1) ? 0x01000C0Cu : 0x0C0C0100u; sh[d] = !on ? 0x0C0C0C0Cu : (n & 1) ? 0x03020C0Cu : 0x0C0C0302u; }
    v4u yb = {0, 0, 0, 0};
    lds_wait_ge<16>(lds, L_FULL, 72u);
#pragma unroll 1
    for (int pass = 0; pass < 2; ++pass) {
      const __amdgpu_buffer_rsrc_t rw = mk((pass == 0 ? wob : woa) + (size_t)h * 128 * 512, (C::DBG & 1) ? 0 : 128 * 1024);
      v4u wo[32];
#pragma unroll
      for (int i = 0; i < 32; ++i) {
        wo[i] = __builtin_amdgcn_raw_buffer_load_b128(rw, g * 1024 + w * 256 + n * 16, i * 4096, C::WO_AUX);
        __builtin_amdgcn_sched_barrier(0);
        __builtin_amdgcn_s_sleep(C::WO_SLEEP);
        __builtin_amdgcn_sched_barrier(0);
      }
      if (pass == 1) { lds_wait_ge<8>(lds, L_FULL, NSL - 8); st_sc1(yb, ry, yo, YHALF); }
      asm volatile("s_waitcnt vmcnt(1)" ::: "memory");
      lds_wait_ge<1>(lds, L_RCNT + 4 * pass, 4u);
      const unsigned tag = *LDSP(unsigned, L_TAG);
      const int rb = L_HREC + pass * 2048 + 8 * lane;
      const f32x2 o = (*LDSP(f32x2, rb) + *LDSP(f32x2, rb + 512)) + (*LDSP(f32x2, rb + 1024) + *LDSP(f32x2, rb + 1536));
      const int oi = L_OIMG + w * 256;
      *LDSP(unsigned, oi + 4 * lane) = cvtpk(o[0], o[1]);
      CBAR();
      v4u of[4];
#pragma unroll
      for (int u = 0; u < 4; ++u) of[u] = *LDSP(v4u, oi + 64 * g + 16 * u);
      f32x4 y0 = {0, 0, 0, 0}, y1 = {0, 0, 0, 0};
#pragma unroll
      for (int i = 0; i < 32; i += 2) {
        const unsigned pk = of[i >> 3][(i >> 1) & 3];
        v4u a0, a1;
#pragma unroll
        for (int d = 0; d < 4; ++d) { a0[d] = __builtin_amdgcn_perm(pk, pk, sl[d]); a1[d] = __builtin_amdgcn_perm(pk, pk, sh[d]); }
        y0 = mfma32(a0, wo[i], y0); y1 = mfma32(a1, wo[i + 1], y1);
      }
      y0 += y1;
      yb = v4u{__float_as_uint(g < 2 ? y0[0] : y0[2]), tag, __float_as_uint(g < 2 ? y0[1] : y0[3]), tag};
      st_sc1(yb, ry, pass == 0 ? OOB : yo, 0);
      lds_inc(lds, L_YPUB, lane);
      if (w == 0) STAMP(4 + pass);
    }
    if (w == 0) STAMP_OUT(4, 6);
  } else {
    if (C::DBG & 8) return;
    const u64 tstart = __builtin_amdgcn_s_memrealtime();
    unsigned G;
    asm volatile("global_load_dword %0, %1, %2 sc1" : "=v"(G) : "v"(W_EPOCH), "s"(ws) : "memory");
    const int cb = xcd * 32 + jw;
    const __amdgpu_buffer_rsrc_t rs = mk(ws, W_END);
    unsigned tag = 0;
    const int ta = L_P + (8 * g + (n >> 2)) * 32 + (n & 3) * 8;
    int it = -1; OPAQUE_S(it);
    v4u xf[16];
#pragma unroll 1
    for (; it < 2; ++it) {
      if (it != 0) {
        if (it == 1) lds_wait_ge<16>(lds, L_FULL, 52u);
        const v4i rx = mksrd(it == 1 ? xb : xa, (C::DBG & 128) ? 0 : 8 * 512 * 2);
#pragma unroll
        for (int s = 0; s < 16; ++s) asm volatile("buffer_load_dwordx4 %0, %1, %2, 0 offen offset:%3" : "+v"(xf[s]) : "v"(n * 1024 + 16 * g), "s"(rx), "i"(64 * s) : "memory");
      }
      if (it >= 0) {
        if (it == 0) lds_wait_ge<1>(lds, L_FULL, C::T_SLICE);
        const v4i rw = mksrd((it ? wqb : wqa) + cb * 16, (C::DBG & 32) ? 0 : 512 * 8192 - cb * 32);
        const unsigned vo = (lane >> 1) * 8192 + (lane & 1) * 16;
#pragma unroll
        for (int i = 0; i < 16; ++i) {
          asm volatile("s_mov_b32 m0, %0\n\ts_nop 0\n\tbuffer_load_dwordx4 %1, %2, %3 offen nt lds" :: "s"(lds0 + L_P + i * 1024), "v"(vo), "s"(rw), "s"(i * 32 * 8192) : "memory");
          if (it) __builtin_amdgcn_s_sleep(C::WQB_SLEEP); else if (C::WQA_SLEEP) __builtin_amdgcn_s_sleep(C::WQA_SLEEP);
        }
        asm volatile("s_waitcnt vmcnt(0)\n\tv_readfirstlane_b32 %0, %1\n\ts_nop 1" : "=s"(tag) : "v"(G) : "memory");
        tag += 1u;
        if (it == 0) STAMP(6);
      }
#pragma unroll
      for (int s = 0; s < 16; ++s) asm volatile("" : "+v"(xf[s]));
      f32x4 d0 = {0, 0, 0, 0}, d1 = {0, 0, 0, 0};
      {
        v2u t[16][2];
#pragma unroll
        for (int s = 0; s < 16; ++s)
#pragma unroll
          for (int u = 0; u < 2; ++u) t[s][u] = __builtin_bit_cast(v2u, __builtin_amdgcn_ds_read_tr16_b64_v4i16(LDSP(s16x4, ta + s * 1024 + u * 128)));
        __builtin_amdgcn_sched_barrier(0);
#pragma unroll
        for (int s = 0; s < 16; s += 2) {
          d0 = mfma32(v4u{t[s][0][0], t[s][0][1], t[s][1][0], t[s][1][1]}, xf[s], d0);
          d1 = mfma32(v4u{t[s + 1][0][0], t[s + 1][0][1], t[s + 1][1][0], t[s + 1][1][1]}, xf[s + 1], d1);
        }
      }
      d0 += d1;
      st_sc1(v4u{cvtpk(d0[0], d0[1]), tag, cvtpk(d0[2], d0[3]), tag}, rs, (it >= 0 && n < 8 && !(C::DBG & 64)) ? W_QG + ((it * 8 + n) * 2048 + cb * 8 + 2 * g) * 8 : OOB, 0);
      if (it == 0) STAMP(7);
      if (it < 0) continue;
      lds_wait_ge<4>(lds, L_FULL, it ? 104u : C::T_GATHER);
      const int go = (C::DBG & (64 | 256)) ? OOB : it == 0 ? (lane < 32 ? W_QG + (b * 2048 + h * 64 + 2 * lane) * 8 : OOB) : W_QG + ((8 + b) * 2048 + xcd * 256 + 2 * lane) * 8;
      const u64 need = (C::DBG & (64 | 256)) ? 0ull : it == 0 ? 0xFFFFFFFFull : ~0ull;
#pragma unroll 1
      for (unsigned spn = 0; spn < SPIN_CAP; ++spn) {
        const v4u v = ld_sc1(rs, go, 0), u = ld_sc1(rs, it == 1 ? go : OOB, 1024);
        if (it == 1 || lane < 32) *LDSP(v2u, L_QIMG + (it == 1 ? 256 : 0) + 8 * lane) = v2u{v[0], v[2]};
        if (it == 1) *LDSP(v2u, L_QIMG + 768 + 8 * lane) = v2u{u[0], u[2]};
        if ((__builtin_amdgcn_ballot_w64(v[1] != tag || v[3] != tag || (it == 1 && (u[1] != tag || u[3] != tag))) & need) == 0) break;
        __builtin_amdgcn_s_sleep(24);
      }
      CBAR();
      lds_st(lds, L_QRDY + 4 * it, 1u);
      if (it == 0) STAMP(8);
    }
    lds_st(lds, L_TAG, tag);
    lds_wait_ge<16>(lds, L_BCNT, 4u);
    {
      float Mk = 0.f, Lk = 0.f;
#pragma unroll
      for (int hp = 0; hp < 2; ++hp) {
        const int hh = 2 * hp + (lane >> 5);
        f32x2 ml[4]; f32x4 ac[4];
#pragma unroll
        for (int u = 0; u < 4; ++u) { ml[u] = *LDSP(f32x2, L_ML + 128 + u * 32 + hh * 8); ac[u] = *LDSP(f32x4, L_S + u * 4096 + hh * 512 + 16 * (lane & 31)); }
        const float M = fmaxf(fmaxf(ml[0][0], ml[1][0]), fmaxf(ml[2][0], ml[3][0]));
        float L = 0.f; f32x4 o = {0, 0, 0, 0};
#pragma unroll
        for (int u = 0; u < 4; ++u) { const float f = ex2((ml[u][0] - M) * SC2); L = fmaf(f, ml[u][1], L); o += f * ac[u]; }
        st_sc1(__builtin_bit_cast(v4u, o), rs, W_PO + (xcd + 8 * (4 * b + hh)) * 2048 + sp * 512 + 16 * (lane & 31), 0);
        if ((lane & 31) == hp) { Mk = M; Lk = L; }
      }
      asm volatile("s_waitcnt vmcnt(0)" ::: "memory");
      const int hh = 2 * (lane & 31) + (lane >> 5);
      st_sc1(v4u{__float_as_uint(Mk), tag, __float_as_uint(Lk), tag}, rs, (lane & 31) < 2 ? W_ML + (xcd + 8 * (4 * b + hh)) * 128 + sp * 16 : OOB, 0);
    }
    lds_wait_ge<16>(lds, L_FULL, 192u + C::T_MLPOLL);
    v4u mlv = {0, 0, 0, 0};
#pragma unroll 1
    for (unsigned spn = 0; spn < SPIN_CAP; ++spn) {
      mlv = ld_sc1(rs, lane < 4 ? W_ML + wg * 128 + lane * 16 : OOB, 0);
      if ((__builtin_amdgcn_ballot_w64(mlv[1] != tag || mlv[3] != tag) & 15u) == 0u) break;
      __builtin_amdgcn_s_sleep(64);
    }
    __builtin_amdgcn_fence(__ATOMIC_ACQUIRE, "wavefront");
    {
      const v4u p0 = ld_sc1(rs, W_PO + wg * 2048 + 16 * lane, 0), p1 = ld_sc1(rs, W_PO + wg * 2048 + 16 * lane, 1024);
      const float m = __uint_as_float(mlv[0]);
      float M = fmaxf(m, dpp<XOR1>(m)); M = fmaxf(M, dpp<XOR2>(M));
      const float e = ex2((m - M) * SC2);
      float L = e * __uint_as_float(mlv[2]); L += dpp<XOR1>(L); L += dpp<XOR2>(L);
      const int fi = __builtin_bit_cast(int, e / L);
      const float f0 = __builtin_bit_cast(float, lane < 32 ? __builtin_amdgcn_readlane(fi, 0) : __builtin_amdgcn_readlane(fi, 1));
      const float f1 = __builtin_bit_cast(float, lane < 32 ? __builtin_amdgcn_readlane(fi, 2) : __builtin_amdgcn_readlane(fi, 3));
      *LDSP(f32x4, L_HREC + 16 * lane) = f0 * __builtin_bit_cast(f32x4, p0);
      *LDSP(f32x4, L_HREC + 1024 + 16 * lane) = f1 * __builtin_bit_cast(f32x4, p1);
      CBAR(); lds_st(lds, L_RCNT, 4u);
    }
    STAMP(9);
    if (C::DBG & 16) return;
    u64 tl = 0;
#pragma unroll 1
    for (int pass = 0; pass < 2; ++pass) {
      if (pass == 0) lds_wait_ge<8>(lds, L_FULL, NSL - 1); else lds_wait_ge<1>(lds, L_YPUB, 8u);
      if (pass == 0) tl = __builtin_amdgcn_s_memrealtime() - tstart;
      const int yo = W_YG + (pass == 0 ? YHALF : 0) + (b * 32 * 512 + (lane >> 3) * 512 + 16 * h + 2 * (lane & 7)) * 8;
      v4u y[4]; bool ok[4];
#pragma unroll
      for (int i = 0; i < 4; ++i) { y[i] = v4u{0, 0, 0, 0}; ok[i] = false; }
#pragma unroll 1
      for (unsigned spn = 0; spn < SPIN_CAP; ++spn) {
        v4u z[4];
#pragma unroll
        for (int i = 0; i < 4; ++i) z[i] = ld_sc1(rs, ok[i] ? OOB : yo, i * 8 * 512 * 8);
        bool all = true;
#pragma unroll
        for (int i = 0; i < 4; ++i) { if (!ok[i]) { y[i] = z[i]; ok[i] = z[i][1] == tag && z[i][3] == tag; } all &= ok[i]; }
        if (__builtin_amdgcn_ballot_w64(!all) == 0) break;
        __builtin_amdgcn_s_sleep(3);
      }
      float s0 = (__uint_as_float(y[0][0]) + __uint_as_float(y[1][0])) + (__uint_as_float(y[2][0]) + __uint_as_float(y[3][0]));
      float s1 = (__uint_as_float(y[0][2]) + __uint_as_float(y[1][2])) + (__uint_as_float(y[2][2]) + __uint_as_float(y[3][2]));
      s0 += dpp<ROR8>(s0); s1 += dpp<ROR8>(s1);
      s0 = x32_sum(x16_sum(s0)); s1 = x32_sum(x16_sum(s1));
      if (lane < 8) *(GAS unsigned*)(out + b * 1024 + (pass == 0 ? 512 : 0) + 16 * h + 2 * lane) = cvtpk(s0, s1);
      STAMP(10 + pass);
    }
    if (wg == 0 && lane == 0) __hip_atomic_store((GAS unsigned*)(ws + W_EPOCH), tag, RLX_AGENT);
    if (wg == 77) while (__builtin_amdgcn_s_memrealtime() < tstart + 5000 + tl) __builtin_amdgcn_s_sleep(1);
    STAMP_OUT(6, 12);
  }
}

template <class C>
static void launch_cfg(void* const* d_in, void* d_out, void* d_ws, hipStream_t stream) {
  (void)hipFuncSetAttribute((const void*)fused_decode<C>, hipFuncAttributeMaxDynamicSharedMemorySize, LDS_BYTES);
  hipLaunchKernelGGL(fused_decode<C>, dim3(256), dim3(NTHR), LDS_BYTES, stream,
                     (const u16*)d_in[0], (const u16*)d_in[1], (const u16*)d_in[2], (const u16*)d_in[3], (const u16*)d_in[4],
                     (const u16*)d_in[5], (const u16*)d_in[6], (const u16*)d_in[7], (const u16*)d_in[8], (const u16*)d_in[9],
                     (u16*)d_out, (unsigned char*)d_ws);
}
extern "C" void kernel_launch(void* const* d_in, const int* in_sizes, int n_in, void* d_out, int out_size, void* d_ws, size_t ws_size, hipStream_t stream) {
  (void)in_sizes; (void)n_in; (void)out_size; (void)ws_size;
  launch_cfg<CfgDefault>(d_in, d_out, d_ws, stream);
}
```

```cpp
#include <hip/hip_runtime.h>
#include <hip/hip_bf16.h>
#include <stdint.h>

typedef unsigned v4u __attribute__((ext_vector_type(4)));
typedef unsigned v2u __attribute__((ext_vector_type(2)));
typedef int v4i __attribute__((ext_vector_type(4)));
typedef float f32x4 __attribute__((ext_vector_type(4)));
typedef float f32x2 __attribute__((ext_vector_type(2)));
typedef short s16x4 __attribute__((ext_vector_type(4)));
typedef __bf16 bf16x8 __attribute__((ext_vector_type(8)));
typedef __bf16 bf16x2 __attribute__((ext_vector_type(2)));
typedef unsigned short u16;
typedef unsigned long long u64;
#define GAS __attribute__((address_space(1)))
#define LAS __attribute__((address_space(3)))
#define RLX_AGENT __ATOMIC_RELAXED, __HIP_MEMORY_SCOPE_AGENT

struct CfgDefault {
  static constexpr int NFL = 20;
  static constexpr int MUL = 5;
  static constexpr int WO_AUX = 0;
  static constexpr int WO_SLEEP = 12;
  static constexpr int PF_AUX = 0;
  static constexpr int T_SLICE = 3, T_GATHER = 16, T_MLPOLL = 40;
  static constexpr int WQA_SLEEP = 0, WQB_SLEEP = 12;
  static constexpr int PARK = 4;
  static constexpr int DBG = 0;
};

constexpr int SK = 4096, DH = 128, NH = 32, HKV = 8;
constexpr float SC2 = 0.088388347648318440f * 1.4426950408889634f;
constexpr int NWAVE = 10, NTHR = NWAVE * 64;
constexpr int NSL = 320;
constexpr int RING = 15, SLOT = 8192;
constexpr int L_FULL = 0, L_QRDY = 4  , L_BCNT = 12, L_DONE = 16, L_RCNT = 32  , L_TAG = 40, L_YPUB = 44, L_MLC = 48;
constexpr int L_QIMG = 64;
constexpr int L_OIMG = L_QIMG + 1280;
constexpr int L_ML = L_OIMG + 1024;
constexpr int L_HREC = L_ML + 256;
constexpr int L_S = 8192;
constexpr int L_P = L_S + 16384;
constexpr int L_RING = L_P + 16384;
constexpr int LDS_BYTES = 160 * 1024;
static_assert(L_HREC % 16 == 0 && L_HREC + 8 * 512 <= 7168 && L_RING + RING * SLOT <= LDS_BYTES, "lds map");
constexpr int W_EPOCH = 0;
constexpr int W_ML = 4096;
constexpr int W_QG = 65536;
constexpr int W_PO = W_QG + 2 * 8 * 2048 * 8;
constexpr int W_YG = W_PO + 256 * 2048;
constexpr int YHALF = 8 * 32 * 512 * 8;
constexpr int W_END = W_YG + 2 * YHALF;
constexpr size_t W_STAMP = 64u << 20;
constexpr unsigned SPIN_CAP = 1u << 21;
constexpr int OOB = 0x7FFFFF00;

__device__ __forceinline__ unsigned cvtpk(float lo, float hi) { return __builtin_bit_cast(unsigned, __builtin_convertvector(f32x2{lo, hi}, bf16x2)); }
__device__ __forceinline__ __amdgpu_buffer_rsrc_t mk(const void* p, unsigned bytes) { return __builtin_amdgcn_make_buffer_rsrc((void*)p, 0, bytes, 0x00020000); }
__device__ __forceinline__ v4i mksrd(const void* p, unsigned bytes) { return v4i{(int)(uintptr_t)p, (int)((uintptr_t)p >> 32), (int)bytes, 0x00020000}; }
__device__ __forceinline__ f32x4 mfma32(v4u a, v4u b, f32x4 c) { return __builtin_amdgcn_mfma_f32_16x16x32_bf16(__builtin_bit_cast(bf16x8, a), __builtin_bit_cast(bf16x8, b), c, 0, 0, 0); }
__device__ __forceinline__ f32x4 mfma16(v2u a, v2u b, f32x4 c) { return __builtin_amdgcn_mfma_f32_16x16x16bf16_1k(__builtin_bit_cast(s16x4, a), __builtin_bit_cast(s16x4, b), c, 0, 0, 0); }
__device__ __forceinline__ float ex2(float x) { return __builtin_amdgcn_exp2f(x); }
__device__ __forceinline__ v4u ld_sc1(__amdgpu_buffer_rsrc_t r, int vo, int so) { return __builtin_amdgcn_raw_buffer_load_b128(r, vo, so, 16); }
__device__ __forceinline__ void st_sc1(v4u v, __amdgpu_buffer_rsrc_t r, int vo, int so) { __builtin_amdgcn_raw_buffer_store_b128(v, r, vo, so, 16); }
template <int CTRL> __device__ __forceinline__ float dpp(float x) { return __builtin_bit_cast(float, __builtin_amdgcn_mov_dpp(__builtin_bit_cast(int, x), CTRL, 0xf, 0xf, true)); }
constexpr int XOR1 = 0xB1, XOR2 = 0x4E, HMIR = 0x141, ROR8 = 0x128;
__device__ __forceinline__ float x16_sum(float x) { auto s = __builtin_amdgcn_permlane16_swap(__float_as_uint(x), __float_as_uint(x), false, false); return __uint_as_float(s[0]) + __uint_as_float(s[1]); }
__device__ __forceinline__ float x32_sum(float x) { auto s = __builtin_amdgcn_permlane32_swap(__float_as_uint(x), __float_as_uint(x), false, false); return __uint_as_float(s[0]) + __uint_as_float(s[1]); }
__device__ __forceinline__ float xg_max(float x) {
  auto s = __builtin_amdgcn_permlane16_swap(__float_as_uint(x), __float_as_uint(x), false, false);
  x = fmaxf(__uint_as_float(s[0]), __uint_as_float(s[1]));
  auto t = __builtin_amdgcn_permlane32_swap(__float_as_uint(x), __float_as_uint(x), false, false);
  return fmaxf(__uint_as_float(t[0]), __uint_as_float(t[1]));
}
#define CBAR() asm volatile("" ::: "memory")
#define OPAQUE_S(x) asm volatile("" : "+s"(x))
#define LDSP(T, off) ((LAS T*)(lds + (off)))
__device__ __forceinline__ unsigned lds_ld(LAS unsigned char* lds, int off) { return __builtin_amdgcn_readfirstlane(*(volatile LAS unsigned*)(lds + off)); }
__device__ __forceinline__ void lds_st(LAS unsigned char* lds, int off, unsigned v) { *(volatile LAS unsigned*)(lds + off) = v; }
template <int SLP> __device__ __forceinline__ unsigned lds_wait_ge(LAS unsigned char* lds, int off, unsigned want) {
  unsigned v = lds_ld(lds, off);
#pragma unroll 1
  for (unsigned sp = 0; v < want && sp < SPIN_CAP * 8; ++sp) { __builtin_amdgcn_s_sleep(SLP); v = lds_ld(lds, off); }
  CBAR();
  return v;
}
__device__ __forceinline__ void lds_inc(LAS unsigned char* lds, int off, int lane) {
  CBAR();
  if (lane == 0) __hip_atomic_fetch_add(LDSP(unsigned, off), 1u, __ATOMIC_RELAXED, __HIP_MEMORY_SCOPE_WORKGROUP);
  CBAR();
}
#define STAMP(i) do {} while (0)
#define STAMP_OUT(lo, hi) do {} while (0)

template <int NFL> __device__ __forceinline__ void dma4(unsigned m0v, unsigned v0, unsigned v1, unsigned v2, unsigned v3, v4i srd, unsigned so) {
  asm volatile("s_mov_b32 m0, %0\n\t"
               "s_waitcnt vmcnt(%7)\n\tbuffer_load_dwordx4 %1, %5, %6 offen nt lds\n\t"
               "s_waitcnt vmcnt(%7)\n\tbuffer_load_dwordx4 %2, %5, %6 offen offset:1024 nt lds\n\t"
               "s_waitcnt vmcnt(%7)\n\tbuffer_load_dwordx4 %3, %5, %6 offen offset:2048 nt lds\n\t"
               "s_waitcnt vmcnt(%7)\n\tbuffer_load_dwordx4 %4, %5, %6 offen offset:3072 nt lds"
               :: "s"(m0v), "v"(v0), "v"(v1), "v"(v2), "v"(v3), "s"(srd), "s"(so), "i"(NFL - 1) : "memory");
}

template <class C>
__global__ void __launch_bounds__(NTHR, 1) fused_decode(const u16* xa, const u16* ak, const u16* av, const u16* wqa, const u16* woa,
                                                        const u16* xb, const u16* bk, const u16* bv, const u16* wqb, const u16* wob,
                                                        u16* out, unsigned char* ws) {
  u64 tstart; asm volatile("s_memrealtime %0" : "=s"(tstart) :: "memory");
  constexpr int NFL = C::NFL, PARK = C::PARK;
  extern __shared__ __attribute__((aligned(16))) unsigned char lds_raw[];
  LAS unsigned char* lds = (LAS unsigned char*)lds_raw;
  const int wg = blockIdx.x, lane = threadIdx.x & 63, wave = __builtin_amdgcn_readfirstlane(threadIdx.x >> 6);
  const int xcd = wg & 7, jw = wg >> 3, b = jw >> 2, sp = jw & 3, h = 4 * xcd + sp;
  const int g = lane >> 4, n = lane & 15;
  const unsigned lds0 = (unsigned)(uintptr_t)lds;
  if (threadIdx.x < 16) *(volatile LAS unsigned*)(lds + 4 * lane) = 0u;
  asm volatile("s_waitcnt lgkmcnt(0)" :: "s"(wqb), "s"(wob), "s"(out), "s"(ws), "s"(xa) : "memory");
  __builtin_amdgcn_s_barrier();
  CBAR();

  if (wave == 0) {
    __builtin_amdgcn_s_setprio(3);
    const size_t ua = (size_t)(b * NH + h) * SK * DH, ub = (size_t)(b * HKV + xcd) * SK * DH;
    const unsigned k0 = g * 256 + ((n ^ g) * 16), vv = lane * 16;
    unsigned slot = lds0 + L_RING;
    int limit = RING;
    constexpr int LAG0 = (NFL + 4 + 7) / 8, LAG1 = (NFL + 7) / 8;
    static_assert(NFL >= 17 && NFL <= 32, "publish lag");
    STAMP(0);
    int t = 0;
#pragma unroll 1
    for (int ph = 0; ph < 4; ++ph) {
      const v4i rs = mksrd(ph == 0 ? ak + ua : ph == 1 ? bk + ub : ph == 2 ? bv + ub : av + ua, SK * 256);
      const int tend = ph == 0 ? 128 : ph == 1 ? 160 : ph == 2 ? 192 : NSL;
      const unsigned v0 = ph < 2 ? k0 : vv, v1 = ph < 2 ? k0 ^ 64 : vv, v2 = ph < 2 ? k0 ^ 128 : vv, v3 = ph < 2 ? k0 ^ 192 : vv;
      unsigned r = (ph == 1 || ph == 2) ? (sp * 32 * C::MUL) & 127 : 0u;
#pragma unroll 1
      for (; t < tend; ++t) {
        if (!(C::DBG & 4) && t >= limit) {
#pragma unroll 1
          for (unsigned spn = 0; spn < SPIN_CAP * 8; ++spn) {
            const v4u d = *(volatile LAS v4u*)(lds + L_DONE);
            const int c = min(min(4 * (int)d[0], 4 * (int)d[1] + 1), min(4 * (int)d[2] + 2, 4 * (int)d[3] + 3));
            limit = __builtin_amdgcn_readfirstlane(c) + RING;
            if (t < limit) break;
            __builtin_amdgcn_s_sleep(1);
          }
        }
        const unsigned so = (((r & 0x70u) << 1) | (r & 15u)) << 12;
        dma4<NFL>(slot, v0, v1, v2, v3, rs, so);
        if (LAG0 == LAG1 && t >= LAG0) lds_st(lds, L_FULL, (unsigned)(t - LAG0 + 1));
        dma4<NFL>(slot + 4096, v0, v1, v2, v3, rs, so + 65536u);
        if (LAG0 != LAG1 && t >= LAG1) lds_st(lds, L_FULL, (unsigned)(t - LAG1 + 1));
        slot += SLOT; if (slot >= lds0 + L_RING + RING * SLOT) slot = lds0 + L_RING;
        r = (r + C::MUL) & 127u;
      }
    }
    asm volatile("s_waitcnt vmcnt(24)" ::: "memory"); lds_st(lds, L_FULL, NSL - 3);
    asm volatile("s_waitcnt vmcnt(16)" ::: "memory"); lds_st(lds, L_FULL, NSL - 2);
    asm volatile("s_waitcnt vmcnt(8)" ::: "memory");  lds_st(lds, L_FULL, NSL - 1);
    asm volatile("s_waitcnt vmcnt(0)" ::: "memory");  lds_st(lds, L_FULL, NSL);
    STAMP(1); STAMP_OUT(0, 2); STAMP_OUT(16, 26);
  } else if (wave >= 2 && wave < 6) {
    if (C::DBG & 8) return;
    const int w = wave - 2, jj = n >> 2;
    int slot = L_RING + w * SLOT; unsigned full = 0, cnt = 0; int t = w;
#define SLOT_WAIT() do { if (full <= (unsigned)t) full = lds_wait_ge<1>(lds, L_FULL, (unsigned)t + 1u); CBAR(); } while (0)
#define SLOT_DONE() do { CBAR(); ++cnt; lds_st(lds, L_DONE + 4 * w, cnt);     \
        CBAR(); slot += 4 * SLOT; if (slot >= L_RING + RING * SLOT) slot -= RING * SLOT; t += 4; } while (0)
#define K_READ(T_) do { SLOT_WAIT(); _Pragma("unroll") for (int m_ = 0; m_ < 8; ++m_) T_[m_] = *LDSP(v4u, ((slot + kb) ^ (64 * (m_ & 3))) + (m_ >> 2) * 4096); SLOT_DONE(); } while (0)
    const int sw = L_S + w * 4096;
    {
      const int kb = n * 256 + ((g ^ n) * 16);
      v4u q[4];
      float mx = -3.0e38f;
      const int sa0 = sw + ((n & 3) * 8 + g * 2 + (jj & 1)) * 16;
      int sa = sa0;
#define K_MATH(T_, act_, step_) do { f32x4 s0 = {0.f, 0.f, 0.f, 0.f}, s1 = {0.f, 0.f, 0.f, 0.f}; \
        _Pragma("unroll") for (int m = 0; m < 4; ++m) { s0 = mfma32(T_[m], q[m], s0); s1 = mfma32(T_[4 + m], q[m], s1); }       \
        mx = fmaxf(fmaxf(fmaxf(fmaxf(s0[0], s0[1]), fmaxf(s0[2], s0[3])), fmaxf(fmaxf(s1[0], s1[1]), fmaxf(s1[2], s1[3]))), mx); \
        if (act_) *LDSP(f32x4, sa) = (jj & 1) ? s1 : s0; \
        sa += (step_); } while (0)
      {
        v4u P[PARK][8];
#pragma unroll
        for (int i = 0; i < PARK; ++i) K_READ(P[i]);
        lds_wait_ge<1>(lds, L_QRDY, 1u);
#pragma unroll
        for (int m = 0; m < 4; ++m) q[m] = *LDSP(v4u, L_QIMG + g * 16 + 64 * m);
        if (w == 0) STAMP(2);
#pragma unroll
        for (int i = 0; i < PARK; ++i) K_MATH(P[i], (n & 11) == 0, 128);
      }
#pragma unroll 1
      for (int ph = 0; ph < 2; ++ph) {
        const bool act = ph == 1 ? n < 8 : (n & 11) == 0;
        const int sstep = ph == 1 ? 512 : 128, tend = ph == 0 ? 128 : 160;
#pragma unroll 1
        while (t < tend) { v4u T[8]; K_READ(T); K_MATH(T, act, sstep); }
        {
          lds_wait_ge<1>(lds, L_QRDY + 4, 1u);
          const float Mh = xg_max(mx);
          const float M = __builtin_bit_cast(float, __builtin_amdgcn_ds_bpermute(((lane >> 3) & 3) * 4, __builtin_bit_cast(int, Mh)));
          const float mc = -M * SC2;
          float l = 0.f;
          CBAR();
#pragma unroll
          for (int j = 0; j < 4; ++j) {
            const f32x4 s = *LDSP(f32x4, sw + 16 * lane + 1024 * j);
            const float p0 = ex2(fmaf(s[0], SC2, mc)), p1 = ex2(fmaf(s[1], SC2, mc)), p2 = ex2(fmaf(s[2], SC2, mc)), p3 = ex2(fmaf(s[3], SC2, mc));
            l += (p0 + p1) + (p2 + p3);
            *LDSP(v2u, L_P + ph * 8192 + w * 2048 + 8 * lane + 512 * j) = v2u{cvtpk(p0, p1), cvtpk(p2, p3)};
          }
          l += dpp<XOR1>(l); l += dpp<XOR2>(l); l += dpp<HMIR>(l); l = x32_sum(l);
          const float la = x16_sum(l + dpp<ROR8>(l));
          if ((lane & 39) == 0) *LDSP(f32x2, L_ML + ph * 128 + w * 32 + lane) = f32x2{M, ph == 1 ? l : la};
          lds_inc(lds, L_MLC, lane);
        }
        mx = -3.0e38f; sa = sa0;
#pragma unroll
        for (int m = 0; m < 4; ++m) q[m] = *LDSP(v4u, L_QIMG + 256 + (n & 3) * 256 + g * 16 + 64 * m);
      }
    }
    {
      const unsigned Z = 0x0C0C0C0Cu;
      const unsigned el0 = jj == 0 ? 0x0C0C0100u : jj == 1 ? 0x01000C0Cu : Z, el1 = jj == 2 ? 0x0C0C0100u : jj == 3 ? 0x01000C0Cu : Z;
      const unsigned eh0 = jj == 0 ? 0x0C0C0302u : jj == 1 ? 0x03020C0Cu : Z, eh1 = jj == 2 ? 0x0C0C0302u : jj == 3 ? 0x03020C0Cu : Z;
      const int vb = g * 1024 + n * 16;
#pragma unroll 1
      for (int ph = 1; ph >= 0; --ph) {
        float f = 1.f;
        if (ph == 0) {
          lds_wait_ge<1>(lds, L_MLC, 8u);
          f32x2 ml[4];
#pragma unroll
          for (int u = 0; u < 4; ++u) ml[u] = *LDSP(f32x2, L_ML + u * 32);
          const float M = fmaxf(fmaxf(ml[0][0], ml[1][0]), fmaxf(ml[2][0], ml[3][0]));
          float L = 0.f;
#pragma unroll
          for (int u = 0; u < 4; ++u) L = fmaf(ex2((ml[u][0] - M) * SC2), ml[u][1], L);
          f = ex2(((*LDSP(f32x2, L_ML + w * 32))[0] - M) * SC2) / L;
        }
        const int tend = ph == 1 ? 192 : NSL, pstep = ph == 1 ? 256 : 64;
        int pa = L_P + ph * 8192 + w * 2048 + (ph == 1 ? (n & 3) * 64 : 0) + g * 16;
        f32x4 a0 = {0, 0, 0, 0}, a1 = {0, 0, 0, 0};
#pragma unroll 1
        while (t < tend) {
          const v4u pp = *LDSP(v4u, pa); pa += pstep;
          v2u bf[8];
#pragma unroll
          for (int i = 0; i < 8; ++i) { const unsigned pk = pp[i >> 1]; bf[i] = v2u{__builtin_amdgcn_perm(pk, pk, (i & 1) ? eh0 : el0), __builtin_amdgcn_perm(pk, pk, (i & 1) ? eh1 : el1)}; }
#pragma unroll
          for (int i = 0; i < 8; ++i) asm volatile("" : "+v"(bf[i]));
          SLOT_WAIT();
          v4u T[8];
#pragma unroll
          for (int i = 0; i < 8; ++i) T[i] = *LDSP(v4u, slot + vb + (i >> 2) * 4096 + (i & 3) * 256);
          SLOT_DONE();
#pragma unroll
          for (int i = 0; i < 8; ++i) { a0 = mfma16(v2u{T[i][0], T[i][1]}, bf[i], a0); a1 = mfma16(v2u{T[i][2], T[i][3]}, bf[i], a1); }
        }
        a0 *= f; a1 *= f;
        const int ro = (ph == 1 ? sw + (n & 3) * 512 : L_HREC + 2048 + w * 512) + jj * 128 + g * 32;
        if (ph == 1 || (n & 3) == 0) {
          *LDSP(f32x4, ro) = f32x4{a0[0], a1[0], a0[1], a1[1]};
          *LDSP(f32x4, ro + 16) = f32x4{a0[2], a1[2], a0[3], a1[3]};
        }
        lds_inc(lds, ph == 1 ? L_BCNT : L_RCNT + 4, lane);
      }
    }
    if (w == 3) { STAMP(3); STAMP_OUT(2, 4); }
  } else if (wave >= 6) {
    if (C::DBG & 8) return;
    const int w = wave - 6;
    if (C::PF_AUX >= 0) {
#pragma unroll 1
      for (int mat = 0; mat < 2; ++mat) {
        if (mat) lds_wait_ge<16>(lds, L_FULL, 32u);
        const __amdgpu_buffer_rsrc_t rw = mk((mat ? wqb : wqa) + (size_t)(16 * jw + w) * 4096 + xcd * 512, 13 * 8192);
        v4u d[4];
#pragma unroll
        for (int i = 0; i < 4; ++i) d[i] = __builtin_amdgcn_raw_buffer_load_b128(rw, lane * 16, ((i & 1) * 8 + (i >> 1) * 4) * 8192, C::PF_AUX < 0 ? 0 : C::PF_AUX);
#pragma unroll
        for (int i = 0; i < 4; ++i) asm volatile("" :: "v"(d[i]));

      }
    }
    if (C::DBG & 16) return;
    const __amdgpu_buffer_rsrc_t ry = mk(ws + W_YG, 2 * YHALF);
    const int yo = ((b * 32 + h) * 512 + 128 * w + 8 * n + 4 * (g & 1) + 2 * (g >> 1)) * 8;
    unsigned sl[4], sh[4];
#pragma unroll
    for (int d = 0; d < 4; ++d) { const bool on = ((n & 7) >> 1) == d; sl[d] = !on ? 0x0C0C0C0Cu : (n & 1) ? 0x01000C0Cu : 0x0C0C0100u; sh[d] = !on ? 0x0C0C0C0Cu : (n & 1) ? 0x03020C0Cu : 0x0C0C0302u; }
    v4u yb = {0, 0, 0, 0};
    lds_wait_ge<16>(lds, L_FULL, 72u);
#pragma unroll 1
    for (int pass = 0; pass < 2; ++pass) {
      const __amdgpu_buffer_rsrc_t rw = mk((pass == 0 ? wob : woa) + (size_t)h * 128 * 512, (C::DBG & 1) ? 0 : 128 * 1024);
      v4u wo[32];
#pragma unroll
      for (int i = 0; i < 32; ++i) {
        wo[i] = __builtin_amdgcn_raw_buffer_load_b128(rw, g * 1024 + w * 256 + n * 16, i * 4096, C::WO_AUX);
        __builtin_amdgcn_sched_barrier(0);
        __builtin_amdgcn_s_sleep(C::WO_SLEEP);
        __builtin_amdgcn_sched_barrier(0);
      }
      if (pass == 1) { lds_wait_ge<8>(lds, L_FULL, NSL - 8); st_sc1(yb, ry, yo, YHALF); }
      asm volatile("s_waitcnt vmcnt(1)" ::: "memory");
      lds_wait_ge<1>(lds, L_RCNT + 4 * pass, 4u);
      const unsigned tag = *LDSP(unsigned, L_TAG);
      const int rb = L_HREC + pass * 2048 + 8 * lane;
      const f32x2 o = (*LDSP(f32x2, rb) + *LDSP(f32x2, rb + 512)) + (*LDSP(f32x2, rb + 1024) + *LDSP(f32x2, rb + 1536));
      const int oi = L_OIMG + w * 256;
      *LDSP(unsigned, oi + 4 * lane) = cvtpk(o[0], o[1]);
      CBAR();
      v4u of[4];
#pragma unroll
      for (int u = 0; u < 4; ++u) of[u] = *LDSP(v4u, oi + 64 * g + 16 * u);
      f32x4 y0 = {0, 0, 0, 0}, y1 = {0, 0, 0, 0};
#pragma unroll
      for (int i = 0; i < 32; i += 2) {
        const unsigned pk = of[i >> 3][(i >> 1) & 3];
        v4u a0, a1;
#pragma unroll
        for (int d = 0; d < 4; ++d) { a0[d] = __builtin_amdgcn_perm(pk, pk, sl[d]); a1[d] = __builtin_amdgcn_perm(pk, pk, sh[d]); }
        y0 = mfma32(a0, wo[i], y0); y1 = mfma32(a1, wo[i + 1], y1);
      }
      y0 += y1;
      yb = v4u{__float_as_uint(g < 2 ? y0[0] : y0[2]), tag, __float_as_uint(g < 2 ? y0[1] : y0[3]), tag};
      st_sc1(yb, ry, pass == 0 ? OOB : yo, 0);
      lds_inc(lds, L_YPUB, lane);
      if (w == 0) STAMP(4 + pass);
    }
    if (w == 0) STAMP_OUT(4, 6);
  } else {
    if (C::DBG & 8) return;
    unsigned G;
    asm volatile("global_load_dword %0, %1, %2 sc1" : "=v"(G) : "v"(W_EPOCH), "s"(ws) : "memory");
    const int cb = xcd * 32 + jw;
    const __amdgpu_buffer_rsrc_t rs = mk(ws, W_END);
    unsigned tag = 0;
    const int ta = L_P + (8 * g + (n >> 2)) * 32 + (n & 3) * 8;
    int it = -1; OPAQUE_S(it);
    v4u xf[16];
#pragma unroll 1
    for (; it < 2; ++it) {
      if (it != 0) {
        if (it == 1) lds_wait_ge<16>(lds, L_FULL, 52u);
        const v4i rx = mksrd(it == 1 ? xb : xa, (C::DBG & 128) ? 0 : 8 * 512 * 2);
#pragma unroll
        for (int s = 0; s < 16; ++s) asm volatile("buffer_load_dwordx4 %0, %1, %2, 0 offen offset:%3" : "+v"(xf[s]) : "v"(n * 1024 + 16 * g), "s"(rx), "i"(64 * s) : "memory");
      }
      if (it >= 0) {
        if (it == 0) lds_wait_ge<1>(lds, L_FULL, C::T_SLICE);
        const v4i rw = mksrd((it ? wqb : wqa) + cb * 16, (C::DBG & 32) ? 0 : 512 * 8192 - cb * 32);
        const unsigned vo = (lane >> 1) * 8192 + (lane & 1) * 16;
#pragma unroll
        for (int i = 0; i < 16; ++i) {
          asm volatile("s_mov_b32 m0, %0\n\ts_nop 0\n\tbuffer_load_dwordx4 %1, %2, %3 offen nt lds" :: "s"(lds0 + L_P + i * 1024), "v"(vo), "s"(rw), "s"(i * 32 * 8192) : "memory");
          if (it) __builtin_amdgcn_s_sleep(C::WQB_SLEEP); else if (C::WQA_SLEEP) __builtin_amdgcn_s_sleep(C::WQA_SLEEP);
        }
        asm volatile("s_waitcnt vmcnt(0)\n\tv_readfirstlane_b32 %0, %1\n\ts_nop 1" : "=s"(tag) : "v"(G) : "memory");
        tag += 1u;
        if (it == 0) STAMP(6);
      }
#pragma unroll
      for (int s = 0; s < 16; ++s) asm volatile("" : "+v"(xf[s]));
      f32x4 d0 = {0, 0, 0, 0}, d1 = {0, 0, 0, 0};
      {
        v2u t[16][2];
#pragma unroll
        for (int s = 0; s < 16; ++s)
#pragma unroll
          for (int u = 0; u < 2; ++u) t[s][u] = __builtin_bit_cast(v2u, __builtin_amdgcn_ds_read_tr16_b64_v4i16(LDSP(s16x4, ta + s * 1024 + u * 128)));
        __builtin_amdgcn_sched_barrier(0);
#pragma unroll
        for (int s = 0; s < 16; s += 2) {
          d0 = mfma32(v4u{t[s][0][0], t[s][0][1], t[s][1][0], t[s][1][1]}, xf[s], d0);
          d1 = mfma32(v4u{t[s + 1][0][0], t[s + 1][0][1], t[s + 1][1][0], t[s + 1][1][1]}, xf[s + 1], d1);
        }
      }
      d0 += d1;
      st_sc1(v4u{cvtpk(d0[0], d0[1]), tag, cvtpk(d0[2], d0[3]), tag}, rs, (it >= 0 && n < 8 && !(C::DBG & 64)) ? W_QG + ((it * 8 + n) * 2048 + cb * 8 + 2 * g) * 8 : OOB, 0);
      if (it == 0) STAMP(7);
      if (it < 0) continue;
      lds_wait_ge<4>(lds, L_FULL, it ? 104u : C::T_GATHER);
      const int go = (C::DBG & (64 | 256)) ? OOB : it == 0 ? (lane < 32 ? W_QG + (b * 2048 + h * 64 + 2 * lane) * 8 : OOB) : W_QG + ((8 + b) * 2048 + xcd * 256 + 2 * lane) * 8;
      const u64 need = (C::DBG & (64 | 256)) ? 0ull : it == 0 ? 0xFFFFFFFFull : ~0ull;
#pragma unroll 1
      for (unsigned spn = 0; spn < SPIN_CAP; ++spn) {
        const v4u v = ld_sc1(rs, go, 0), u = ld_sc1(rs, it == 1 ? go : OOB, 1024);
        if (it == 1 || lane < 32) *LDSP(v2u, L_QIMG + (it == 1 ? 256 : 0) + 8 * lane) = v2u{v[0], v[2]};
        if (it == 1) *LDSP(v2u, L_QIMG + 768 + 8 * lane) = v2u{u[0], u[2]};
        if ((__builtin_amdgcn_ballot_w64(v[1] != tag || v[3] != tag || (it == 1 && (u[1] != tag || u[3] != tag))) & need) == 0) break;
        __builtin_amdgcn_s_sleep(24);
      }
      CBAR();
      lds_st(lds, L_QRDY + 4 * it, 1u);
      if (it == 0) STAMP(8);
    }
    lds_st(lds, L_TAG, tag);
    lds_wait_ge<16>(lds, L_BCNT, 4u);
    {
      float Mk = 0.f, Lk = 0.f;
#pragma unroll
      for (int hp = 0; hp < 2; ++hp) {
        const int hh = 2 * hp + (lane >> 5);
        f32x2 ml[4]; f32x4 ac[4];
#pragma unroll
        for (int u = 0; u < 4; ++u) { ml[u] = *LDSP(f32x2, L_ML + 128 + u * 32 + hh * 8); ac[u] = *LDSP(f32x4, L_S + u * 4096 + hh * 512 + 16 * (lane & 31)); }
        const float M = fmaxf(fmaxf(ml[0][0], ml[1][0]), fmaxf(ml[2][0], ml[3][0]));
        float L = 0.f; f32x4 o = {0, 0, 0, 0};
#pragma unroll
        for (int u = 0; u < 4; ++u) { const float f = ex2((ml[u][0] - M) * SC2); L = fmaf(f, ml[u][1], L); o += f * ac[u]; }
        st_sc1(__builtin_bit_cast(v4u, o), rs, W_PO + (xcd + 8 * (4 * b + hh)) * 2048 + sp * 512 + 16 * (lane & 31), 0);
        if ((lane & 31) == hp) { Mk = M; Lk = L; }
      }
      asm volatile("s_waitcnt vmcnt(0)" ::: "memory");
      const int hh = 2 * (lane & 31) + (lane >> 5);
      st_sc1(v4u{__float_as_uint(Mk), tag, __float_as_uint(Lk), tag}, rs, (lane & 31) < 2 ? W_ML + (xcd + 8 * (4 * b + hh)) * 128 + sp * 16 : OOB, 0);
    }
    lds_wait_ge<16>(lds, L_FULL, 192u + C::T_MLPOLL);
    v4u mlv = {0, 0, 0, 0};
#pragma unroll 1
    for (unsigned spn = 0; spn < SPIN_CAP; ++spn) {
      mlv = ld_sc1(rs, lane < 4 ? W_ML + wg * 128 + lane * 16 : OOB, 0);
      if ((__builtin_amdgcn_ballot_w64(mlv[1] != tag || mlv[3] != tag) & 15u) == 0u) break;
      __builtin_amdgcn_s_sleep(64);
    }
    __builtin_amdgcn_fence(__ATOMIC_ACQUIRE, "wavefront");
    {
      const v4u p0 = ld_sc1(rs, W_PO + wg * 2048 + 16 * lane, 0), p1 = ld_sc1(rs, W_PO + wg * 2048 + 16 * lane, 1024);
      const float m = __uint_as_float(mlv[0]);
      float M = fmaxf(m, dpp<XOR1>(m)); M = fmaxf(M, dpp<XOR2>(M));
      const float e = ex2((m - M) * SC2);
      float L = e * __uint_as_float(mlv[2]); L += dpp<XOR1>(L); L += dpp<XOR2>(L);
      const int fi = __builtin_bit_cast(int, e / L);
      const float f0 = __builtin_bit_cast(float, lane < 32 ? __builtin_amdgcn_readlane(fi, 0) : __builtin_amdgcn_readlane(fi, 1));
      const float f1 = __builtin_bit_cast(float, lane < 32 ? __builtin_amdgcn_readlane(fi, 2) : __builtin_amdgcn_readlane(fi, 3));
      *LDSP(f32x4, L_HREC + 16 * lane) = f0 * __builtin_bit_cast(f32x4, p0);
      *LDSP(f32x4, L_HREC + 1024 + 16 * lane) = f1 * __builtin_bit_cast(f32x4, p1);
      CBAR(); lds_st(lds, L_RCNT, 4u);
    }
    STAMP(9);
    if (C::DBG & 16) return;
#pragma unroll 1
    for (int pass = 0; pass < 2; ++pass) {
      if (pass == 0) lds_wait_ge<8>(lds, L_FULL, NSL - 1); else lds_wait_ge<1>(lds, L_YPUB, 8u);
      const int yo = W_YG + (pass == 0 ? YHALF : 0) + (b * 32 * 512 + (lane >> 3) * 512 + 16 * h + 2 * (lane & 7)) * 8;
      v4u y[4]; bool ok[4];
#pragma unroll
      for (int i = 0; i < 4; ++i) { y[i] = v4u{0, 0, 0, 0}; ok[i] = false; }
#pragma unroll 1
      for (unsigned spn = 0; spn < SPIN_CAP; ++spn) {
        v4u z[4];
#pragma unroll
        for (int i = 0; i < 4; ++i) z[i] = ld_sc1(rs, ok[i] ? OOB : yo, i * 8 * 512 * 8);
        bool all = true;
#pragma unroll
        for (int i = 0; i < 4; ++i) { if (!ok[i]) { y[i] = z[i]; ok[i] = z[i][1] == tag && z[i][3] == tag; } all &= ok[i]; }
        if (__builtin_amdgcn_ballot_w64(!all) == 0) break;
        __builtin_amdgcn_s_sleep(3);
      }
      float s0 = (__uint_as_float(y[0][0]) + __uint_as_float(y[1][0])) + (__uint_as_float(y[2][0]) + __uint_as_float(y[3][0]));
      float s1 = (__uint_as_float(y[0][2]) + __uint_as_float(y[1][2])) + (__uint_as_float(y[2][2]) + __uint_as_float(y[3][2]));
      s0 += dpp<ROR8>(s0); s1 += dpp<ROR8>(s1);
      s0 = x32_sum(x16_sum(s0)); s1 = x32_sum(x16_sum(s1));
      if (lane < 8) *(GAS unsigned*)(out + b * 1024 + (pass == 0 ? 512 : 0) + 16 * h + 2 * lane) = cvtpk(s0, s1);
      STAMP(10 + pass);
    }
    if (wg == 0 && lane == 0) __hip_atomic_store((GAS unsigned*)(ws + W_EPOCH), tag, RLX_AGENT);
    if (wg == 0) while (__builtin_amdgcn_s_memrealtime() < tstart + 15000) __builtin_amdgcn_s_sleep(1);
    STAMP_OUT(6, 12);
  }
}

template <class C>
static void launch_cfg(void* const* d_in, void* d_out, void* d_ws, hipStream_t stream) {
  (void)hipFuncSetAttribute((const void*)fused_decode<C>, hipFuncAttributeMaxDynamicSharedMemorySize, LDS_BYTES);
  hipLaunchKernelGGL(fused_decode<C>, dim3(256), dim3(NTHR), LDS_BYTES, stream,
                     (const u16*)d_in[0], (const u16*)d_in[1], (const u16*)d_in[2], (const u16*)d_in[3], (const u16*)d_in[4],
                     (const u16*)d_in[5], (const u16*)d_in[6], (const u16*)d_in[7], (const u16*)d_in[8], (const u16*)d_in[9],
                     (u16*)d_out, (unsigned char*)d_ws);
}
extern "C" void kernel_launch(void* const* d_in, const int* in_sizes, int n_in, void* d_out, int out_size, void* d_ws, size_t ws_size, hipStream_t stream) {
  (void)in_sizes; (void)n_in; (void)out_size; (void)ws_size;
  launch_cfg<CfgDefault>(d_in, d_out, d_ws, stream);
}
```

```cpp
#include <hip/hip_runtime.h>
#include <hip/hip_bf16.h>
#include <stdint.h>

typedef unsigned v4u __attribute__((ext_vector_type(4)));
typedef unsigned v2u __attribute__((ext_vector_type(2)));
typedef int v4i __attribute__((ext_vector_type(4)));
typedef float f32x4 __attribute__((ext_vector_type(4)));
typedef float f32x2 __attribute__((ext_vector_type(2)));
typedef short s16x4 __attribute__((ext_vector_type(4)));
typedef __bf16 bf16x8 __attribute__((ext_vector_type(8)));
typedef __bf16 bf16x2 __attribute__((ext_vector_type(2)));
typedef unsigned short u16;
typedef unsigned long long u64;
#define GAS __attribute__((address_space(1)))
#define LAS __attribute__((address_space(3)))
#define RLX_AGENT __ATOMIC_RELAXED, __HIP_MEMORY_SCOPE_AGENT

struct CfgDefault {
  static constexpr int NFL = 20;
  static constexpr int PACE = 2950, PACE2 = 2825;
  static constexpr int UP0 = 40, DN0 = 40;
  static constexpr int PACE0 = 20;
  static constexpr int STEP = 50;
  static constexpr int NMIN = 18;
  static constexpr int SL_NT = 1, WO_PF = 0, WO_PF_AUX = 0, WO_PF_GAP = 12;
  static constexpr int MUL = 5;
  static constexpr int WO_AUX = 0;
  static constexpr int WO_SLEEP = 12;
  static constexpr int PF_AUX = 0;
  static constexpr int T_SLICE = 3, T_GATHER = 16, T_MLPOLL = 24;
  static constexpr int WQA_SLEEP = 0, WQB_SLEEP = 12;
  static constexpr int PARK = 4;
  static constexpr int DBG = 0;
};

constexpr int SK = 4096, DH = 128, NH = 32, HKV = 8;
constexpr float SC2 = 0.088388347648318440f * 1.4426950408889634f;
constexpr int NWAVE = 10, NTHR = NWAVE * 64;
constexpr int NSL = 320;
constexpr int RING = 15, SLOT = 8192;
constexpr int L_FULL = 0, L_QRDY = 4  , L_BCNT = 12, L_DONE = 16, L_RCNT = 32  , L_TAG = 40, L_YPUB = 44, L_MLC = 48;
constexpr int L_QIMG = 64;
constexpr int L_OIMG = L_QIMG + 1280;
constexpr int L_ML = L_OIMG + 1024;
constexpr int L_HREC = L_ML + 256;
constexpr int L_S = 8192;
constexpr int L_P = L_S + 16384;
constexpr int L_RING = L_P + 16384;
constexpr int LDS_BYTES = 160 * 1024;
static_assert(L_HREC % 16 == 0 && L_HREC + 8 * 512 <= 7168 && L_RING + RING * SLOT <= LDS_BYTES, "lds map");
constexpr int W_EPOCH = 0;
constexpr int W_ML = 4096;
constexpr int W_QG = 65536;
constexpr int W_PO = W_QG + 2 * 8 * 2048 * 8;
constexpr int W_YG = W_PO + 256 * 2048;
constexpr int YHALF = 8 * 32 * 512 * 8;
constexpr int W_END = W_YG + 2 * YHALF;
constexpr size_t W_STAMP = 64u << 20;
constexpr unsigned SPIN_CAP = 1u << 21;
constexpr int OOB = 0x7FFFFF00;

__device__ __forceinline__ unsigned cvtpk(float lo, float hi) { return __builtin_bit_cast(unsigned, __builtin_convertvector(f32x2{lo, hi}, bf16x2)); }
__device__ __forceinline__ __amdgpu_buffer_rsrc_t mk(const void* p, unsigned bytes) { return __builtin_amdgcn_make_buffer_rsrc((void*)p, 0, bytes, 0x00020000); }
__device__ __forceinline__ v4i mksrd(const void* p, unsigned bytes) { return v4i{(int)(uintptr_t)p, (int)((uintptr_t)p >> 32), (int)bytes, 0x00020000}; }
__device__ __forceinline__ f32x4 mfma32(v4u a, v4u b, f32x4 c) { return __builtin_amdgcn_mfma_f32_16x16x32_bf16(__builtin_bit_cast(bf16x8, a), __builtin_bit_cast(bf16x8, b), c, 0, 0, 0); }
__device__ __forceinline__ f32x4 mfma16(v2u a, v2u b, f32x4 c) { return __builtin_amdgcn_mfma_f32_16x16x16bf16_1k(__builtin_bit_cast(s16x4, a), __builtin_bit_cast(s16x4, b), c, 0, 0, 0); }
__device__ __forceinline__ float ex2(float x) { return __builtin_amdgcn_exp2f(x); }
__device__ __forceinline__ v4u ld_sc1(__amdgpu_buffer_rsrc_t r, int vo, int so) { return __builtin_amdgcn_raw_buffer_load_b128(r, vo, so, 16); }
__device__ __forceinline__ void st_sc1(v4u v, __amdgpu_buffer_rsrc_t r, int vo, int so) { __builtin_amdgcn_raw_buffer_store_b128(v, r, vo, so, 16); }
template <int CTRL> __device__ __forceinline__ float dpp(float x) { return __builtin_bit_cast(float, __builtin_amdgcn_mov_dpp(__builtin_bit_cast(int, x), CTRL, 0xf, 0xf, true)); }
constexpr int XOR1 = 0xB1, XOR2 = 0x4E, HMIR = 0x141, ROR8 = 0x128;
__device__ __forceinline__ float x16_sum(float x) { auto s = __builtin_amdgcn_permlane16_swap(__float_as_uint(x), __float_as_uint(x), false, false); return __uint_as_float(s[0]) + __uint_as_float(s[1]); }
__device__ __forceinline__ float x32_sum(float x) { auto s = __builtin_amdgcn_permlane32_swap(__float_as_uint(x), __float_as_uint(x), false, false); return __uint_as_float(s[0]) + __uint_as_float(s[1]); }
__device__ __forceinline__ float xg_max(float x) {
  auto s = __builtin_amdgcn_permlane16_swap(__float_as_uint(x), __float_as_uint(x), false, false);
  x = fmaxf(__uint_as_float(s[0]), __uint_as_float(s[1]));
  auto t = __builtin_amdgcn_permlane32_swap(__float_as_uint(x), __float_as_uint(x), false, false);
  return fmaxf(__uint_as_float(t[0]), __uint_as_float(t[1]));
}
#define CBAR() asm volatile("" ::: "memory")
#define OPAQUE_S(x) asm volatile("" : "+s"(x))
#define LDSP(T, off) ((LAS T*)(lds + (off)))
__device__ __forceinline__ unsigned lds_ld(LAS unsigned char* lds, int off) { return __builtin_amdgcn_readfirstlane(*(volatile LAS unsigned*)(lds + off)); }
__device__ __forceinline__ void lds_st(LAS unsigned char* lds, int off, unsigned v) { *(volatile LAS unsigned*)(lds + off) = v; }
template <int SLP> __device__ __forceinline__ unsigned lds_wait_ge(LAS unsigned char* lds, int off, unsigned want) {
  unsigned v = lds_ld(lds, off);
#pragma unroll 1
  for (unsigned sp = 0; v < want && sp < SPIN_CAP * 8; ++sp) { __builtin_amdgcn_s_sleep(SLP); v = lds_ld(lds, off); }
  CBAR();
  return v;
}
__device__ __forceinline__ void lds_inc(LAS unsigned char* lds, int off, int lane) {
  CBAR();
  if (lane == 0) __hip_atomic_fetch_add(LDSP(unsigned, off), 1u, __ATOMIC_RELAXED, __HIP_MEMORY_SCOPE_WORKGROUP);
  CBAR();
}
#define STAMP(i) do {} while (0)
#define STAMP_OUT(lo, hi) do {} while (0)

template <int NFL> __device__ __forceinline__ void dma4(unsigned m0v, unsigned v0, unsigned v1, unsigned v2, unsigned v3, v4i srd, unsigned so) {
  asm volatile("s_mov_b32 m0, %0\n\t"
               "s_waitcnt vmcnt(%7)\n\tbuffer_load_dwordx4 %1, %5, %6 offen nt lds\n\t"
               "s_waitcnt vmcnt(%7)\n\tbuffer_load_dwordx4 %2, %5, %6 offen offset:1024 nt lds\n\t"
               "s_waitcnt vmcnt(%7)\n\tbuffer_load_dwordx4 %3, %5, %6 offen offset:2048 nt lds\n\t"
               "s_waitcnt vmcnt(%7)\n\tbuffer_load_dwordx4 %4, %5, %6 offen offset:3072 nt lds"
               :: "s"(m0v), "v"(v0), "v"(v1), "v"(v2), "v"(v3), "s"(srd), "s"(so), "i"(NFL - 1) : "memory");
}

template <class C>
__global__ void __launch_bounds__(NTHR, 1) fused_decode(const u16* xa, const u16* ak, const u16* av, const u16* wqa, const u16* woa,
                                                        const u16* xb, const u16* bk, const u16* bv, const u16* wqb, const u16* wob,
                                                        u16* out, unsigned char* ws) {
  constexpr int NFL = C::NFL, PARK = C::PARK;
  extern __shared__ __attribute__((aligned(16))) unsigned char lds_raw[];
  LAS unsigned char* lds = (LAS unsigned char*)lds_raw;
  const int wg = blockIdx.x, lane = threadIdx.x & 63, wave = __builtin_amdgcn_readfirstlane(threadIdx.x >> 6);
  const int xcd = wg & 7, jw = wg >> 3, b = jw >> 2, sp = jw & 3, h = 4 * xcd + sp;
  const int g = lane >> 4, n = lane & 15;
  const unsigned lds0 = (unsigned)(uintptr_t)lds;
  if (threadIdx.x < 16) *(volatile LAS unsigned*)(lds + 4 * lane) = 0u;
  asm volatile("s_waitcnt lgkmcnt(0)" :: "s"(wqb), "s"(wob), "s"(out), "s"(ws), "s"(xa) : "memory");
  __builtin_amdgcn_s_barrier();
  CBAR();

  if (wave == 0) {
    __builtin_amdgcn_s_setprio(3);
    const size_t ua = (size_t)(b * NH + h) * SK * DH, ub = (size_t)(b * HKV + xcd) * SK * DH;
    const unsigned k0 = g * 256 + ((n ^ g) * 16), vv = lane * 16;
    unsigned slot = lds0 + L_RING;
    int limit = RING;
    constexpr int NTOP = C::PACE ? NFL + 4 : NFL;
    constexpr int LAG0 = (NTOP + 4 + 7) / 8, LAG1 = (NTOP + 7) / 8;
    static_assert(NTOP >= 17 && NTOP <= 32, "publish lag");
    u64 now = __builtin_amdgcn_s_memrealtime();
    unsigned due = (unsigned)now + C::PACE0 - 4 * C::PACE / 100;
    now = due;
    STAMP(0);
    int t = 0;
#pragma unroll 1
    for (int ph = 0; ph < 4; ++ph) {
      const v4i rs = mksrd(ph == 0 ? ak + ua : ph == 1 ? bk + ub : ph == 2 ? bv + ub : av + ua, SK * 256);
      const int tend = ph == 0 ? 128 : ph == 1 ? 160 : ph == 2 ? 192 : NSL;
      const unsigned v0 = ph < 2 ? k0 : vv, v1 = ph < 2 ? k0 ^ 64 : vv, v2 = ph < 2 ? k0 ^ 128 : vv, v3 = ph < 2 ? k0 ^ 192 : vv;
      unsigned r = (ph == 1 || ph == 2) ? (sp * 32 * C::MUL) & 127 : 0u;
#define RUN4(N_) do { _Pragma("unroll 1") for (const int te = t + 4; t < te; ++t) { \
          const unsigned so = (((r & 0x70u) << 1) | (r & 15u)) << 12;       \
          dma4<N_>(slot, v0, v1, v2, v3, rs, so); \
          if (LAG0 == LAG1) lds_st(lds, L_FULL, (unsigned)max(t - LAG0 + 1, 0)); \
          dma4<N_>(slot + 4096, v0, v1, v2, v3, rs, so + 65536u); \
          if (LAG0 != LAG1) lds_st(lds, L_FULL, (unsigned)max(t - LAG1 + 1, 0)); \
          slot += SLOT; if (slot >= lds0 + L_RING + RING * SLOT) slot = lds0 + L_RING; \
          r = (r + C::MUL) & 127u; } } while (0)
#pragma unroll 1
      while (t < tend) {
        if (!(C::DBG & 4) && t + 3 >= limit) {
#pragma unroll 1
          for (unsigned spn = 0; spn < SPIN_CAP * 8; ++spn) {
            const v4u d = *(volatile LAS v4u*)(lds + L_DONE);
            const int c = min(min(4 * (int)d[0], 4 * (int)d[1] + 1), min(4 * (int)d[2] + 2, 4 * (int)d[3] + 3));
            limit = __builtin_amdgcn_readfirstlane(c) + RING;
            if (t + 3 < limit) break;
            __builtin_amdgcn_s_sleep(1);
          }
        }
        if (C::PACE == 0) RUN4(NFL);
        else {
          asm volatile("" : "+s"(now));
          const int lag = (int)((unsigned)now - due); due += ph == 0 ? 4 * C::PACE / 100 : 4 * C::PACE2 / 100;
          asm volatile("s_memrealtime %0" : "=s"(now));
          if (lag < C::UP0) { if (lag >= -C::DN0) RUN4(NFL); else if (lag >= -C::DN0 - C::STEP || C::NMIN > NFL - 2) RUN4(NFL - 1); else RUN4(NFL - 2); }
          else if (lag < C::UP0 + 2 * C::STEP) { if (lag < C::UP0 + C::STEP) RUN4(NFL + 1); else RUN4(NFL + 2); }
          else if (lag < C::UP0 + 3 * C::STEP) RUN4(NFL + 3); else RUN4(NFL + 4);
        }
      }
    }
    asm volatile("s_waitcnt vmcnt(24)" ::: "memory"); lds_st(lds, L_FULL, NSL - 3);
    asm volatile("s_waitcnt vmcnt(16)" ::: "memory"); lds_st(lds, L_FULL, NSL - 2);
    asm volatile("s_waitcnt vmcnt(8)" ::: "memory");  lds_st(lds, L_FULL, NSL - 1);
    asm volatile("s_waitcnt vmcnt(0)" ::: "memory");  lds_st(lds, L_FULL, NSL);
    STAMP(1); STAMP_OUT(0, 2); STAMP_OUT(16, 26);
  } else if (wave >= 2 && wave < 6) {
    if (C::DBG & 8) return;
    const int w = wave - 2, jj = n >> 2;
    int slot = L_RING + w * SLOT; unsigned full = 0, cnt = 0; int t = w;
#define SLOT_WAIT() do { if (full <= (unsigned)t) full = lds_wait_ge<1>(lds, L_FULL, (unsigned)t + 1u); CBAR(); } while (0)
#define SLOT_DONE() do { CBAR(); ++cnt; lds_st(lds, L_DONE + 4 * w, cnt);     \
        CBAR(); slot += 4 * SLOT; if (slot >= L_RING + RING * SLOT) slot -= RING * SLOT; t += 4; } while (0)
#define K_READ(T_) do { SLOT_WAIT(); _Pragma("unroll") for (int m_ = 0; m_ < 8; ++m_) T_[m_] = *LDSP(v4u, ((slot + kb) ^ (64 * (m_ & 3))) + (m_ >> 2) * 4096); SLOT_DONE(); } while (0)
    const int sw = L_S + w * 4096;
    {
      const int kb = n * 256 + ((g ^ n) * 16);
      v4u q[4];
      float mx = -3.0e38f;
      const int sa0 = sw + ((n & 3) * 8 + g * 2 + (jj & 1)) * 16;
      int sa = sa0;
#define K_MATH(T_, act_, step_) do { f32x4 s0 = {0.f, 0.f, 0.f, 0.f}, s1 = {0.f, 0.f, 0.f, 0.f}; \
        _Pragma("unroll") for (int m = 0; m < 4; ++m) { s0 = mfma32(T_[m], q[m], s0); s1 = mfma32(T_[4 + m], q[m], s1); }       \
        mx = fmaxf(fmaxf(fmaxf(fmaxf(s0[0], s0[1]), fmaxf(s0[2], s0[3])), fmaxf(fmaxf(s1[0], s1[1]), fmaxf(s1[2], s1[3]))), mx); \
        if (act_) *LDSP(f32x4, sa) = (jj & 1) ? s1 : s0; \
        sa += (step_); } while (0)
      {
        v4u P[PARK][8];
#pragma unroll
        for (int i = 0; i < PARK; ++i) K_READ(P[i]);
        lds_wait_ge<1>(lds, L_QRDY, 1u);
#pragma unroll
        for (int m = 0; m < 4; ++m) q[m] = *LDSP(v4u, L_QIMG + g * 16 + 64 * m);
        if (w == 0) STAMP(2);
#pragma unroll
        for (int i = 0; i < PARK; ++i) K_MATH(P[i], (n & 11) == 0, 128);
      }
#pragma unroll 1
      for (int ph = 0; ph < 2; ++ph) {
        const bool act = ph == 1 ? n < 8 : (n & 11) == 0;
        const int sstep = ph == 1 ? 512 : 128, tend = ph == 0 ? 128 : 160;
#pragma unroll 1
        while (t < tend) { v4u T[8]; K_READ(T); K_MATH(T, act, sstep); }
        {
          lds_wait_ge<1>(lds, L_QRDY + 4, 1u);
          const float Mh = xg_max(mx);
          const float M = __builtin_bit_cast(float, __builtin_amdgcn_ds_bpermute(((lane >> 3) & 3) * 4, __builtin_bit_cast(int, Mh)));
          const float mc = -M * SC2;
          float l = 0.f;
          CBAR();
#pragma unroll
          for (int j = 0; j < 4; ++j) {
            const f32x4 s = *LDSP(f32x4, sw + 16 * lane + 1024 * j);
            const float p0 = ex2(fmaf(s[0], SC2, mc)), p1 = ex2(fmaf(s[1], SC2, mc)), p2 = ex2(fmaf(s[2], SC2, mc)), p3 = ex2(fmaf(s[3], SC2, mc));
            l += (p0 + p1) + (p2 + p3);
            *LDSP(v2u, L_P + ph * 8192 + w * 2048 + 8 * lane + 512 * j) = v2u{cvtpk(p0, p1), cvtpk(p2, p3)};
          }
          l += dpp<XOR1>(l); l += dpp<XOR2>(l); l += dpp<HMIR>(l); l = x32_sum(l);
          const float la = x16_sum(l + dpp<ROR8>(l));
          if ((lane & 39) == 0) *LDSP(f32x2, L_ML + ph * 128 + w * 32 + lane) = f32x2{M, ph == 1 ? l : la};
          lds_inc(lds, L_MLC, lane);
        }
        mx = -3.0e38f; sa = sa0;
#pragma unroll
        for (int m = 0; m < 4; ++m) q[m] = *LDSP(v4u, L_QIMG + 256 + (n & 3) * 256 + g * 16 + 64 * m);
      }
    }
    {
      const unsigned Z = 0x0C0C0C0Cu;
      const unsigned el0 = jj == 0 ? 0x0C0C0100u : jj == 1 ? 0x01000C0Cu : Z, el1 = jj == 2 ? 0x0C0C0100u : jj == 3 ? 0x01000C0Cu : Z;
      const unsigned eh0 = jj == 0 ? 0x0C0C0302u : jj == 1 ? 0x03020C0Cu : Z, eh1 = jj == 2 ? 0x0C0C0302u : jj == 3 ? 0x03020C0Cu : Z;
      const int vb = g * 1024 + n * 16;
#pragma unroll 1
      for (int ph = 1; ph >= 0; --ph) {
        float f = 1.f;
        if (ph == 0) {
          lds_wait_ge<1>(lds, L_MLC, 8u);
          f32x2 ml[4];
#pragma unroll
          for (int u = 0; u < 4; ++u) ml[u] = *LDSP(f32x2, L_ML + u * 32);
          const float M = fmaxf(fmaxf(ml[0][0], ml[1][0]), fmaxf(ml[2][0], ml[3][0]));
          float L = 0.f;
#pragma unroll
          for (int u = 0; u < 4; ++u) L = fmaf(ex2((ml[u][0] - M) * SC2), ml[u][1], L);
          f = ex2(((*LDSP(f32x2, L_ML + w * 32))[0] - M) * SC2) / L;
        }
        const int tend = ph == 1 ? 192 : NSL, pstep = ph == 1 ? 256 : 64;
        int pa = L_P + ph * 8192 + w * 2048 + (ph == 1 ? (n & 3) * 64 : 0) + g * 16;
        f32x4 a0 = {0, 0, 0, 0}, a1 = {0, 0, 0, 0};
#pragma unroll 1
        while (t < tend) {
          const v4u pp = *LDSP(v4u, pa); pa += pstep;
          v2u bf[8];
#pragma unroll
          for (int i = 0; i < 8; ++i) { const unsigned pk = pp[i >> 1]; bf[i] = v2u{__builtin_amdgcn_perm(pk, pk, (i & 1) ? eh0 : el0), __builtin_amdgcn_perm(pk, pk, (i & 1) ? eh1 : el1)}; }
#pragma unroll
          for (int i = 0; i < 8; ++i) asm volatile("" : "+v"(bf[i]));
          SLOT_WAIT();
          v4u T[8];
#pragma unroll
          for (int i = 0; i < 8; ++i) T[i] = *LDSP(v4u, slot + vb + (i >> 2) * 4096 + (i & 3) * 256);
          SLOT_DONE();
#pragma unroll
          for (int i = 0; i < 8; ++i) { a0 = mfma16(v2u{T[i][0], T[i][1]}, bf[i], a0); a1 = mfma16(v2u{T[i][2], T[i][3]}, bf[i], a1); }
        }
        a0 *= f; a1 *= f;
        const int ro = (ph == 1 ? sw + (n & 3) * 512 : L_HREC + 2048 + w * 512) + jj * 128 + g * 32;
        if (ph == 1 || (n & 3) == 0) {
          *LDSP(f32x4, ro) = f32x4{a0[0], a1[0], a0[1], a1[1]};
          *LDSP(f32x4, ro + 16) = f32x4{a0[2], a1[2], a0[3], a1[3]};
        }
        lds_inc(lds, ph == 1 ? L_BCNT : L_RCNT + 4, lane);
      }
    }
    if (w == 3) { STAMP(3); STAMP_OUT(2, 4); }
  } else if (wave >= 6) {
    if (C::DBG & 8) return;
    const int w = wave - 6;
    if (C::PF_AUX >= 0) {
#pragma unroll 1
      for (int mat = 0; mat < 2; ++mat) {
        if (mat) lds_wait_ge<16>(lds, L_FULL, 32u);
        const __amdgpu_buffer_rsrc_t rw = mk((mat ? wqb : wqa) + (size_t)(16 * jw + w) * 4096 + xcd * 512, 13 * 8192);
        v4u d[4];
#pragma unroll
        for (int i = 0; i < 4; ++i) d[i] = __builtin_amdgcn_raw_buffer_load_b128(rw, lane * 16, ((i & 1) * 8 + (i >> 1) * 4) * 8192, C::PF_AUX < 0 ? 0 : C::PF_AUX);
#pragma unroll
        for (int i = 0; i < 4; ++i) asm volatile("" :: "v"(d[i]));

      }
    }
    if (C::DBG & 16) return;
    const __amdgpu_buffer_rsrc_t ry = mk(ws + W_YG, 2 * YHALF);
    const int yo = ((b * 32 + h) * 512 + 128 * w + 8 * n + 4 * (g & 1) + 2 * (g >> 1)) * 8;
    unsigned sl[4], sh[4];
#pragma unroll
    for (int d = 0; d < 4; ++d) { const bool on = ((n & 7) >> 1) == d; sl[d] = !on ? 0x0C0C0C0Cu : (n & 1) ? 0x01000C0Cu : 0x0C0C0100u; sh[d] = !on ? 0x0C0C0C0Cu : (n & 1) ? 0x03020C0Cu : 0x0C0C0302u; }
    v4u yb = {0, 0, 0, 0};
    lds_wait_ge<16>(lds, L_FULL, 72u);
#pragma unroll 1
    for (int pass = 0; pass < 2; ++pass) {
      const __amdgpu_buffer_rsrc_t rw = mk((pass == 0 ? wob : woa) + (size_t)h * 128 * 512, (C::DBG & 1) ? 0 : 128 * 1024);
      if (C::WO_PF) {
        v4u d[4];
#pragma unroll
        for (int i = 0; i < 4; ++i) d[i] = __builtin_amdgcn_raw_buffer_load_b128(rw, (2 * b + (w >> 1) + 16 * (w & 1)) * 4096 + lane * 16, i * 1024, C::WO_PF_AUX);
#pragma unroll
        for (int i = 0; i < 4; ++i) asm volatile("" :: "v"(d[i]));
        lds_wait_ge<16>(lds, L_FULL, lds_ld(lds, L_FULL) + C::WO_PF_GAP);
      }
      v4u wo[32];
#pragma unroll
      for (int i = 0; i < 32; ++i) {
        wo[i] = __builtin_amdgcn_raw_buffer_load_b128(rw, g * 1024 + w * 256 + n * 16, i * 4096, C::WO_AUX);
        __builtin_amdgcn_sched_barrier(0);
        __builtin_amdgcn_s_sleep(C::WO_SLEEP);
        __builtin_amdgcn_sched_barrier(0);
      }
      if (pass == 1) { lds_wait_ge<8>(lds, L_FULL, NSL - 8); st_sc1(yb, ry, yo, YHALF); }
      asm volatile("s_waitcnt vmcnt(1)" ::: "memory");
      lds_wait_ge<1>(lds, L_RCNT + 4 * pass, 4u);
      const unsigned tag = *LDSP(unsigned, L_TAG);
      const int rb = L_HREC + pass * 2048 + 8 * lane;
      const f32x2 o = (*LDSP(f32x2, rb) + *LDSP(f32x2, rb + 512)) + (*LDSP(f32x2, rb + 1024) + *LDSP(f32x2, rb + 1536));
      const int oi = L_OIMG + w * 256;
      *LDSP(unsigned, oi + 4 * lane) = cvtpk(o[0], o[1]);
      CBAR();
      v4u of[4];
#pragma unroll
      for (int u = 0; u < 4; ++u) of[u] = *LDSP(v4u, oi + 64 * g + 16 * u);
      f32x4 y0 = {0, 0, 0, 0}, y1 = {0, 0, 0, 0};
#pragma unroll
      for (int i = 0; i < 32; i += 2) {
        const unsigned pk = of[i >> 3][(i >> 1) & 3];
        v4u a0, a1;
#pragma unroll
        for (int d = 0; d < 4; ++d) { a0[d] = __builtin_amdgcn_perm(pk, pk, sl[d]); a1[d] = __builtin_amdgcn_perm(pk, pk, sh[d]); }
        y0 = mfma32(a0, wo[i], y0); y1 = mfma32(a1, wo[i + 1], y1);
      }
      y0 += y1;
      yb = v4u{__float_as_uint(g < 2 ? y0[0] : y0[2]), tag, __float_as_uint(g < 2 ? y0[1] : y0[3]), tag};
      st_sc1(yb, ry, pass == 0 ? OOB : yo, 0);
      lds_inc(lds, L_YPUB, lane);
      if (w == 0) STAMP(4 + pass);
    }
    if (w == 0) STAMP_OUT(4, 6);
  } else {
    if (C::DBG & 8) return;
    unsigned G;
    asm volatile("global_load_dword %0, %1, %2 sc1" : "=v"(G) : "v"(W_EPOCH), "s"(ws) : "memory");
    const int cb = xcd * 32 + jw;
    const __amdgpu_buffer_rsrc_t rs = mk(ws, W_END);
    unsigned tag = 0;
    const int ta = L_P + (8 * g + (n >> 2)) * 32 + (n & 3) * 8;
    int it = -1; OPAQUE_S(it);
    v4u xf[16];
#pragma unroll 1
    for (; it < 2; ++it) {
      if (it != 0) {
        if (it == 1) lds_wait_ge<16>(lds, L_FULL, 52u);
        const v4i rx = mksrd(it == 1 ? xb : xa, (C::DBG & 128) ? 0 : 8 * 512 * 2);
#pragma unroll
        for (int s = 0; s < 16; ++s) asm volatile("buffer_load_dwordx4 %0, %1, %2, 0 offen offset:%3" : "+v"(xf[s]) : "v"(n * 1024 + 16 * g), "s"(rx), "i"(64 * s) : "memory");
      }
      if (it >= 0) {
        if (it == 0) lds_wait_ge<1>(lds, L_FULL, C::T_SLICE);
        const v4i rw = mksrd((it ? wqb : wqa) + cb * 16, (C::DBG & 32) ? 0 : 512 * 8192 - cb * 32);
        const unsigned vo = (lane >> 1) * 8192 + (lane & 1) * 16;
#pragma unroll
        for (int i = 0; i < 16; ++i) {
          if (C::SL_NT) asm volatile("s_mov_b32 m0, %0\n\ts_nop 0\n\tbuffer_load_dwordx4 %1, %2, %3 offen nt lds" :: "s"(lds0 + L_P + i * 1024), "v"(vo), "s"(rw), "s"(i * 32 * 8192) : "memory");
          else asm volatile("s_mov_b32 m0, %0\n\ts_nop 0\n\tbuffer_load_dwordx4 %1, %2, %3 offen lds" :: "s"(lds0 + L_P + i * 1024), "v"(vo), "s"(rw), "s"(i * 32 * 8192) : "memory");
          if (it) __builtin_amdgcn_s_sleep(C::WQB_SLEEP); else if (C::WQA_SLEEP) __builtin_amdgcn_s_sleep(C::WQA_SLEEP);
        }
        asm volatile("s_waitcnt vmcnt(0)\n\tv_readfirstlane_b32 %0, %1\n\ts_nop 1" : "=s"(tag) : "v"(G) : "memory");
        tag += 1u;
        if (it == 0) STAMP(6);
      }
#pragma unroll
      for (int s = 0; s < 16; ++s) asm volatile("" : "+v"(xf[s]));
      f32x4 d0 = {0, 0, 0, 0}, d1 = {0, 0, 0, 0};
      {
        v2u t[16][2];
#pragma unroll
        for (int s = 0; s < 16; ++s)
#pragma unroll
          for (int u = 0; u < 2; ++u) t[s][u] = __builtin_bit_cast(v2u, __builtin_amdgcn_ds_read_tr16_b64_v4i16(LDSP(s16x4, ta + s * 1024 + u * 128)));
        __builtin_amdgcn_sched_barrier(0);
#pragma unroll
        for (int s = 0; s < 16; s += 2) {
          d0 = mfma32(v4u{t[s][0][0], t[s][0][1], t[s][1][0], t[s][1][1]}, xf[s], d0);
          d1 = mfma32(v4u{t[s + 1][0][0], t[s + 1][0][1], t[s + 1][1][0], t[s + 1][1][1]}, xf[s + 1], d1);
        }
      }
      d0 += d1;
      st_sc1(v4u{cvtpk(d0[0], d0[1]), tag, cvtpk(d0[2], d0[3]), tag}, rs, (it >= 0 && n < 8 && !(C::DBG & 64)) ? W_QG + ((it * 8 + n) * 2048 + cb * 8 + 2 * g) * 8 : OOB, 0);
      if (it == 0) STAMP(7);
      if (it < 0) continue;
      lds_wait_ge<4>(lds, L_FULL, it ? 104u : C::T_GATHER);
      const int go = (C::DBG & (64 | 256)) ? OOB : it == 0 ? (lane < 32 ? W_QG + (b * 2048 + h * 64 + 2 * lane) * 8 : OOB) : W_QG + ((8 + b) * 2048 + xcd * 256 + 2 * lane) * 8;
      const u64 need = (C::DBG & (64 | 256)) ? 0ull : it == 0 ? 0xFFFFFFFFull : ~0ull;
#pragma unroll 1
      for (unsigned spn = 0; spn < SPIN_CAP; ++spn) {
        const v4u v = ld_sc1(rs, go, 0), u = ld_sc1(rs, it == 1 ? go : OOB, 1024);
        if (it == 1 || lane < 32) *LDSP(v2u, L_QIMG + (it == 1 ? 256 : 0) + 8 * lane) = v2u{v[0], v[2]};
        if (it == 1) *LDSP(v2u, L_QIMG + 768 + 8 * lane) = v2u{u[0], u[2]};
        if ((__builtin_amdgcn_ballot_w64(v[1] != tag || v[3] != tag || (it == 1 && (u[1] != tag || u[3] != tag))) & need) == 0) break;
        __builtin_amdgcn_s_sleep(24);
      }
      CBAR();
      lds_st(lds, L_QRDY + 4 * it, 1u);
      if (it == 0) STAMP(8);
    }
    lds_st(lds, L_TAG, tag);
    lds_wait_ge<16>(lds, L_BCNT, 4u);
    {
      float Mk = 0.f, Lk = 0.f;
#pragma unroll
      for (int hp = 0; hp < 2; ++hp) {
        const int hh = 2 * hp + (lane >> 5);
        f32x2 ml[4]; f32x4 ac[4];
#pragma unroll
        for (int u = 0; u < 4; ++u) { ml[u] = *LDSP(f32x2, L_ML + 128 + u * 32 + hh * 8); ac[u] = *LDSP(f32x4, L_S + u * 4096 + hh * 512 + 16 * (lane & 31)); }
        const float M = fmaxf(fmaxf(ml[0][0], ml[1][0]), fmaxf(ml[2][0], ml[3][0]));
        float L = 0.f; f32x4 o = {0, 0, 0, 0};
#pragma unroll
        for (int u = 0; u < 4; ++u) { const float f = ex2((ml[u][0] - M) * SC2); L = fmaf(f, ml[u][1], L); o += f * ac[u]; }
        st_sc1(__builtin_bit_cast(v4u, o), rs, W_PO + (xcd + 8 * (4 * b + hh)) * 2048 + sp * 512 + 16 * (lane & 31), 0);
        if ((lane & 31) == hp) { Mk = M; Lk = L; }
      }
      asm volatile("s_waitcnt vmcnt(0)" ::: "memory");
      const int hh = 2 * (lane & 31) + (lane >> 5);
      st_sc1(v4u{__float_as_uint(Mk), tag, __float_as_uint(Lk), tag}, rs, (lane & 31) < 2 ? W_ML + (xcd + 8 * (4 * b + hh)) * 128 + sp * 16 : OOB, 0);
    }
    lds_wait_ge<16>(lds, L_FULL, 192u + C::T_MLPOLL);
    v4u mlv = {0, 0, 0, 0};
#pragma unroll 1
    for (unsigned spn = 0; spn < SPIN_CAP; ++spn) {
      mlv = ld_sc1(rs, lane < 4 ? W_ML + wg * 128 + lane * 16 : OOB, 0);
      if ((__builtin_amdgcn_ballot_w64(mlv[1] != tag || mlv[3] != tag) & 15u) == 0u) break;
      __builtin_amdgcn_s_sleep(64);
    }
    __builtin_amdgcn_fence(__ATOMIC_ACQUIRE, "wavefront");
    {
      const v4u p0 = ld_sc1(rs, W_PO + wg * 2048 + 16 * lane, 0), p1 = ld_sc1(rs, W_PO + wg * 2048 + 16 * lane, 1024);
      const float m = __uint_as_float(mlv[0]);
      float M = fmaxf(m, dpp<XOR1>(m)); M = fmaxf(M, dpp<XOR2>(M));
      const float e = ex2((m - M) * SC2);
      float L = e * __uint_as_float(mlv[2]); L += dpp<XOR1>(L); L += dpp<XOR2>(L);
      const int fi = __builtin_bit_cast(int, e / L);
      const float f0 = __builtin_bit_cast(float, lane < 32 ? __builtin_amdgcn_readlane(fi, 0) : __builtin_amdgcn_readlane(fi, 1));
      const float f1 = __builtin_bit_cast(float, lane < 32 ? __builtin_amdgcn_readlane(fi, 2) : __builtin_amdgcn_readlane(fi, 3));
      *LDSP(f32x4, L_HREC + 16 * lane) = f0 * __builtin_bit_cast(f32x4, p0);
      *LDSP(f32x4, L_HREC + 1024 + 16 * lane) = f1 * __builtin_bit_cast(f32x4, p1);
      CBAR(); lds_st(lds, L_RCNT, 4u);
    }
    STAMP(9);
    if (C::DBG & 16) return;
#pragma unroll 1
    for (int pass = 0; pass < 2; ++pass) {
      if (pass == 0) lds_wait_ge<8>(lds, L_FULL, NSL - 1); else lds_wait_ge<1>(lds, L_YPUB, 8u);
      const int yo = W_YG + (pass == 0 ? YHALF : 0) + (b * 32 * 512 + (lane >> 3) * 512 + 16 * h + 2 * (lane & 7)) * 8;
      v4u y[4]; bool ok[4];
#pragma unroll
      for (int i = 0; i < 4; ++i) { y[i] = v4u{0, 0, 0, 0}; ok[i] = false; }
#pragma unroll 1
      for (unsigned spn = 0; spn < SPIN_CAP; ++spn) {
        v4u z[4];
#pragma unroll
        for (int i = 0; i < 4; ++i) z[i] = ld_sc1(rs, ok[i] ? OOB : yo, i * 8 * 512 * 8);
        bool all = true;
#pragma unroll
        for (int i = 0; i < 4; ++i) { if (!ok[i]) { y[i] = z[i]; ok[i] = z[i][1] == tag && z[i][3] == tag; } all &= ok[i]; }
        if (__builtin_amdgcn_ballot_w64(!all) == 0) break;
        __builtin_amdgcn_s_sleep(3);
      }
      float s0 = (__uint_as_float(y[0][0]) + __uint_as_float(y[1][0])) + (__uint_as_float(y[2][0]) + __uint_as_float(y[3][0]));
      float s1 = (__uint_as_float(y[0][2]) + __uint_as_float(y[1][2])) + (__uint_as_float(y[2][2]) + __uint_as_float(y[3][2]));
      s0 += dpp<ROR8>(s0); s1 += dpp<ROR8>(s1);
      s0 = x32_sum(x16_sum(s0)); s1 = x32_sum(x16_sum(s1));
      if (lane < 8) *(GAS unsigned*)(out + b * 1024 + (pass == 0 ? 512 : 0) + 16 * h + 2 * lane) = cvtpk(s0, s1);
      STAMP(10 + pass);
    }
    if (wg == 0 && lane == 0) __hip_atomic_store((GAS unsigned*)(ws + W_EPOCH), tag, RLX_AGENT);
    STAMP_OUT(6, 12);
  }
}

template <class C>
static void launch_cfg(void* const* d_in, void* d_out, void* d_ws, hipStream_t stream) {
  (void)hipFuncSetAttribute((const void*)fused_decode<C>, hipFuncAttributeMaxDynamicSharedMemorySize, LDS_BYTES);
  hipLaunchKernelGGL(fused_decode<C>, dim3(256), dim3(NTHR), LDS_BYTES, stream,
                     (const u16*)d_in[0], (const u16*)d_in[1], (const u16*)d_in[2], (const u16*)d_in[3], (const u16*)d_in[4],
                     (const u16*)d_in[5], (const u16*)d_in[6], (const u16*)d_in[7], (const u16*)d_in[8], (const u16*)d_in[9],
                     (u16*)d_out, (unsigned char*)d_ws);
}
extern "C" void kernel_launch(void* const* d_in, const int* in_sizes, int n_in, void* d_out, int out_size, void* d_ws, size_t ws_size, hipStream_t stream) {
  (void)in_sizes; (void)n_in; (void)out_size; (void)ws_size;
  launch_cfg<CfgDefault>(d_in, d_out, d_ws, stream);
}
```

```cpp
#include <hip/hip_runtime.h>
#include <hip/hip_bf16.h>
#include <stdint.h>

typedef unsigned v4u __attribute__((ext_vector_type(4)));
typedef unsigned v2u __attribute__((ext_vector_type(2)));
typedef int v4i __attribute__((ext_vector_type(4)));
typedef float f32x4 __attribute__((ext_vector_type(4)));
typedef float f32x2 __attribute__((ext_vector_type(2)));
typedef short s16x4 __attribute__((ext_vector_type(4)));
typedef __bf16 bf16x8 __attribute__((ext_vector_type(8)));
typedef __bf16 bf16x2 __attribute__((ext_vector_type(2)));
typedef unsigned short u16;
typedef unsigned long long u64;
#define GAS __attribute__((address_space(1)))
#define LAS __attribute__((address_space(3)))
#define RLX_AGENT __ATOMIC_RELAXED, __HIP_MEMORY_SCOPE_AGENT

struct CfgDefault {
  static constexpr int NFL = 20;
  static constexpr int PACE = 2925, PACE2 = 2825;
  static constexpr int UP0 = 60, DN0 = 40;
  static constexpr int PACE0 = 20;
  static constexpr int STEP = 50;
  static constexpr int NDN = 1, NUP = 2, USTEP = 80, ROTA = 0, ROTB = 0, PF_SER = 0;
  static constexpr int T_YB = 12, Y_SLEEP = 3;
  static constexpr int SL_NT = 1, WO_PF = 1, WO_PF_AUX = 0, WO_PF_GAP = 12;
  static constexpr int MUL = 11;
  static constexpr int WO_AUX = 0;
  static constexpr int WO_SLEEP = 0;
  static constexpr int PF_AUX = 0;
  static constexpr int T_SLICE = 3, T_GATHER = 15, T_MLPOLL = 24;
  static constexpr int WQA_SLEEP = 0, WQB_SLEEP = 12;
  static constexpr int PARK = 4;
  static constexpr int DBG = 0;
};

constexpr int SK = 4096, DH = 128, NH = 32, HKV = 8;
constexpr float SC2 = 0.088388347648318440f * 1.4426950408889634f;
constexpr int NWAVE = 10, NTHR = NWAVE * 64;
constexpr int NSL = 320;
constexpr int RING = 15, SLOT = 8192;
constexpr int L_FULL = 0, L_QRDY = 4  , L_BCNT = 12, L_DONE = 16, L_RCNT = 32  , L_TAG = 40, L_YPUB = 44, L_MLC = 48;
constexpr int L_QIMG = 64;
constexpr int L_WGT = L_QIMG + 1280;
constexpr int L_ML = L_WGT + 128;
constexpr int L_HREC = L_ML + 256;
constexpr int L_YS = 8192 + 16384 + 8192;
constexpr int L_S = 8192;
constexpr int L_P = L_S + 16384;
constexpr int L_RING = L_P + 16384;
constexpr int LDS_BYTES = 160 * 1024;
static_assert(L_HREC % 16 == 0 && L_HREC + 5 * 1024 <= 7168 && L_RING + RING * SLOT <= LDS_BYTES, "lds map");
constexpr int W_EPOCH = 0;
constexpr int W_ML = 4096;
constexpr int W_QG = 65536;
constexpr int W_PO = W_QG + 2 * 256 * 512;
constexpr int W_YB = W_PO + 256 * 1024;
constexpr int W_YA = W_YB + 256 * 1024;
constexpr int W_END = W_YA + 8 * 32 * 512 * 8;
constexpr size_t W_STAMP = 64u << 20;
constexpr unsigned SPIN_CAP = 1u << 21;
constexpr int OOB = 0x7FFFFF00;

__device__ __forceinline__ unsigned cvtpk(float lo, float hi) { return __builtin_bit_cast(unsigned, __builtin_convertvector(f32x2{lo, hi}, bf16x2)); }
__device__ __forceinline__ __amdgpu_buffer_rsrc_t mk(const void* p, unsigned bytes) { return __builtin_amdgcn_make_buffer_rsrc((void*)p, 0, bytes, 0x00020000); }
__device__ __forceinline__ v4i mksrd(const void* p, unsigned bytes) { return v4i{(int)(uintptr_t)p, (int)((uintptr_t)p >> 32), (int)bytes, 0x00020000}; }
__device__ __forceinline__ f32x4 mfma32(v4u a, v4u b, f32x4 c) { return __builtin_amdgcn_mfma_f32_16x16x32_bf16(__builtin_bit_cast(bf16x8, a), __builtin_bit_cast(bf16x8, b), c, 0, 0, 0); }
__device__ __forceinline__ f32x4 mfma16(v2u a, v2u b, f32x4 c) { return __builtin_amdgcn_mfma_f32_16x16x16bf16_1k(__builtin_bit_cast(s16x4, a), __builtin_bit_cast(s16x4, b), c, 0, 0, 0); }
__device__ __forceinline__ float ex2(float x) { return __builtin_amdgcn_exp2f(x); }
__device__ __forceinline__ v4u ld_sc1(__amdgpu_buffer_rsrc_t r, int vo, int so) { return __builtin_amdgcn_raw_buffer_load_b128(r, vo, so, 16); }
__device__ __forceinline__ void st_sc1(v4u v, __amdgpu_buffer_rsrc_t r, int vo, int so) { __builtin_amdgcn_raw_buffer_store_b128(v, r, vo, so, 16); }
template <int CTRL> __device__ __forceinline__ float dpp(float x) { return __builtin_bit_cast(float, __builtin_amdgcn_mov_dpp(__builtin_bit_cast(int, x), CTRL, 0xf, 0xf, true)); }
constexpr int XOR1 = 0xB1, XOR2 = 0x4E, HMIR = 0x141, ROR8 = 0x128;
__device__ __forceinline__ float x16_sum(float x) { auto s = __builtin_amdgcn_permlane16_swap(__float_as_uint(x), __float_as_uint(x), false, false); return __uint_as_float(s[0]) + __uint_as_float(s[1]); }
__device__ __forceinline__ float x32_sum(float x) { auto s = __builtin_amdgcn_permlane32_swap(__float_as_uint(x), __float_as_uint(x), false, false); return __uint_as_float(s[0]) + __uint_as_float(s[1]); }
__device__ __forceinline__ float xg_max(float x) {
  auto s = __builtin_amdgcn_permlane16_swap(__float_as_uint(x), __float_as_uint(x), false, false);
  x = fmaxf(__uint_as_float(s[0]), __uint_as_float(s[1]));
  auto t = __builtin_amdgcn_permlane32_swap(__float_as_uint(x), __float_as_uint(x), false, false);
  return fmaxf(__uint_as_float(t[0]), __uint_as_float(t[1]));
}
#define CBAR() asm volatile("" ::: "memory")
#define OPAQUE_S(x) asm volatile("" : "+s"(x))
#define LDSP(T, off) ((LAS T*)(lds + (off)))
__device__ __forceinline__ unsigned lds_ld(LAS unsigned char* lds, int off) { return __builtin_amdgcn_readfirstlane(*(volatile LAS unsigned*)(lds + off)); }
__device__ __forceinline__ void lds_st(LAS unsigned char* lds, int off, unsigned v) { *(volatile LAS unsigned*)(lds + off) = v; }
template <int SLP> __device__ __forceinline__ unsigned lds_wait_ge(LAS unsigned char* lds, int off, unsigned want) {
  unsigned v = lds_ld(lds, off);
#pragma unroll 1
  for (unsigned sp = 0; v < want && sp < SPIN_CAP * 8; ++sp) { __builtin_amdgcn_s_sleep(SLP); v = lds_ld(lds, off); }
  CBAR();
  return v;
}
__device__ __forceinline__ void lds_inc(LAS unsigned char* lds, int off, int lane) {
  CBAR();
  if (lane == 0) __hip_atomic_fetch_add(LDSP(unsigned, off), 1u, __ATOMIC_RELAXED, __HIP_MEMORY_SCOPE_WORKGROUP);
  CBAR();
}
#define STAMP(i) do {} while (0)
#define STAMP_OUT(lo, hi) do {} while (0)

template <int NFL> __device__ __forceinline__ void dma4(unsigned m0v, unsigned v0, unsigned v1, unsigned v2, unsigned v3, v4i srd, unsigned so) {
  asm volatile("s_mov_b32 m0, %0\n\t"
               "s_waitcnt vmcnt(%7)\n\tbuffer_load_dwordx4 %1, %5, %6 offen nt lds\n\t"
               "s_waitcnt vmcnt(%7)\n\tbuffer_load_dwordx4 %2, %5, %6 offen offset:1024 nt lds\n\t"
               "s_waitcnt vmcnt(%7)\n\tbuffer_load_dwordx4 %3, %5, %6 offen offset:2048 nt lds\n\t"
               "s_waitcnt vmcnt(%7)\n\tbuffer_load_dwordx4 %4, %5, %6 offen offset:3072 nt lds"
               :: "s"(m0v), "v"(v0), "v"(v1), "v"(v2), "v"(v3), "s"(srd), "s"(so), "i"(NFL - 1) : "memory");
}

template <class C>
__global__ void __launch_bounds__(NTHR, 1) fused_decode(const u16* xa, const u16* ak, const u16* av, const u16* wqa, const u16* woa,
                                                        const u16* xb, const u16* bk, const u16* bv, const u16* wqb, const u16* wob,
                                                        u16* out, unsigned char* ws) {
  constexpr int NFL = C::NFL, PARK = C::PARK;
  extern __shared__ __attribute__((aligned(16))) unsigned char lds_raw[];
  LAS unsigned char* lds = (LAS unsigned char*)lds_raw;
  const int wg = blockIdx.x, lane = threadIdx.x & 63, wave = __builtin_amdgcn_readfirstlane(threadIdx.x >> 6);
  const int xcd = wg & 7, jw = wg >> 3, b = jw >> 2, sp = jw & 3, h = 4 * xcd + sp;
  const int g = lane >> 4, n = lane & 15;
  const unsigned lds0 = (unsigned)(uintptr_t)lds;
  if (threadIdx.x < 16) *(volatile LAS unsigned*)(lds + 4 * lane) = 0u;
  asm volatile("s_waitcnt lgkmcnt(0)" :: "s"(wqb), "s"(wob), "s"(out), "s"(ws), "s"(xa) : "memory");
#define START_BARRIER() do { __builtin_amdgcn_s_barrier(); CBAR(); } while (0)

  if (wave == 0) {
    __builtin_amdgcn_s_setprio(3);
    const size_t ua = (size_t)(b * NH + h) * SK * DH, ub = (size_t)(b * HKV + xcd) * SK * DH;
    const unsigned k0 = g * 256 + ((n ^ g) * 16), vv = lane * 16;
    unsigned slot = lds0 + L_RING;
    int limit = RING;
    constexpr int NMAX = C::PACE ? NFL + C::NUP : NFL;
    constexpr int LAG0 = (NMAX + 4 + 7) / 8, LAG1 = (NMAX + 7) / 8;
    static_assert(NMAX >= 17 && NMAX <= 32, "publish lag, ladder");
    u64 now = __builtin_amdgcn_s_memrealtime();
    unsigned due = (unsigned)now + C::PACE0 - 4 * C::PACE / 100;
    now = due;
    STAMP(0);
    int t = 0;
#pragma unroll 1
    for (int ph = 0; ph < 4; ++ph) {
      const v4i rs = mksrd(ph == 0 ? ak + ua : ph == 1 ? bk + ub : ph == 2 ? bv + ub : av + ua, SK * 256);
      const int tend = ph == 0 ? 128 : ph == 1 ? 160 : ph == 2 ? 192 : NSL;
      const unsigned v0 = ph < 2 ? k0 : vv, v1 = ph < 2 ? k0 ^ 64 : vv, v2 = ph < 2 ? k0 ^ 128 : vv, v3 = ph < 2 ? k0 ^ 192 : vv;
      unsigned r = ((ph == 1 || ph == 2) ? sp * 32 * C::MUL + (b * HKV + xcd) * C::ROTB : (b * NH + h) * C::ROTA) & 127u;
#pragma unroll 1
      while (t < tend) {
        if (!(C::DBG & 4) && t + 3 >= limit) {
#pragma unroll 1
          for (unsigned spn = 0; spn < SPIN_CAP * 8; ++spn) {
            const v4u d = *(volatile LAS v4u*)(lds + L_DONE);
            const int c = min(min(4 * (int)d[0], 4 * (int)d[1] + 1), min(4 * (int)d[2] + 2, 4 * (int)d[3] + 3));
            limit = __builtin_amdgcn_readfirstlane(c) + RING;
            if (t + 3 < limit) break;
            __builtin_amdgcn_s_sleep(1);
          }
        }
#define RUN4(N_) do { _Pragma("unroll 1") for (const int te = t + 4; t < te; ++t) { \
          const unsigned so = (((r & 0x70u) << 1) | (r & 15u)) << 12;       \
          if ((N_) == NFL && t == 2) START_BARRIER(); \
          dma4<N_>(slot, v0, v1, v2, v3, rs, so); \
          if (LAG0 == LAG1) lds_st(lds, L_FULL, (unsigned)max(t - LAG0 + 1, 0)); \
          dma4<N_>(slot + 4096, v0, v1, v2, v3, rs, so + 65536u); \
          if (LAG0 != LAG1) lds_st(lds, L_FULL, (unsigned)max(t - LAG1 + 1, 0)); \
          slot += SLOT; if (slot >= lds0 + L_RING + RING * SLOT) slot = lds0 + L_RING; \
          r = (r + C::MUL) & 127u; } } while (0)
        if (C::PACE == 0) RUN4(NFL);
        else {
          asm volatile("" : "+s"(now));
          const int lag = (int)((unsigned)now - due); due += ph == 0 ? 4 * C::PACE / 100 : 4 * C::PACE2 / 100;
          asm volatile("s_memrealtime %0" : "=s"(now));
          if (lag >= -C::DN0) {
            if (lag < C::UP0 || C::NUP == 0) RUN4(NFL); else if (lag < C::UP0 + C::USTEP || C::NUP == 1) RUN4(NFL + 1);
            else if (lag < C::UP0 + 2 * C::USTEP || C::NUP == 2) RUN4(NFL + 2); else RUN4(NFL + 3);
          } else if (lag >= -C::DN0 - C::STEP || C::NDN == 1) RUN4(NFL - 1);
          else if (lag >= -C::DN0 - 2 * C::STEP || C::NDN == 2) RUN4(NFL - 2); else RUN4(NFL - 3);
        }
      }
    }
    asm volatile("s_waitcnt vmcnt(24)" ::: "memory"); lds_st(lds, L_FULL, NSL - 3);
    asm volatile("s_waitcnt vmcnt(16)" ::: "memory"); lds_st(lds, L_FULL, NSL - 2);
    asm volatile("s_waitcnt vmcnt(8)" ::: "memory");  lds_st(lds, L_FULL, NSL - 1);
    asm volatile("s_waitcnt vmcnt(0)" ::: "memory");  lds_st(lds, L_FULL, NSL);
    STAMP(1); STAMP_OUT(0, 2); STAMP_OUT(17, 26);
  } else if (wave >= 2 && wave < 6) {
    START_BARRIER();
    if (C::DBG & 8) return;
    const int w = wave - 2, jj = n >> 2;
    int slot = L_RING + w * SLOT; unsigned full = 0, cnt = 0; int t = w;
#define SLOT_WAIT() do { if (full <= (unsigned)t) full = lds_wait_ge<1>(lds, L_FULL, (unsigned)t + 1u); CBAR(); } while (0)
#define SLOT_DONE() do { CBAR(); ++cnt; lds_st(lds, L_DONE + 4 * w, cnt);     \
        CBAR(); slot += 4 * SLOT; if (slot >= L_RING + RING * SLOT) slot -= RING * SLOT; t += 4; } while (0)
#define K_READ(T_) do { SLOT_WAIT(); _Pragma("unroll") for (int m_ = 0; m_ < 8; ++m_) T_[m_] = *LDSP(v4u, ((slot + kb) ^ (64 * (m_ & 3))) + (m_ >> 2) * 4096); SLOT_DONE(); } while (0)
    const int sw = L_S + w * 4096;
    {
      const int kb = n * 256 + ((g ^ n) * 16);
      v4u q[4];
      float mx = -3.0e38f;
      const int sa0 = sw + ((n & 3) * 8 + g * 2 + (jj & 1)) * 16;
      int sa = sa0;
#define K_MATH(T_, act_, step_) do { f32x4 s0 = {0.f, 0.f, 0.f, 0.f}, s1 = {0.f, 0.f, 0.f, 0.f}; \
        _Pragma("unroll") for (int m = 0; m < 4; ++m) { s0 = mfma32(T_[m], q[m], s0); s1 = mfma32(T_[4 + m], q[m], s1); }       \
        mx = fmaxf(fmaxf(fmaxf(fmaxf(s0[0], s0[1]), fmaxf(s0[2], s0[3])), fmaxf(fmaxf(s1[0], s1[1]), fmaxf(s1[2], s1[3]))), mx); \
        if (act_) *LDSP(f32x4, sa) = (jj & 1) ? s1 : s0; \
        sa += (step_); } while (0)
      {
        v4u P[PARK][8];
#pragma unroll
        for (int i = 0; i < PARK; ++i) K_READ(P[i]);
        lds_wait_ge<1>(lds, L_QRDY, 1u);
#pragma unroll
        for (int m = 0; m < 4; ++m) q[m] = *LDSP(v4u, L_QIMG + g * 16 + 64 * m);
        if (w == 0) STAMP(2);
#pragma unroll
        for (int i = 0; i < PARK; ++i) K_MATH(P[i], (n & 11) == 0, 128);
      }
#pragma unroll 1
      for (int ph = 0; ph < 2; ++ph) {
        const bool act = ph == 1 ? n < 8 : (n & 11) == 0;
        const int sstep = ph == 1 ? 512 : 128, tend = ph == 0 ? 128 : 160;
#pragma unroll 1
        while (t < tend) { v4u T[8]; K_READ(T); K_MATH(T, act, sstep); }
        {
          lds_wait_ge<1>(lds, L_QRDY + 4, 1u);
          const float Mh = xg_max(mx);
          const float M = __builtin_bit_cast(float, __builtin_amdgcn_ds_bpermute(((lane >> 3) & 3) * 4, __builtin_bit_cast(int, Mh)));
          const float mc = -M * SC2;
          float l = 0.f;
          CBAR();
#pragma unroll
          for (int j = 0; j < 4; ++j) {
            const f32x4 s = *LDSP(f32x4, sw + 16 * lane + 1024 * j);
            const float p0 = ex2(fmaf(s[0], SC2, mc)), p1 = ex2(fmaf(s[1], SC2, mc)), p2 = ex2(fmaf(s[2], SC2, mc)), p3 = ex2(fmaf(s[3], SC2, mc));
            l += (p0 + p1) + (p2 + p3);
            *LDSP(v2u, L_P + ph * 8192 + w * 2048 + 8 * lane + 512 * j) = v2u{cvtpk(p0, p1), cvtpk(p2, p3)};
          }
          l += dpp<XOR1>(l); l += dpp<XOR2>(l); l += dpp<HMIR>(l); l = x32_sum(l);
          const float la = x16_sum(l + dpp<ROR8>(l));
          if ((lane & 39) == 0) *LDSP(f32x2, L_ML + ph * 128 + w * 32 + lane) = f32x2{M, ph == 1 ? l : la};
          lds_inc(lds, L_MLC, lane);
        }
        mx = -3.0e38f; sa = sa0;
#pragma unroll
        for (int m = 0; m < 4; ++m) q[m] = *LDSP(v4u, L_QIMG + 256 + (n & 3) * 256 + g * 16 + 64 * m);
      }
    }
    {
      const unsigned Z = 0x0C0C0C0Cu;
      const unsigned el0 = jj == 0 ? 0x0C0C0100u : jj == 1 ? 0x01000C0Cu : Z, el1 = jj == 2 ? 0x0C0C0100u : jj == 3 ? 0x01000C0Cu : Z;
      const unsigned eh0 = jj == 0 ? 0x0C0C0302u : jj == 1 ? 0x03020C0Cu : Z, eh1 = jj == 2 ? 0x0C0C0302u : jj == 3 ? 0x03020C0Cu : Z;
      const int vb = g * 1024 + n * 16;
#pragma unroll 1
      for (int ph = 1; ph >= 0; --ph) {
        if (ph == 0) {
          lds_wait_ge<1>(lds, L_MLC, 8u);
          f32x2 ml[4];
#pragma unroll
          for (int u = 0; u < 4; ++u) ml[u] = *LDSP(f32x2, L_ML + u * 32);
          const float M = fmaxf(fmaxf(ml[0][0], ml[1][0]), fmaxf(ml[2][0], ml[3][0]));
          float L = 0.f;
#pragma unroll
          for (int u = 0; u < 4; ++u) L = fmaf(ex2((ml[u][0] - M) * SC2), ml[u][1], L);
          *LDSP(float, L_WGT + 64 + 4 * w) = ex2(((*LDSP(f32x2, L_ML + w * 32))[0] - M) * SC2) / L;
        }
        const int tend = ph == 1 ? 192 : NSL, pstep = ph == 1 ? 256 : 64;
        int pa = L_P + ph * 8192 + w * 2048 + (ph == 1 ? (n & 3) * 64 : 0) + g * 16;
        f32x4 a0 = {0, 0, 0, 0}, a1 = {0, 0, 0, 0};
#pragma unroll 1
        while (t < tend) {
          const v4u pp = *LDSP(v4u, pa); pa += pstep;
          v2u bf[8];
#pragma unroll
          for (int i = 0; i < 8; ++i) { const unsigned pk = pp[i >> 1]; bf[i] = v2u{__builtin_amdgcn_perm(pk, pk, (i & 1) ? eh0 : el0), __builtin_amdgcn_perm(pk, pk, (i & 1) ? eh1 : el1)}; }
#pragma unroll
          for (int i = 0; i < 8; ++i) asm volatile("" : "+v"(bf[i]));
          SLOT_WAIT();
          v4u T[8];
#pragma unroll
          for (int i = 0; i < 8; ++i) T[i] = *LDSP(v4u, slot + vb + (i >> 2) * 4096 + (i & 3) * 256);
          SLOT_DONE();
#pragma unroll
          for (int i = 0; i < 8; ++i) { a0 = mfma16(v2u{T[i][0], T[i][1]}, bf[i], a0); a1 = mfma16(v2u{T[i][2], T[i][3]}, bf[i], a1); }
        }
        if ((n & 3) == 0) *LDSP(v4u, L_HREC + 4096 + w * 256 + jj * 64 + g * 16) = v4u{cvtpk(a0[0], a1[0]), cvtpk(a0[1], a1[1]), cvtpk(a0[2], a1[2]), cvtpk(a0[3], a1[3])};
        if (ph == 1) {
          const int ro = sw + (n & 3) * 512 + jj * 128 + g * 32;
          *LDSP(f32x4, ro) = f32x4{a0[0], a1[0], a0[1], a1[1]};
          *LDSP(f32x4, ro + 16) = f32x4{a0[2], a1[2], a0[3], a1[3]};
        }
        lds_inc(lds, ph == 1 ? L_BCNT : L_RCNT + 4, lane);
      }
    }
    if (w == 3) { STAMP(3); STAMP_OUT(2, 4); }
  } else if (wave >= 6) {
    const int w = wave - 6;
#pragma unroll 1
    for (int mat = 0; mat < 2; ++mat) {
      if (mat) lds_wait_ge<16>(lds, L_FULL, 32u);
      const __amdgpu_buffer_rsrc_t rw = mk((mat ? wqb : wqa) + (size_t)(16 * jw + w) * 4096 + xcd * 512, (C::DBG & (8 | 2)) ? 0 : 13 * 8192);
      v4u d[4];
#pragma unroll
      for (int i = 0; i < 4; ++i) { d[i] = __builtin_amdgcn_raw_buffer_load_b128(rw, lane * 16, ((i & 1) * 8 + (i >> 1) * 4) * 8192, 0);
        if (C::PF_SER && mat) { __builtin_amdgcn_sched_barrier(0); __builtin_amdgcn_s_sleep(C::PF_SER); __builtin_amdgcn_sched_barrier(0); } }
      if (!mat) { __builtin_amdgcn_sched_barrier(0); START_BARRIER(); __builtin_amdgcn_sched_barrier(0); }
#pragma unroll
      for (int i = 0; i < 4; ++i) asm volatile("" :: "v"(d[i]));
      if (C::DBG & 8) return;
    }
    if (C::DBG & 16) return;
    const __amdgpu_buffer_rsrc_t ry = mk(ws + W_YA, (C::DBG & 1024) ? 0 : W_END - W_YA);
    const int yo = ((b * 32 + h) * 512 + 128 * w + 8 * n + 2 * g) * 8;
    lds_wait_ge<16>(lds, L_FULL, 72u);
#pragma unroll 1
    for (int pass = 0; pass < 2; ++pass) {
      const __amdgpu_buffer_rsrc_t rw = mk((pass == 0 ? wob : woa) + (size_t)xcd * 512 * 512, (C::DBG & 1) ? 0 : 512 * 1024);
      if (C::WO_PF) {
        v4u d[4];
#pragma unroll
        for (int i = 0; i < 4; ++i) { d[i] = __builtin_amdgcn_raw_buffer_load_b128(rw, (2 * b + (i >> 1) + 16 * (i & 1)) * 4096 + lane * 16, sp * 131072 + w * 1024, 0);
          if (C::PF_SER && (i & 1)) { __builtin_amdgcn_sched_barrier(0); __builtin_amdgcn_s_sleep(2 * C::PF_SER); __builtin_amdgcn_sched_barrier(0); } }
#pragma unroll
        for (int i = 0; i < 4; ++i) asm volatile("" :: "v"(d[i]));
        lds_wait_ge<16>(lds, L_FULL, lds_ld(lds, L_FULL) + C::WO_PF_GAP);
      }
      const int wv = (pass == 0 ? w * 131072 + sp * 256 : sp * 131072 + w * 256) + g * 32768 + n * 16;
      v4u F[4][8];
#pragma unroll
      for (int s = 0; s < 4; ++s) {
#pragma unroll
        for (int jp = 0; jp < 4; ++jp) {
          const v4u w0 = __builtin_amdgcn_raw_buffer_load_b128(rw, wv, (8 * jp + s) * 1024, 0);
          const v4u w1 = __builtin_amdgcn_raw_buffer_load_b128(rw, wv, (8 * jp + 4 + s) * 1024, 0);
#pragma unroll
          for (int d = 0; d < 4; ++d) {
            unsigned lo = __builtin_amdgcn_perm(w1[d], w0[d], 0x05040100u), hi = __builtin_amdgcn_perm(w1[d], w0[d], 0x07060302u);
            asm volatile("" : "+v"(lo), "+v"(hi));
            F[s][2 * d][jp] = lo; F[s][2 * d + 1][jp] = hi;
          }
          __builtin_amdgcn_sched_barrier(0);
          if (C::WO_SLEEP) __builtin_amdgcn_s_sleep(C::WO_SLEEP);
          __builtin_amdgcn_sched_barrier(0);
        }
#pragma unroll
        for (int c = 0; c < 8; ++c) asm volatile("" : "+v"(F[s][c]));
      }
      const int hr = pass == 0 ? w : 4;
      lds_wait_ge<1>(lds, L_RCNT + 4 * pass, 4u);
      v4u of[4];
#pragma unroll
      for (int s = 0; s < 4; ++s) of[s] = *LDSP(v4u, L_HREC + hr * 1024 + (n & 3) * 256 + s * 64 + g * 16);
      const unsigned tag = *LDSP(unsigned, L_TAG);
      const f32x4 wt = *LDSP(f32x4, L_WGT + hr * 16);
      f32x4 da = {0, 0, 0, 0}, dc = {0, 0, 0, 0};
      __builtin_amdgcn_sched_barrier(0);
#pragma unroll
      for (int cp = 0; cp < 4; ++cp) {
        f32x4 d0 = {0, 0, 0, 0}, d1 = {0, 0, 0, 0};
#pragma unroll
        for (int s = 0; s < 4; ++s) { d0 = mfma32(of[s], F[s][2 * cp], d0); d1 = mfma32(of[s], F[s][2 * cp + 1], d1); }
#pragma unroll
        for (int r = 0; r < 4; ++r) { da[r] = (cp == 0 || g == cp) ? d0[r] : da[r]; dc[r] = (cp == 0 || g == cp) ? d1[r] : dc[r]; }
      }
#pragma unroll
      for (int i = 0; i < 18; ++i) __builtin_amdgcn_sched_group_barrier(0x8, 1, 0);
#pragma unroll
      for (int i = 0; i < 14; ++i) { __builtin_amdgcn_sched_group_barrier(0x8, 1, 0); __builtin_amdgcn_sched_group_barrier(0x2, 1, 0); }
      const float ya = fmaf(wt[0], da[0], wt[1] * da[1]) + fmaf(wt[2], da[2], wt[3] * da[3]), yc = fmaf(wt[0], dc[0], wt[1] * dc[1]) + fmaf(wt[2], dc[2], wt[3] * dc[3]);
      st_sc1(v4u{__float_as_uint(ya), tag, __float_as_uint(yc), tag}, ry, pass == 0 ? OOB : yo, 0);
      if (pass == 0) *LDSP(f32x2, L_YS + w * 512 + 8 * lane) = f32x2{ya, yc};
      lds_inc(lds, L_YPUB, lane);
      if (w == 0) STAMP(4 + pass);
    }
    if (w == 0) STAMP_OUT(4, 6);
  } else {
    if (C::DBG & 8) { START_BARRIER(); return; }
    unsigned G;
    asm volatile("global_load_dword %0, %1, %2 sc1" : "=v"(G) : "v"(W_EPOCH), "s"(ws) : "memory");
    const int cb = xcd * 32 + jw;
    const __amdgpu_buffer_rsrc_t rs = mk(ws, W_END);
    unsigned tag = 0;
    const int ta = L_P + (8 * g + (n >> 2)) * 32 + (n & 3) * 8;
    int it = -1; OPAQUE_S(it);
    v4u xf[16];
#pragma unroll 1
    for (; it < 2; ++it) {
      if (it != 0) {
        if (it == 1) lds_wait_ge<16>(lds, L_FULL, 52u);
        const v4i rx = mksrd(it == 1 ? xb : xa, (C::DBG & 128) ? 0 : 8 * 512 * 2);
#pragma unroll
        for (int s = 0; s < 16; ++s) asm volatile("buffer_load_dwordx4 %0, %1, %2, 0 offen offset:%3" : "+v"(xf[s]) : "v"(n * 1024 + 16 * g), "s"(rx), "i"(64 * s) : "memory");
      }
      if (it < 0) START_BARRIER();
      if (it >= 0) {
        if (it == 0) lds_wait_ge<1>(lds, L_FULL, C::T_SLICE);
        const v4i rw = mksrd((it ? wqb : wqa) + cb * 16, (C::DBG & 32) ? 0 : 512 * 8192 - cb * 32);
        const unsigned vo = (lane >> 1) * 8192 + (lane & 1) * 16;
#pragma unroll
        for (int i = 0; i < 16; ++i) {
          if (C::SL_NT) asm volatile("s_mov_b32 m0, %0\n\ts_nop 0\n\tbuffer_load_dwordx4 %1, %2, %3 offen nt lds" :: "s"(lds0 + L_P + i * 1024), "v"(vo), "s"(rw), "s"(i * 32 * 8192) : "memory");
          else asm volatile("s_mov_b32 m0, %0\n\ts_nop 0\n\tbuffer_load_dwordx4 %1, %2, %3 offen lds" :: "s"(lds0 + L_P + i * 1024), "v"(vo), "s"(rw), "s"(i * 32 * 8192) : "memory");
          if (it) __builtin_amdgcn_s_sleep(C::WQB_SLEEP); else if (C::WQA_SLEEP) __builtin_amdgcn_s_sleep(C::WQA_SLEEP);
        }
        asm volatile("s_waitcnt vmcnt(0)\n\tv_readfirstlane_b32 %0, %1\n\ts_nop 1" : "=s"(tag) : "v"(G) : "memory");
        tag += 1u;
        if (it == 0) STAMP(6);
      }
#pragma unroll
      for (int s = 0; s < 16; ++s) asm volatile("" : "+v"(xf[s]));
      f32x4 d0 = {0, 0, 0, 0}, d1 = {0, 0, 0, 0};
      {
        v2u t[16][2];
#pragma unroll
        for (int s = 0; s < 16; ++s)
#pragma unroll
          for (int u = 0; u < 2; ++u) t[s][u] = __builtin_bit_cast(v2u, __builtin_amdgcn_ds_read_tr16_b64_v4i16(LDSP(s16x4, ta + s * 1024 + u * 128)));
        __builtin_amdgcn_sched_barrier(0);
#pragma unroll
        for (int s = 0; s < 16; s += 2) {
          d0 = mfma32(v4u{t[s][0][0], t[s][0][1], t[s][1][0], t[s][1][1]}, xf[s], d0);
          d1 = mfma32(v4u{t[s + 1][0][0], t[s + 1][0][1], t[s + 1][1][0], t[s + 1][1][1]}, xf[s + 1], d1);
        }
      }
      d0 += d1;
      st_sc1(v4u{cvtpk(d0[0], d0[1]), tag, cvtpk(d0[2], d0[3]), tag}, rs, (it >= 0 && n < 8 && !(C::DBG & 64)) ? W_QG + (it * 256 + cb) * 512 + n * 64 + g * 16 : OOB, 0);
      if (it == 0) STAMP(7);
      if (it < 0) continue;
      lds_wait_ge<4>(lds, L_FULL, it ? 104u : C::T_GATHER);
      const int go = (C::DBG & (64 | 256)) ? OOB : it == 0 ? (lane < 32 ? W_QG + (h * 8 + (lane >> 2)) * 512 + b * 64 + (lane & 3) * 16 : OOB) : W_QG + (256 + xcd * 32 + (lane >> 2)) * 512 + b * 64 + (lane & 3) * 16;
      const u64 need = (C::DBG & (64 | 256)) ? 0ull : it == 0 ? 0xFFFFFFFFull : ~0ull;
#pragma unroll 1
      for (unsigned spn = 0; spn < SPIN_CAP; ++spn) {
        const v4u v = ld_sc1(rs, go, 0), u = ld_sc1(rs, it == 1 ? go : OOB, 16 * 512);
        if (it == 1 || lane < 32) *LDSP(v2u, L_QIMG + (it == 1 ? 256 : 0) + 8 * lane) = v2u{v[0], v[2]};
        if (it == 1) *LDSP(v2u, L_QIMG + 768 + 8 * lane) = v2u{u[0], u[2]};
        if ((__builtin_amdgcn_ballot_w64(v[1] != tag || v[3] != tag || (it == 1 && (u[1] != tag || u[3] != tag))) & need) == 0) break;
        __builtin_amdgcn_s_sleep(24);
      }
      CBAR();
      lds_st(lds, L_QRDY + 4 * it, 1u);
      if (it == 0) STAMP(8);
    }
    lds_st(lds, L_TAG, tag);
    lds_wait_ge<16>(lds, L_BCNT, 4u);
    if (!(C::DBG & 512)) {
      f32x2 ml[4]; f32x4 ac[4][2];
#pragma unroll
      for (int u = 0; u < 4; ++u) { ml[u] = *LDSP(f32x2, L_ML + 128 + u * 32 + g * 8); ac[u][0] = *LDSP(f32x4, L_S + u * 4096 + 32 * lane); ac[u][1] = *LDSP(f32x4, L_S + u * 4096 + 32 * lane + 16); }
      const float M = fmaxf(fmaxf(ml[0][0], ml[1][0]), fmaxf(ml[2][0], ml[3][0]));
      float L = 0.f; f32x4 o0 = {0, 0, 0, 0}, o1 = {0, 0, 0, 0};
#pragma unroll
      for (int u = 0; u < 4; ++u) { const float f = ex2((ml[u][0] - M) * SC2); L = fmaf(f, ml[u][1], L); o0 += f * ac[u][0]; o1 += f * ac[u][1]; }
      const float inv = 1.f / L; o0 *= inv; o1 *= inv;
      st_sc1(v4u{cvtpk(o0[0], o0[1]), cvtpk(o0[2], o0[3]), cvtpk(o1[0], o1[1]), cvtpk(o1[2], o1[3])}, rs, W_PO + wg * 1024 + 16 * lane, 0);
      asm volatile("s_waitcnt vmcnt(0)" ::: "memory");
      st_sc1(v4u{__float_as_uint(M), tag, __float_as_uint(L), tag}, rs, n == 0 ? W_ML + wg * 128 + g * 16 : OOB, 0);
    }
    lds_wait_ge<16>(lds, L_FULL, 192u + C::T_MLPOLL);
    v4u mlv = {0, 0, 0, 0};
#pragma unroll 1
    for (unsigned spn = 0; spn < SPIN_CAP; ++spn) {
      if (C::DBG & 512) break;
      mlv = ld_sc1(rs, lane < 16 ? W_ML + (xcd + 8 * (4 * b + (lane & 3))) * 128 + (lane >> 2) * 16 : OOB, 0);
      if ((__builtin_amdgcn_ballot_w64(mlv[1] != tag || mlv[3] != tag) & 0xFFFFu) == 0u) break;
      __builtin_amdgcn_s_sleep(64);
    }
    __builtin_amdgcn_fence(__ATOMIC_ACQUIRE, "wavefront");
    {
      v4u p[4];
#pragma unroll
      for (int u = 0; u < 4; ++u) p[u] = ld_sc1(rs, (C::DBG & 512) ? OOB : W_PO + (xcd + 32 * b) * 1024 + 16 * lane, u * 8192);
      const float m = __uint_as_float(mlv[0]);
      float M = fmaxf(m, dpp<XOR1>(m)); M = fmaxf(M, dpp<XOR2>(M));
      const float e = ex2((m - M) * SC2) * __uint_as_float(mlv[2]);
      float L = e + dpp<XOR1>(e); L += dpp<XOR2>(L);
      if (lane < 16) *LDSP(float, L_WGT + 4 * lane) = e / L;
#pragma unroll
      for (int u = 0; u < 4; ++u) *LDSP(v4u, L_HREC + g * 1024 + u * 256 + n * 16) = p[u];
      CBAR(); lds_st(lds, L_RCNT, 4u);
    }
    STAMP(9);
    if (C::DBG & (16 | 1024)) return;
    lds_wait_ge<16>(lds, L_YPUB, 4u);
    {
      const f32x2 yb = (*LDSP(f32x2, L_YS + 8 * lane) + *LDSP(f32x2, L_YS + 512 + 8 * lane)) + (*LDSP(f32x2, L_YS + 1024 + 8 * lane) + *LDSP(f32x2, L_YS + 1536 + 8 * lane));
      lds_wait_ge<8>(lds, L_FULL, NSL - C::T_YB);
      st_sc1(v4u{__float_as_uint(yb[0]), tag, __float_as_uint(yb[1]), tag}, rs, W_YB + wg * 1024 + (8 * n + 2 * g) * 8, 0);
    }
#pragma unroll 1
    for (int pass = 0; pass < 2; ++pass) {
      if (pass == 0) lds_wait_ge<8>(lds, L_FULL, NSL - 1); else lds_wait_ge<1>(lds, L_YPUB, 8u);
      const int yo = pass == 0 ? W_YB + ((lane >> 3) + 8 * (4 * b + (h >> 3))) * 1024 + (h & 7) * 128 + (lane & 7) * 16
                               : W_YA + (b * 32 * 512 + (lane >> 3) * 512 + 16 * h + 2 * (lane & 7)) * 8;
      v4u y[4]; bool ok[4];
#pragma unroll
      for (int i = 0; i < 4; ++i) { y[i] = v4u{0, 0, 0, 0}; ok[i] = pass == 0 && i > 0; }
#pragma unroll 1
      for (unsigned spn = 0; spn < SPIN_CAP; ++spn) {
        v4u z[4];
#pragma unroll
        for (int i = 0; i < 4; ++i) z[i] = ld_sc1(rs, ok[i] ? OOB : yo, i * 8 * 512 * 8);
        bool all = true;
#pragma unroll
        for (int i = 0; i < 4; ++i) { if (!ok[i]) { y[i] = z[i]; ok[i] = z[i][1] == tag && z[i][3] == tag; } all &= ok[i]; }
        if (__builtin_amdgcn_ballot_w64(!all) == 0) break;
        __builtin_amdgcn_s_sleep(C::Y_SLEEP);
      }
      float s0 = (__uint_as_float(y[0][0]) + __uint_as_float(y[1][0])) + (__uint_as_float(y[2][0]) + __uint_as_float(y[3][0]));
      float s1 = (__uint_as_float(y[0][2]) + __uint_as_float(y[1][2])) + (__uint_as_float(y[2][2]) + __uint_as_float(y[3][2]));
      s0 += dpp<ROR8>(s0); s1 += dpp<ROR8>(s1);
      s0 = x32_sum(x16_sum(s0)); s1 = x32_sum(x16_sum(s1));
      if (lane < 8) *(GAS unsigned*)(out + b * 1024 + (pass == 0 ? 512 : 0) + 16 * h + 2 * lane) = cvtpk(s0, s1);
      STAMP(10 + pass);
    }
    if (wg == 0 && lane == 0) __hip_atomic_store((GAS unsigned*)(ws + W_EPOCH), tag, RLX_AGENT);
    STAMP_OUT(6, 12);
  }
}

template <class C>
static void launch_cfg(void* const* d_in, void* d_out, void* d_ws, hipStream_t stream) {
  (void)hipFuncSetAttribute((const void*)fused_decode<C>, hipFuncAttributeMaxDynamicSharedMemorySize, LDS_BYTES);
  hipLaunchKernelGGL(fused_decode<C>, dim3(256), dim3(NTHR), LDS_BYTES, stream,
                     (const u16*)d_in[0], (const u16*)d_in[1], (const u16*)d_in[2], (const u16*)d_in[3], (const u16*)d_in[4],
                     (const u16*)d_in[5], (const u16*)d_in[6], (const u16*)d_in[7], (const u16*)d_in[8], (const u16*)d_in[9],
                     (u16*)d_out, (unsigned char*)d_ws);
}
extern "C" void kernel_launch(void* const* d_in, const int* in_sizes, int n_in, void* d_out, int out_size, void* d_ws, size_t ws_size, hipStream_t stream) {
  (void)in_sizes; (void)n_in; (void)out_size; (void)ws_size;
  launch_cfg<CfgDefault>(d_in, d_out, d_ws, stream);
}
```

```cpp
#include <hip/hip_runtime.h>
#include <hip/hip_bf16.h>
#include <stdint.h>

typedef unsigned v4u __attribute__((ext_vector_type(4)));
typedef unsigned v2u __attribute__((ext_vector_type(2)));
typedef int v4i __attribute__((ext_vector_type(4)));
typedef float f32x4 __attribute__((ext_vector_type(4)));
typedef float f32x2 __attribute__((ext_vector_type(2)));
typedef short s16x4 __attribute__((ext_vector_type(4)));
typedef __bf16 bf16x8 __attribute__((ext_vector_type(8)));
typedef __bf16 bf16x2 __attribute__((ext_vector_type(2)));
typedef unsigned short u16;
typedef unsigned long long u64;
#define GAS __attribute__((address_space(1)))
#define LAS __attribute__((address_space(3)))
#define RLX_AGENT __ATOMIC_RELAXED, __HIP_MEMORY_SCOPE_AGENT

struct CfgDefault {
  static constexpr int NFL = 20;
  static constexpr int PACE = 2900, PACE2 = 2800;
  static constexpr int UP0 = 60, DN0 = 40;
  static constexpr int PACE0 = 20;
  static constexpr int STEP = 50;
  static constexpr int NDN = 1, NUP = 2, USTEP = 80, ROTA = 0, ROTB = 0, PF_SER = 0;
  static constexpr int ANCH = 15, MARGIN = 20;
  static constexpr int T_YB = 12, Y_SLEEP = 3;
  static constexpr int SL_NT = 1, WO_PF = 1, WO_PF_AUX = 0, WO_PF_GAP = 12;
  static constexpr int MUL = 11;
  static constexpr int WO_AUX = 0;
  static constexpr int WO_SLEEP = 0;
  static constexpr int PF_AUX = 0;
  static constexpr int T_SLICE = 3, T_GATHER = 15, T_MLPOLL = 24;
  static constexpr int WQA_SLEEP = 0, WQB_SLEEP = 12;
  static constexpr int PARK = 4;
  static constexpr int DBG = 0;
};

constexpr int SK = 4096, DH = 128, NH = 32, HKV = 8;
constexpr float SC2 = 0.088388347648318440f * 1.4426950408889634f;
constexpr int NWAVE = 10, NTHR = NWAVE * 64;
constexpr int NSL = 320;
constexpr int RING = 15, SLOT = 8192;
constexpr int L_FULL = 0, L_QRDY = 4  , L_BCNT = 12, L_DONE = 16, L_RCNT = 32  , L_TAG = 40, L_YPUB = 44, L_MLC = 48, L_IMPL = 52, L_BASE = 56;
constexpr int L_QIMG = 64;
constexpr int L_WGT = L_QIMG + 1280;
constexpr int L_ML = L_WGT + 128;
constexpr int L_HREC = L_ML + 256;
constexpr int L_YS = 8192 + 16384 + 8192;
constexpr int L_S = 8192;
constexpr int L_P = L_S + 16384;
constexpr int L_RING = L_P + 16384;
constexpr int LDS_BYTES = 160 * 1024;
static_assert(L_HREC % 16 == 0 && L_HREC + 5 * 1024 <= 7168 && L_RING + RING * SLOT <= LDS_BYTES, "lds map");
constexpr int W_EPOCH = 0;
constexpr int W_ML = 4096;
constexpr int W_AN = 49152;
constexpr int W_QG = 65536;
constexpr int W_PO = W_QG + 2 * 256 * 512;
constexpr int W_YB = W_PO + 256 * 1024;
constexpr int W_YA = W_YB + 256 * 1024;
constexpr int W_END = W_YA + 8 * 32 * 512 * 8;
constexpr size_t W_STAMP = 64u << 20;
constexpr unsigned SPIN_CAP = 1u << 21;
constexpr int OOB = 0x7FFFFF00;

__device__ __forceinline__ unsigned cvtpk(float lo, float hi) { return __builtin_bit_cast(unsigned, __builtin_convertvector(f32x2{lo, hi}, bf16x2)); }
__device__ __forceinline__ __amdgpu_buffer_rsrc_t mk(const void* p, unsigned bytes) { return __builtin_amdgcn_make_buffer_rsrc((void*)p, 0, bytes, 0x00020000); }
__device__ __forceinline__ v4i mksrd(const void* p, unsigned bytes) { return v4i{(int)(uintptr_t)p, (int)((uintptr_t)p >> 32), (int)bytes, 0x00020000}; }
__device__ __forceinline__ f32x4 mfma32(v4u a, v4u b, f32x4 c) { return __builtin_amdgcn_mfma_f32_16x16x32_bf16(__builtin_bit_cast(bf16x8, a), __builtin_bit_cast(bf16x8, b), c, 0, 0, 0); }
__device__ __forceinline__ f32x4 mfma16(v2u a, v2u b, f32x4 c) { return __builtin_amdgcn_mfma_f32_16x16x16bf16_1k(__builtin_bit_cast(s16x4, a), __builtin_bit_cast(s16x4, b), c, 0, 0, 0); }
__device__ __forceinline__ float ex2(float x) { return __builtin_amdgcn_exp2f(x); }
__device__ __forceinline__ v4u ld_sc1(__amdgpu_buffer_rsrc_t r, int vo, int so) { return __builtin_amdgcn_raw_buffer_load_b128(r, vo, so, 16); }
__device__ __forceinline__ void st_sc1(v4u v, __amdgpu_buffer_rsrc_t r, int vo, int so) { __builtin_amdgcn_raw_buffer_store_b128(v, r, vo, so, 16); }
template <int CTRL> __device__ __forceinline__ float dpp(float x) { return __builtin_bit_cast(float, __builtin_amdgcn_mov_dpp(__builtin_bit_cast(int, x), CTRL, 0xf, 0xf, true)); }
constexpr int XOR1 = 0xB1, XOR2 = 0x4E, HMIR = 0x141, ROR8 = 0x128;
__device__ __forceinline__ float x16_sum(float x) { auto s = __builtin_amdgcn_permlane16_swap(__float_as_uint(x), __float_as_uint(x), false, false); return __uint_as_float(s[0]) + __uint_as_float(s[1]); }
__device__ __forceinline__ float x32_sum(float x) { auto s = __builtin_amdgcn_permlane32_swap(__float_as_uint(x), __float_as_uint(x), false, false); return __uint_as_float(s[0]) + __uint_as_float(s[1]); }
__device__ __forceinline__ float xg_max(float x) {
  auto s = __builtin_amdgcn_permlane16_swap(__float_as_uint(x), __float_as_uint(x), false, false);
  x = fmaxf(__uint_as_float(s[0]), __uint_as_float(s[1]));
  auto t = __builtin_amdgcn_permlane32_swap(__float_as_uint(x), __float_as_uint(x), false, false);
  return fmaxf(__uint_as_float(t[0]), __uint_as_float(t[1]));
}
#define CBAR() asm volatile("" ::: "memory")
#define OPAQUE_S(x) asm volatile("" : "+s"(x))
#define LDSP(T, off) ((LAS T*)(lds + (off)))
__device__ __forceinline__ unsigned lds_ld(LAS unsigned char* lds, int off) { return __builtin_amdgcn_readfirstlane(*(volatile LAS unsigned*)(lds + off)); }
__device__ __forceinline__ void lds_st(LAS unsigned char* lds, int off, unsigned v) { *(volatile LAS unsigned*)(lds + off) = v; }
template <int SLP> __device__ __forceinline__ unsigned lds_wait_ge(LAS unsigned char* lds, int off, unsigned want) {
  unsigned v = lds_ld(lds, off);
#pragma unroll 1
  for (unsigned sp = 0; v < want && sp < SPIN_CAP * 8; ++sp) { __builtin_amdgcn_s_sleep(SLP); v = lds_ld(lds, off); }
  CBAR();
  return v;
}
__device__ __forceinline__ void lds_inc(LAS unsigned char* lds, int off, int lane) {
  CBAR();
  if (lane == 0) __hip_atomic_fetch_add(LDSP(unsigned, off), 1u, __ATOMIC_RELAXED, __HIP_MEMORY_SCOPE_WORKGROUP);
  CBAR();
}
#define STAMP(i) do {} while (0)
#define STAMP_OUT(lo, hi) do {} while (0)

template <int NFL> __device__ __forceinline__ void dma4(unsigned m0v, unsigned v0, unsigned v1, unsigned v2, unsigned v3, v4i srd, unsigned so) {
  asm volatile("s_mov_b32 m0, %0\n\t"
               "s_waitcnt vmcnt(%7)\n\tbuffer_load_dwordx4 %1, %5, %6 offen nt lds\n\t"
               "s_waitcnt vmcnt(%7)\n\tbuffer_load_dwordx4 %2, %5, %6 offen offset:1024 nt lds\n\t"
               "s_waitcnt vmcnt(%7)\n\tbuffer_load_dwordx4 %3, %5, %6 offen offset:2048 nt lds\n\t"
               "s_waitcnt vmcnt(%7)\n\tbuffer_load_dwordx4 %4, %5, %6 offen offset:3072 nt lds"
               :: "s"(m0v), "v"(v0), "v"(v1), "v"(v2), "v"(v3), "s"(srd), "s"(so), "i"(NFL - 1) : "memory");
}

template <class C>
__global__ void __launch_bounds__(NTHR, 1) fused_decode(const u16* xa, const u16* ak, const u16* av, const u16* wqa, const u16* woa,
                                                        const u16* xb, const u16* bk, const u16* bv, const u16* wqb, const u16* wob,
                                                        u16* out, unsigned char* ws) {
  constexpr int NFL = C::NFL, PARK = C::PARK;
  extern __shared__ __attribute__((aligned(16))) unsigned char lds_raw[];
  LAS unsigned char* lds = (LAS unsigned char*)lds_raw;
  const int wg = blockIdx.x, lane = threadIdx.x & 63, wave = __builtin_amdgcn_readfirstlane(threadIdx.x >> 6);
  const int xcd = wg & 7, jw = wg >> 3, b = jw >> 2, sp = jw & 3, h = 4 * xcd + sp;
  const int g = lane >> 4, n = lane & 15;
  const unsigned lds0 = (unsigned)(uintptr_t)lds;
  if (threadIdx.x < 16) *(volatile LAS unsigned*)(lds + 4 * lane) = 0u;
  asm volatile("s_waitcnt lgkmcnt(0)" :: "s"(wqb), "s"(wob), "s"(out), "s"(ws), "s"(xa) : "memory");
#define START_BARRIER() do { __builtin_amdgcn_s_barrier(); CBAR(); } while (0)

  if (wave == 0) {
    __builtin_amdgcn_s_setprio(3);
    const size_t ua = (size_t)(b * NH + h) * SK * DH, ub = (size_t)(b * HKV + xcd) * SK * DH;
    const unsigned k0 = g * 256 + ((n ^ g) * 16), vv = lane * 16;
    unsigned slot = lds0 + L_RING;
    int limit = RING;
    constexpr int NMAX = C::PACE ? NFL + C::NUP : NFL;
    constexpr int LAG0 = (NMAX + 4 + 7) / 8, LAG1 = (NMAX + 7) / 8;
    static_assert(NMAX >= 17 && NMAX <= 32, "publish lag, ladder");
    u64 now = __builtin_amdgcn_s_memrealtime();
    unsigned base = (unsigned)now + C::PACE0, due = -(4 * C::PACE / 100);
    now = base + due;
    STAMP(0);
    int t = 0;
#pragma unroll 1
    for (int ph = 0; ph < 4; ++ph) {
      const v4i rs = mksrd(ph == 0 ? ak + ua : ph == 1 ? bk + ub : ph == 2 ? bv + ub : av + ua, SK * 256);
      const int tend = ph == 0 ? 128 : ph == 1 ? 160 : ph == 2 ? 192 : NSL;
      const unsigned v0 = ph < 2 ? k0 : vv, v1 = ph < 2 ? k0 ^ 64 : vv, v2 = ph < 2 ? k0 ^ 128 : vv, v3 = ph < 2 ? k0 ^ 192 : vv;
      unsigned r = ((ph == 1 || ph == 2) ? sp * 32 * C::MUL + (b * HKV + xcd) * C::ROTB : (b * NH + h) * C::ROTA) & 127u;
#pragma unroll 1
      while (t < tend) {
        if (!(C::DBG & 4) && t + 3 >= limit) {
#pragma unroll 1
          for (unsigned spn = 0; spn < SPIN_CAP * 8; ++spn) {
            const v4u d = *(volatile LAS v4u*)(lds + L_DONE);
            if (C::ANCH) { const unsigned nb = lds_ld(lds, L_BASE); base = nb ? nb : base; }
            const int c = min(min(4 * (int)d[0], 4 * (int)d[1] + 1), min(4 * (int)d[2] + 2, 4 * (int)d[3] + 3));
            limit = __builtin_amdgcn_readfirstlane(c) + RING;
            if (t + 3 < limit) break;
            __builtin_amdgcn_s_sleep(1);
          }
        }
#define RUN4(N_) do { _Pragma("unroll 1") for (const int te = t + 4; t < te; ++t) { \
          const unsigned so = (((r & 0x70u) << 1) | (r & 15u)) << 12;       \
          if ((N_) == NFL && t == 2) START_BARRIER(); \
          dma4<N_>(slot, v0, v1, v2, v3, rs, so); \
          if (LAG0 == LAG1) lds_st(lds, L_FULL, (unsigned)max(t - LAG0 + 1, 0)); \
          dma4<N_>(slot + 4096, v0, v1, v2, v3, rs, so + 65536u); \
          if (LAG0 != LAG1) lds_st(lds, L_FULL, (unsigned)max(t - LAG1 + 1, 0)); \
          slot += SLOT; if (slot >= lds0 + L_RING + RING * SLOT) slot = lds0 + L_RING; \
          r = (r + C::MUL) & 127u; } } while (0)
        if (C::PACE == 0) RUN4(NFL);
        else {
          asm volatile("" : "+s"(now));
          if (C::ANCH) lds_st(lds, L_IMPL, (unsigned)now - due);
          const int lag = (int)((unsigned)now - due - base); due += ph == 0 ? 4 * C::PACE / 100 : 4 * C::PACE2 / 100;
          asm volatile("s_memrealtime %0" : "=s"(now));
          if (lag >= -C::DN0) {
            if (lag < C::UP0 || C::NUP == 0) RUN4(NFL); else if (lag < C::UP0 + C::USTEP || C::NUP == 1) RUN4(NFL + 1);
            else if (lag < C::UP0 + 2 * C::USTEP || C::NUP == 2) RUN4(NFL + 2); else RUN4(NFL + 3);
          } else if (lag >= -C::DN0 - C::STEP || C::NDN == 1) RUN4(NFL - 1);
          else if (lag >= -C::DN0 - 2 * C::STEP || C::NDN == 2) RUN4(NFL - 2); else RUN4(NFL - 3);
        }
      }
    }
    asm volatile("s_waitcnt vmcnt(24)" ::: "memory"); lds_st(lds, L_FULL, NSL - 3);
    asm volatile("s_waitcnt vmcnt(16)" ::: "memory"); lds_st(lds, L_FULL, NSL - 2);
    asm volatile("s_waitcnt vmcnt(8)" ::: "memory");  lds_st(lds, L_FULL, NSL - 1);
    asm volatile("s_waitcnt vmcnt(0)" ::: "memory");  lds_st(lds, L_FULL, NSL);
    STAMP(1); STAMP_OUT(0, 2); STAMP_OUT(17, 26);
  } else if (wave >= 2 && wave < 6) {
    START_BARRIER();
    if (C::DBG & 8) return;
    const int w = wave - 2, jj = n >> 2;
    int slot = L_RING + w * SLOT; unsigned full = 0, cnt = 0; int t = w;
#define SLOT_WAIT() do { if (full <= (unsigned)t) full = lds_wait_ge<1>(lds, L_FULL, (unsigned)t + 1u); CBAR(); } while (0)
#define SLOT_DONE() do { CBAR(); ++cnt; lds_st(lds, L_DONE + 4 * w, cnt);     \
        CBAR(); slot += 4 * SLOT; if (slot >= L_RING + RING * SLOT) slot -= RING * SLOT; t += 4; } while (0)
#define K_READ(T_) do { SLOT_WAIT(); _Pragma("unroll") for (int m_ = 0; m_ < 8; ++m_) T_[m_] = *LDSP(v4u, ((slot + kb) ^ (64 * (m_ & 3))) + (m_ >> 2) * 4096); SLOT_DONE(); } while (0)
    const int sw = L_S + w * 4096;
    {
      const int kb = n * 256 + ((g ^ n) * 16);
      v4u q[4];
      float mx = -3.0e38f;
      const int sa0 = sw + ((n & 3) * 8 + g * 2 + (jj & 1)) * 16;
      int sa = sa0;
#define K_MATH(T_, act_, step_) do { f32x4 s0 = {0.f, 0.f, 0.f, 0.f}, s1 = {0.f, 0.f, 0.f, 0.f}; \
        _Pragma("unroll") for (int m = 0; m < 4; ++m) { s0 = mfma32(T_[m], q[m], s0); s1 = mfma32(T_[4 + m], q[m], s1); }       \
        mx = fmaxf(fmaxf(fmaxf(fmaxf(s0[0], s0[1]), fmaxf(s0[2], s0[3])), fmaxf(fmaxf(s1[0], s1[1]), fmaxf(s1[2], s1[3]))), mx); \
        if (act_) *LDSP(f32x4, sa) = (jj & 1) ? s1 : s0; \
        sa += (step_); } while (0)
      {
        v4u P[PARK][8];
#pragma unroll
        for (int i = 0; i < PARK; ++i) K_READ(P[i]);
        lds_wait_ge<1>(lds, L_QRDY, 1u);
#pragma unroll
        for (int m = 0; m < 4; ++m) q[m] = *LDSP(v4u, L_QIMG + g * 16 + 64 * m);
        if (w == 0) STAMP(2);
#pragma unroll
        for (int i = 0; i < PARK; ++i) K_MATH(P[i], (n & 11) == 0, 128);
      }
#pragma unroll 1
      for (int ph = 0; ph < 2; ++ph) {
        const bool act = ph == 1 ? n < 8 : (n & 11) == 0;
        const int sstep = ph == 1 ? 512 : 128, tend = ph == 0 ? 128 : 160;
#pragma unroll 1
        while (t < tend) { v4u T[8]; K_READ(T); K_MATH(T, act, sstep); }
        {
          lds_wait_ge<1>(lds, L_QRDY + 4, 1u);
          const float Mh = xg_max(mx);
          const float M = __builtin_bit_cast(float, __builtin_amdgcn_ds_bpermute(((lane >> 3) & 3) * 4, __builtin_bit_cast(int, Mh)));
          const float mc = -M * SC2;
          float l = 0.f;
          CBAR();
#pragma unroll
          for (int j = 0; j < 4; ++j) {
            const f32x4 s = *LDSP(f32x4, sw + 16 * lane + 1024 * j);
            const float p0 = ex2(fmaf(s[0], SC2, mc)), p1 = ex2(fmaf(s[1], SC2, mc)), p2 = ex2(fmaf(s[2], SC2, mc)), p3 = ex2(fmaf(s[3], SC2, mc));
            l += (p0 + p1) + (p2 + p3);
            *LDSP(v2u, L_P + ph * 8192 + w * 2048 + 8 * lane + 512 * j) = v2u{cvtpk(p0, p1), cvtpk(p2, p3)};
          }
          l += dpp<XOR1>(l); l += dpp<XOR2>(l); l += dpp<HMIR>(l); l = x32_sum(l);
          const float la = x16_sum(l + dpp<ROR8>(l));
          if ((lane & 39) == 0) *LDSP(f32x2, L_ML + ph * 128 + w * 32 + lane) = f32x2{M, ph == 1 ? l : la};
          lds_inc(lds, L_MLC, lane);
        }
        mx = -3.0e38f; sa = sa0;
#pragma unroll
        for (int m = 0; m < 4; ++m) q[m] = *LDSP(v4u, L_QIMG + 256 + (n & 3) * 256 + g * 16 + 64 * m);
      }
    }
    {
      const unsigned Z = 0x0C0C0C0Cu;
      const unsigned el0 = jj == 0 ? 0x0C0C0100u : jj == 1 ? 0x01000C0Cu : Z, el1 = jj == 2 ? 0x0C0C0100u : jj == 3 ? 0x01000C0Cu : Z;
      const unsigned eh0 = jj == 0 ? 0x0C0C0302u : jj == 1 ? 0x03020C0Cu : Z, eh1 = jj == 2 ? 0x0C0C0302u : jj == 3 ? 0x03020C0Cu : Z;
      const int vb = g * 1024 + n * 16;
#pragma unroll 1
      for (int ph = 1; ph >= 0; --ph) {
        if (ph == 0) {
          lds_wait_ge<1>(lds, L_MLC, 8u);
          f32x2 ml[4];
#pragma unroll
          for (int u = 0; u < 4; ++u) ml[u] = *LDSP(f32x2, L_ML + u * 32);
          const float M = fmaxf(fmaxf(ml[0][0], ml[1][0]), fmaxf(ml[2][0], ml[3][0]));
          float L = 0.f;
#pragma unroll
          for (int u = 0; u < 4; ++u) L = fmaf(ex2((ml[u][0] - M) * SC2), ml[u][1], L);
          *LDSP(float, L_WGT + 64 + 4 * w) = ex2(((*LDSP(f32x2, L_ML + w * 32))[0] - M) * SC2) / L;
        }
        const int tend = ph == 1 ? 192 : NSL, pstep = ph == 1 ? 256 : 64;
        int pa = L_P + ph * 8192 + w * 2048 + (ph == 1 ? (n & 3) * 64 : 0) + g * 16;
        f32x4 a0 = {0, 0, 0, 0}, a1 = {0, 0, 0, 0};
#pragma unroll 1
        while (t < tend) {
          const v4u pp = *LDSP(v4u, pa); pa += pstep;
          v2u bf[8];
#pragma unroll
          for (int i = 0; i < 8; ++i) { const unsigned pk = pp[i >> 1]; bf[i] = v2u{__builtin_amdgcn_perm(pk, pk, (i & 1) ? eh0 : el0), __builtin_amdgcn_perm(pk, pk, (i & 1) ? eh1 : el1)}; }
#pragma unroll
          for (int i = 0; i < 8; ++i) asm volatile("" : "+v"(bf[i]));
          SLOT_WAIT();
          v4u T[8];
#pragma unroll
          for (int i = 0; i < 8; ++i) T[i] = *LDSP(v4u, slot + vb + (i >> 2) * 4096 + (i & 3) * 256);
          SLOT_DONE();
#pragma unroll
          for (int i = 0; i < 8; ++i) { a0 = mfma16(v2u{T[i][0], T[i][1]}, bf[i], a0); a1 = mfma16(v2u{T[i][2], T[i][3]}, bf[i], a1); }
        }
        if ((n & 3) == 0) *LDSP(v4u, L_HREC + 4096 + w * 256 + jj * 64 + g * 16) = v4u{cvtpk(a0[0], a1[0]), cvtpk(a0[1], a1[1]), cvtpk(a0[2], a1[2]), cvtpk(a0[3], a1[3])};
        if (ph == 1) {
          const int ro = sw + (n & 3) * 512 + jj * 128 + g * 32;
          *LDSP(f32x4, ro) = f32x4{a0[0], a1[0], a0[1], a1[1]};
          *LDSP(f32x4, ro + 16) = f32x4{a0[2], a1[2], a0[3], a1[3]};
        }
        lds_inc(lds, ph == 1 ? L_BCNT : L_RCNT + 4, lane);
      }
    }
    if (w == 3) { STAMP(3); STAMP_OUT(2, 4); }
  } else if (wave >= 6) {
    const int w = wave - 6;
#pragma unroll 1
    for (int mat = 0; mat < 2; ++mat) {
      if (mat) lds_wait_ge<16>(lds, L_FULL, 32u);
      const __amdgpu_buffer_rsrc_t rw = mk((mat ? wqb : wqa) + (size_t)(16 * jw + w) * 4096 + xcd * 512, (C::DBG & (8 | 2)) ? 0 : 13 * 8192);
      v4u d[4];
#pragma unroll
      for (int i = 0; i < 4; ++i) { d[i] = __builtin_amdgcn_raw_buffer_load_b128(rw, lane * 16, ((i & 1) * 8 + (i >> 1) * 4) * 8192, 0);
        if (C::PF_SER && mat) { __builtin_amdgcn_sched_barrier(0); __builtin_amdgcn_s_sleep(C::PF_SER); __builtin_amdgcn_sched_barrier(0); } }
      if (!mat) { __builtin_amdgcn_sched_barrier(0); START_BARRIER(); __builtin_amdgcn_sched_barrier(0); }
#pragma unroll
      for (int i = 0; i < 4; ++i) asm volatile("" :: "v"(d[i]));
      if (C::DBG & 8) return;
    }
    if (C::DBG & 16) return;
    const __amdgpu_buffer_rsrc_t ry = mk(ws + W_YA, (C::DBG & 1024) ? 0 : W_END - W_YA);
    const int yo = ((b * 32 + h) * 512 + 128 * w + 8 * n + 2 * g) * 8;
    lds_wait_ge<16>(lds, L_FULL, 72u);
#pragma unroll 1
    for (int pass = 0; pass < 2; ++pass) {
      const __amdgpu_buffer_rsrc_t rw = mk((pass == 0 ? wob : woa) + (size_t)xcd * 512 * 512, (C::DBG & 1) ? 0 : 512 * 1024);
      if (C::WO_PF) {
        v4u d[4];
#pragma unroll
        for (int i = 0; i < 4; ++i) { d[i] = __builtin_amdgcn_raw_buffer_load_b128(rw, (2 * b + (i >> 1) + 16 * (i & 1)) * 4096 + lane * 16, sp * 131072 + w * 1024, 0);
          if (C::PF_SER && (i & 1)) { __builtin_amdgcn_sched_barrier(0); __builtin_amdgcn_s_sleep(2 * C::PF_SER); __builtin_amdgcn_sched_barrier(0); } }
#pragma unroll
        for (int i = 0; i < 4; ++i) asm volatile("" :: "v"(d[i]));
        lds_wait_ge<16>(lds, L_FULL, lds_ld(lds, L_FULL) + C::WO_PF_GAP);
      }
      const int wv = (pass == 0 ? w * 131072 + sp * 256 : sp * 131072 + w * 256) + g * 32768 + n * 16;
      v4u F[4][8];
#pragma unroll
      for (int s = 0; s < 4; ++s) {
#pragma unroll
        for (int jp = 0; jp < 4; ++jp) {
          const v4u w0 = __builtin_amdgcn_raw_buffer_load_b128(rw, wv, (8 * jp + s) * 1024, 0);
          const v4u w1 = __builtin_amdgcn_raw_buffer_load_b128(rw, wv, (8 * jp + 4 + s) * 1024, 0);
#pragma unroll
          for (int d = 0; d < 4; ++d) {
            unsigned lo = __builtin_amdgcn_perm(w1[d], w0[d], 0x05040100u), hi = __builtin_amdgcn_perm(w1[d], w0[d], 0x07060302u);
            asm volatile("" : "+v"(lo), "+v"(hi));
            F[s][2 * d][jp] = lo; F[s][2 * d + 1][jp] = hi;
          }
          __builtin_amdgcn_sched_barrier(0);
          if (C::WO_SLEEP) __builtin_amdgcn_s_sleep(C::WO_SLEEP);
          __builtin_amdgcn_sched_barrier(0);
        }
#pragma unroll
        for (int c = 0; c < 8; ++c) asm volatile("" : "+v"(F[s][c]));
      }
      const int hr = pass == 0 ? w : 4;
      lds_wait_ge<1>(lds, L_RCNT + 4 * pass, 4u);
      v4u of[4];
#pragma unroll
      for (int s = 0; s < 4; ++s) of[s] = *LDSP(v4u, L_HREC + hr * 1024 + (n & 3) * 256 + s * 64 + g * 16);
      const unsigned tag = *LDSP(unsigned, L_TAG);
      const f32x4 wt = *LDSP(f32x4, L_WGT + hr * 16);
      f32x4 da = {0, 0, 0, 0}, dc = {0, 0, 0, 0};
      __builtin_amdgcn_sched_barrier(0);
#pragma unroll
      for (int cp = 0; cp < 4; ++cp) {
        f32x4 d0 = {0, 0, 0, 0}, d1 = {0, 0, 0, 0};
#pragma unroll
        for (int s = 0; s < 4; ++s) { d0 = mfma32(of[s], F[s][2 * cp], d0); d1 = mfma32(of[s], F[s][2 * cp + 1], d1); }
#pragma unroll
        for (int r = 0; r < 4; ++r) { da[r] = (cp == 0 || g == cp) ? d0[r] : da[r]; dc[r] = (cp == 0 || g == cp) ? d1[r] : dc[r]; }
      }
#pragma unroll
      for (int i = 0; i < 18; ++i) __builtin_amdgcn_sched_group_barrier(0x8, 1, 0);
#pragma unroll
      for (int i = 0; i < 14; ++i) { __builtin_amdgcn_sched_group_barrier(0x8, 1, 0); __builtin_amdgcn_sched_group_barrier(0x2, 1, 0); }
      const float ya = fmaf(wt[0], da[0], wt[1] * da[1]) + fmaf(wt[2], da[2], wt[3] * da[3]), yc = fmaf(wt[0], dc[0], wt[1] * dc[1]) + fmaf(wt[2], dc[2], wt[3] * dc[3]);
      st_sc1(v4u{__float_as_uint(ya), tag, __float_as_uint(yc), tag}, ry, pass == 0 ? OOB : yo, 0);
      if (pass == 0) *LDSP(f32x2, L_YS + w * 512 + 8 * lane) = f32x2{ya, yc};
      lds_inc(lds, L_YPUB, lane);
      if (w == 0) STAMP(4 + pass);
    }
    if (w == 0) STAMP_OUT(4, 6);
  } else {
    if (C::DBG & 8) { START_BARRIER(); return; }
    unsigned G;
    asm volatile("global_load_dword %0, %1, %2 sc1" : "=v"(G) : "v"(W_EPOCH), "s"(ws) : "memory");
    const int cb = xcd * 32 + jw;
    const __amdgpu_buffer_rsrc_t rs = mk(ws, W_END);
    unsigned tag = 0;
    const int ta = L_P + (8 * g + (n >> 2)) * 32 + (n & 3) * 8;
#define AN_PUT(k_, at_) do { if ((C::ANCH >> (k_)) & 1) { lds_wait_ge<16>(lds, L_FULL, at_); const unsigned im = lds_ld(lds, L_IMPL); \
      st_sc1(v4u{im, tag, im, tag}, rs, (sp == 0 && lane == 0) ? W_AN + (k_) * 1024 + (b * 8 + xcd) * 16 : OOB, 0); } } while (0)
#define AN_GET(k_, at_) do { if ((C::ANCH >> (k_)) & 1) { lds_wait_ge<16>(lds, L_FULL, at_); const unsigned im = lds_ld(lds, L_IMPL); \
      const v4u a = __builtin_amdgcn_raw_buffer_load_b128(rs, W_AN + (k_) * 1024 + lane * 16, 0, 0); \
      const bool okv = a[1] == tag && a[3] == tag; const int cntv = __builtin_popcountll(__builtin_amdgcn_ballot_w64(okv)); \
      float dv = okv ? (float)(int)(a[0] - im) : 0.f; \
      dv += dpp<XOR1>(dv); dv += dpp<XOR2>(dv); dv += dpp<HMIR>(dv); dv += dpp<ROR8>(dv); dv = x32_sum(x16_sum(dv)); \
      if (cntv >= 32) lds_st(lds, L_BASE, im + (int)(dv / (float)cntv) + C::MARGIN); } } while (0)
    int it = -1; OPAQUE_S(it);
    v4u xf[16];
#pragma unroll 1
    for (; it < 2; ++it) {
      if (it != 0) {
        if (it == 1) { AN_PUT(0, 30u); AN_GET(0, 46u); lds_wait_ge<16>(lds, L_FULL, 52u); }
        const v4i rx = mksrd(it == 1 ? xb : xa, (C::DBG & 128) ? 0 : 8 * 512 * 2);
#pragma unroll
        for (int s = 0; s < 16; ++s) asm volatile("buffer_load_dwordx4 %0, %1, %2, 0 offen offset:%3" : "+v"(xf[s]) : "v"(n * 1024 + 16 * g), "s"(rx), "i"(64 * s) : "memory");
      }
      if (it < 0) START_BARRIER();
      if (it >= 0) {
        if (it == 0) lds_wait_ge<1>(lds, L_FULL, C::T_SLICE);
        const v4i rw = mksrd((it ? wqb : wqa) + cb * 16, (C::DBG & 32) ? 0 : 512 * 8192 - cb * 32);
        const unsigned vo = (lane >> 1) * 8192 + (lane & 1) * 16;
#pragma unroll
        for (int i = 0; i < 16; ++i) {
          if (C::SL_NT) asm volatile("s_mov_b32 m0, %0\n\ts_nop 0\n\tbuffer_load_dwordx4 %1, %2, %3 offen nt lds" :: "s"(lds0 + L_P + i * 1024), "v"(vo), "s"(rw), "s"(i * 32 * 8192) : "memory");
          else asm volatile("s_mov_b32 m0, %0\n\ts_nop 0\n\tbuffer_load_dwordx4 %1, %2, %3 offen lds" :: "s"(lds0 + L_P + i * 1024), "v"(vo), "s"(rw), "s"(i * 32 * 8192) : "memory");
          if (it) __builtin_amdgcn_s_sleep(C::WQB_SLEEP); else if (C::WQA_SLEEP) __builtin_amdgcn_s_sleep(C::WQA_SLEEP);
        }
        asm volatile("s_waitcnt vmcnt(0)\n\tv_readfirstlane_b32 %0, %1\n\ts_nop 1" : "=s"(tag) : "v"(G) : "memory");
        tag += 1u;
        if (it == 0) STAMP(6);
      }
#pragma unroll
      for (int s = 0; s < 16; ++s) asm volatile("" : "+v"(xf[s]));
      f32x4 d0 = {0, 0, 0, 0}, d1 = {0, 0, 0, 0};
      {
        v2u t[16][2];
#pragma unroll
        for (int s = 0; s < 16; ++s)
#pragma unroll
          for (int u = 0; u < 2; ++u) t[s][u] = __builtin_bit_cast(v2u, __builtin_amdgcn_ds_read_tr16_b64_v4i16(LDSP(s16x4, ta + s * 1024 + u * 128)));
        __builtin_amdgcn_sched_barrier(0);
#pragma unroll
        for (int s = 0; s < 16; s += 2) {
          d0 = mfma32(v4u{t[s][0][0], t[s][0][1], t[s][1][0], t[s][1][1]}, xf[s], d0);
          d1 = mfma32(v4u{t[s + 1][0][0], t[s + 1][0][1], t[s + 1][1][0], t[s + 1][1][1]}, xf[s + 1], d1);
        }
      }
      d0 += d1;
      st_sc1(v4u{cvtpk(d0[0], d0[1]), tag, cvtpk(d0[2], d0[3]), tag}, rs, (it >= 0 && n < 8 && !(C::DBG & 64)) ? W_QG + (it * 256 + cb) * 512 + n * 64 + g * 16 : OOB, 0);
      if (it == 0) STAMP(7);
      if (it < 0) continue;
      lds_wait_ge<4>(lds, L_FULL, it ? 104u : C::T_GATHER);
      const int go = (C::DBG & (64 | 256)) ? OOB : it == 0 ? (lane < 32 ? W_QG + (h * 8 + (lane >> 2)) * 512 + b * 64 + (lane & 3) * 16 : OOB) : W_QG + (256 + xcd * 32 + (lane >> 2)) * 512 + b * 64 + (lane & 3) * 16;
      const u64 need = (C::DBG & (64 | 256)) ? 0ull : it == 0 ? 0xFFFFFFFFull : ~0ull;
#pragma unroll 1
      for (unsigned spn = 0; spn < SPIN_CAP; ++spn) {
        const v4u v = ld_sc1(rs, go, 0), u = ld_sc1(rs, it == 1 ? go : OOB, 16 * 512);
        if (it == 1 || lane < 32) *LDSP(v2u, L_QIMG + (it == 1 ? 256 : 0) + 8 * lane) = v2u{v[0], v[2]};
        if (it == 1) *LDSP(v2u, L_QIMG + 768 + 8 * lane) = v2u{u[0], u[2]};
        if ((__builtin_amdgcn_ballot_w64(v[1] != tag || v[3] != tag || (it == 1 && (u[1] != tag || u[3] != tag))) & need) == 0) break;
        __builtin_amdgcn_s_sleep(24);
      }
      CBAR();
      lds_st(lds, L_QRDY + 4 * it, 1u);
      if (it == 0) STAMP(8);
    }
    lds_st(lds, L_TAG, tag);
    AN_PUT(1, 112u); AN_GET(1, 128u); AN_PUT(2, 176u); AN_GET(2, 190u);
    lds_wait_ge<16>(lds, L_BCNT, 4u);
    if (!(C::DBG & 512)) {
      f32x2 ml[4]; f32x4 ac[4][2];
#pragma unroll
      for (int u = 0; u < 4; ++u) { ml[u] = *LDSP(f32x2, L_ML + 128 + u * 32 + g * 8); ac[u][0] = *LDSP(f32x4, L_S + u * 4096 + 32 * lane); ac[u][1] = *LDSP(f32x4, L_S + u * 4096 + 32 * lane + 16); }
      const float M = fmaxf(fmaxf(ml[0][0], ml[1][0]), fmaxf(ml[2][0], ml[3][0]));
      float L = 0.f; f32x4 o0 = {0, 0, 0, 0}, o1 = {0, 0, 0, 0};
#pragma unroll
      for (int u = 0; u < 4; ++u) { const float f = ex2((ml[u][0] - M) * SC2); L = fmaf(f, ml[u][1], L); o0 += f * ac[u][0]; o1 += f * ac[u][1]; }
      const float inv = 1.f / L; o0 *= inv; o1 *= inv;
      st_sc1(v4u{cvtpk(o0[0], o0[1]), cvtpk(o0[2], o0[3]), cvtpk(o1[0], o1[1]), cvtpk(o1[2], o1[3])}, rs, W_PO + wg * 1024 + 16 * lane, 0);
      asm volatile("s_waitcnt vmcnt(0)" ::: "memory");
      st_sc1(v4u{__float_as_uint(M), tag, __float_as_uint(L), tag}, rs, n == 0 ? W_ML + wg * 128 + g * 16 : OOB, 0);
    }
    lds_wait_ge<16>(lds, L_FULL, 192u + C::T_MLPOLL);
    v4u mlv = {0, 0, 0, 0};
#pragma unroll 1
    for (unsigned spn = 0; spn < SPIN_CAP; ++spn) {
      if (C::DBG & 512) break;
      mlv = ld_sc1(rs, lane < 16 ? W_ML + (xcd + 8 * (4 * b + (lane & 3))) * 128 + (lane >> 2) * 16 : OOB, 0);
      if ((__builtin_amdgcn_ballot_w64(mlv[1] != tag || mlv[3] != tag) & 0xFFFFu) == 0u) break;
      __builtin_amdgcn_s_sleep(64);
    }
    __builtin_amdgcn_fence(__ATOMIC_ACQUIRE, "wavefront");
    {
      v4u p[4];
#pragma unroll
      for (int u = 0; u < 4; ++u) p[u] = ld_sc1(rs, (C::DBG & 512) ? OOB : W_PO + (xcd + 32 * b) * 1024 + 16 * lane, u * 8192);
      const float m = __uint_as_float(mlv[0]);
      float M = fmaxf(m, dpp<XOR1>(m)); M = fmaxf(M, dpp<XOR2>(M));
      const float e = ex2((m - M) * SC2) * __uint_as_float(mlv[2]);
      float L = e + dpp<XOR1>(e); L += dpp<XOR2>(L);
      if (lane < 16) *LDSP(float, L_WGT + 4 * lane) = e / L;
#pragma unroll
      for (int u = 0; u < 4; ++u) *LDSP(v4u, L_HREC + g * 1024 + u * 256 + n * 16) = p[u];
      CBAR(); lds_st(lds, L_RCNT, 4u);
    }
    STAMP(9);
    if (C::DBG & (16 | 1024)) return;
    lds_wait_ge<16>(lds, L_YPUB, 4u);
    {
      const f32x2 yb = (*LDSP(f32x2, L_YS + 8 * lane) + *LDSP(f32x2, L_YS + 512 + 8 * lane)) + (*LDSP(f32x2, L_YS + 1024 + 8 * lane) + *LDSP(f32x2, L_YS + 1536 + 8 * lane));
      AN_PUT(3, 244u); AN_GET(3, 258u);
      lds_wait_ge<8>(lds, L_FULL, NSL - C::T_YB);
      st_sc1(v4u{__float_as_uint(yb[0]), tag, __float_as_uint(yb[1]), tag}, rs, W_YB + wg * 1024 + (8 * n + 2 * g) * 8, 0);
    }
#pragma unroll 1
    for (int pass = 0; pass < 2; ++pass) {
      if (pass == 0) lds_wait_ge<8>(lds, L_FULL, NSL - 1); else lds_wait_ge<1>(lds, L_YPUB, 8u);
      const int yo = pass == 0 ? W_YB + ((lane >> 3) + 8 * (4 * b + (h >> 3))) * 1024 + (h & 7) * 128 + (lane & 7) * 16
                               : W_YA + (b * 32 * 512 + (lane >> 3) * 512 + 16 * h + 2 * (lane & 7)) * 8;
      v4u y[4]; bool ok[4];
#pragma unroll
      for (int i = 0; i < 4; ++i) { y[i] = v4u{0, 0, 0, 0}; ok[i] = pass == 0 && i > 0; }
#pragma unroll 1
      for (unsigned spn = 0; spn < SPIN_CAP; ++spn) {
        v4u z[4];
#pragma unroll
        for (int i = 0; i < 4; ++i) z[i] = ld_sc1(rs, ok[i] ? OOB : yo, i * 8 * 512 * 8);
        bool all = true;
#pragma unroll
        for (int i = 0; i < 4; ++i) { if (!ok[i]) { y[i] = z[i]; ok[i] = z[i][1] == tag && z[i][3] == tag; } all &= ok[i]; }
        if (__builtin_amdgcn_ballot_w64(!all) == 0) break;
        __builtin_amdgcn_s_sleep(C::Y_SLEEP);
      }
      float s0 = (__uint_as_float(y[0][0]) + __uint_as_float(y[1][0])) + (__uint_as_float(y[2][0]) + __uint_as_float(y[3][0]));
      float s1 = (__uint_as_float(y[0][2]) + __uint_as_float(y[1][2])) + (__uint_as_float(y[2][2]) + __uint_as_float(y[3][2]));
      s0 += dpp<ROR8>(s0); s1 += dpp<ROR8>(s1);
      s0 = x32_sum(x16_sum(s0)); s1 = x32_sum(x16_sum(s1));
      if (lane < 8) *(GAS unsigned*)(out + b * 1024 + (pass == 0 ? 512 : 0) + 16 * h + 2 * lane) = cvtpk(s0, s1);
      STAMP(10 + pass);
    }
    if (wg == 0 && lane == 0) __hip_atomic_store((GAS unsigned*)(ws + W_EPOCH), tag, RLX_AGENT);
    STAMP_OUT(6, 12);
  }
}

template <class C>
static void launch_cfg(void* const* d_in, void* d_out, void* d_ws, hipStream_t stream) {
  (void)hipFuncSetAttribute((const void*)fused_decode<C>, hipFuncAttributeMaxDynamicSharedMemorySize, LDS_BYTES);
  hipLaunchKernelGGL(fused_decode<C>, dim3(256), dim3(NTHR), LDS_BYTES, stream,
                     (const u16*)d_in[0], (const u16*)d_in[1], (const u16*)d_in[2], (const u16*)d_in[3], (const u16*)d_in[4],
                     (const u16*)d_in[5], (const u16*)d_in[6], (const u16*)d_in[7], (const u16*)d_in[8], (const u16*)d_in[9],
                     (u16*)d_out, (unsigned char*)d_ws);
}
extern "C" void kernel_launch(void* const* d_in, const int* in_sizes, int n_in, void* d_out, int out_size, void* d_ws, size_t ws_size, hipStream_t stream) {
  (void)in_sizes; (void)n_in; (void)out_size; (void)ws_size;
  launch_cfg<CfgDefault>(d_in, d_out, d_ws, stream);
}
```

```cpp
#include <hip/hip_runtime.h>
#include <hip/hip_bf16.h>
#include <stdint.h>

typedef unsigned v4u __attribute__((ext_vector_type(4)));
typedef unsigned v2u __attribute__((ext_vector_type(2)));
typedef int v4i __attribute__((ext_vector_type(4)));
typedef float f32x4 __attribute__((ext_vector_type(4)));
typedef float f32x2 __attribute__((ext_vector_type(2)));
typedef short s16x4 __attribute__((ext_vector_type(4)));
typedef __bf16 bf16x8 __attribute__((ext_vector_type(8)));
typedef __bf16 bf16x2 __attribute__((ext_vector_type(2)));
typedef unsigned short u16;
typedef unsigned long long u64;
#define GAS __attribute__((address_space(1)))
#define LAS __attribute__((address_space(3)))
#define RLX_AGENT __ATOMIC_RELAXED, __HIP_MEMORY_SCOPE_AGENT

struct CfgDefault {
  static constexpr int NFL = 20;
  static constexpr int PACE = 2900, PACE2 = 2800;
  static constexpr int UP0 = 60, DN0 = 40;
  static constexpr int PACE0 = 20;
  static constexpr int STEP = 50;
  static constexpr int NDN = 1, NUP = 2, USTEP = 80, PF_SER = 0, EARLY = 0;
  static constexpr int ANCH = 15, MARGIN = 20;
  static constexpr int T_YB = 12, T_YBR = 1, Y_SLEEP = 0, Y_GATE = 150, SPRINT = 24, SPR0 = 200;
  static constexpr int SL_NT = 1, WO_PF = 1, WO_PF_AUX = 0, WO_PF_GAP = 12;
  static constexpr int MUL = 11;
  static constexpr int WO_AUX = 0;
  static constexpr int WO_SLEEP = 0;
  static constexpr int PF_AUX = 0;
  static constexpr int T_SLICE = 3, T_GATHER = 15, T_MLPOLL = 24;
  static constexpr int WQA_SLEEP = 0, WQB_SLEEP = 12;
  static constexpr int PARK = 4;
  static constexpr int DBG = 0;
};

constexpr int SK = 4096, DH = 128, NH = 32, HKV = 8;
constexpr float SC2 = 0.088388347648318440f * 1.4426950408889634f;
constexpr int NWAVE = 10, NTHR = NWAVE * 64;
constexpr int NSL = 320;
constexpr int RING = 15, SLOT = 8192;
constexpr int L_FULL = 0, L_QRDY = 4  , L_BCNT = 12, L_DONE = 16, L_RCNT = 32  , L_TAG = 40, L_YPUB = 44, L_MLC = 48, L_IMPL = 52, L_BASE = 56, L_PFC = 60;
constexpr int L_QIMG = 64;
constexpr int L_WGT = L_QIMG + 1280;
constexpr int L_ML = L_WGT + 128;
constexpr int L_HREC = L_ML + 256;
constexpr int L_YS = 8192 + 16384 + 8192;
constexpr int L_S = 8192;
constexpr int L_P = L_S + 16384;
constexpr int L_RING = L_P + 16384;
constexpr int LDS_BYTES = 160 * 1024;
static_assert(L_HREC % 16 == 0 && L_HREC + 5 * 1024 <= 7168 && L_RING + RING * SLOT <= LDS_BYTES, "lds map");
constexpr int W_EPOCH = 0;
constexpr int W_ML = 4096;
constexpr int W_AN = 49152;
constexpr int W_QG = 65536;
constexpr int W_PO = W_QG + 2 * 256 * 512;
constexpr int W_YB = W_PO + 256 * 1024;
constexpr int W_YA = W_YB + 256 * 1024;
constexpr int W_END = W_YA + 8 * 32 * 512 * 8;
constexpr size_t W_STAMP = 64u << 20;
constexpr unsigned SPIN_CAP = 1u << 21;
constexpr int OOB = 0x7FFFFF00;

__device__ __forceinline__ unsigned cvtpk(float lo, float hi) { return __builtin_bit_cast(unsigned, __builtin_convertvector(f32x2{lo, hi}, bf16x2)); }
__device__ __forceinline__ __amdgpu_buffer_rsrc_t mk(const void* p, unsigned bytes) { return __builtin_amdgcn_make_buffer_rsrc((void*)p, 0, bytes, 0x00020000); }
__device__ __forceinline__ v4i mksrd(const void* p, unsigned bytes) { return v4i{(int)(uintptr_t)p, (int)((uintptr_t)p >> 32), (int)bytes, 0x00020000}; }
__device__ __forceinline__ f32x4 mfma32(v4u a, v4u b, f32x4 c) { return __builtin_amdgcn_mfma_f32_16x16x32_bf16(__builtin_bit_cast(bf16x8, a), __builtin_bit_cast(bf16x8, b), c, 0, 0, 0); }
__device__ __forceinline__ f32x4 mfma16(v2u a, v2u b, f32x4 c) { return __builtin_amdgcn_mfma_f32_16x16x16bf16_1k(__builtin_bit_cast(s16x4, a), __builtin_bit_cast(s16x4, b), c, 0, 0, 0); }
__device__ __forceinline__ float ex2(float x) { return __builtin_amdgcn_exp2f(x); }
__device__ __forceinline__ v4u ld_sc1(__amdgpu_buffer_rsrc_t r, int vo, int so) { return __builtin_amdgcn_raw_buffer_load_b128(r, vo, so, 16); }
__device__ __forceinline__ void st_sc1(v4u v, __amdgpu_buffer_rsrc_t r, int vo, int so) { __builtin_amdgcn_raw_buffer_store_b128(v, r, vo, so, 16); }
template <int CTRL> __device__ __forceinline__ float dpp(float x) { return __builtin_bit_cast(float, __builtin_amdgcn_mov_dpp(__builtin_bit_cast(int, x), CTRL, 0xf, 0xf, true)); }
constexpr int XOR1 = 0xB1, XOR2 = 0x4E, HMIR = 0x141, ROR8 = 0x128;
__device__ __forceinline__ float x16_sum(float x) { auto s = __builtin_amdgcn_permlane16_swap(__float_as_uint(x), __float_as_uint(x), false, false); return __uint_as_float(s[0]) + __uint_as_float(s[1]); }
__device__ __forceinline__ float x32_sum(float x) { auto s = __builtin_amdgcn_permlane32_swap(__float_as_uint(x), __float_as_uint(x), false, false); return __uint_as_float(s[0]) + __uint_as_float(s[1]); }
__device__ __forceinline__ float xg_max(float x) {
  auto s = __builtin_amdgcn_permlane16_swap(__float_as_uint(x), __float_as_uint(x), false, false);
  x = fmaxf(__uint_as_float(s[0]), __uint_as_float(s[1]));
  auto t = __builtin_amdgcn_permlane32_swap(__float_as_uint(x), __float_as_uint(x), false, false);
  return fmaxf(__uint_as_float(t[0]), __uint_as_float(t[1]));
}
#define CBAR() asm volatile("" ::: "memory")
#define OPAQUE_S(x) asm volatile("" : "+s"(x))
#define LDSP(T, off) ((LAS T*)(lds + (off)))
__device__ __forceinline__ unsigned lds_ld(LAS unsigned char* lds, int off) { return __builtin_amdgcn_readfirstlane(*(volatile LAS unsigned*)(lds + off)); }
__device__ __forceinline__ void lds_st(LAS unsigned char* lds, int off, unsigned v) { *(volatile LAS unsigned*)(lds + off) = v; }
template <int SLP> __device__ __forceinline__ unsigned lds_wait_ge(LAS unsigned char* lds, int off, unsigned want) {
  unsigned v = lds_ld(lds, off);
#pragma unroll 1
  for (unsigned sp = 0; v < want && sp < SPIN_CAP * 8; ++sp) { __builtin_amdgcn_s_sleep(SLP); v = lds_ld(lds, off); }
  CBAR();
  return v;
}
__device__ __forceinline__ void lds_inc(LAS unsigned char* lds, int off, int lane) {
  CBAR();
  if (lane == 0) __hip_atomic_fetch_add(LDSP(unsigned, off), 1u, __ATOMIC_RELAXED, __HIP_MEMORY_SCOPE_WORKGROUP);
  CBAR();
}
#define STAMP(i) do {} while (0)
#define STAMP_OUT(lo, hi) do {} while (0)

template <int NFL> __device__ __forceinline__ void dma4(unsigned m0v, unsigned v0, unsigned v1, unsigned v2, unsigned v3, v4i srd, unsigned so) {
  asm volatile("s_mov_b32 m0, %0\n\t"
               "s_waitcnt vmcnt(%7)\n\tbuffer_load_dwordx4 %1, %5, %6 offen nt lds\n\t"
               "s_waitcnt vmcnt(%7)\n\tbuffer_load_dwordx4 %2, %5, %6 offen offset:1024 nt lds\n\t"
               "s_waitcnt vmcnt(%7)\n\tbuffer_load_dwordx4 %3, %5, %6 offen offset:2048 nt lds\n\t"
               "s_waitcnt vmcnt(%7)\n\tbuffer_load_dwordx4 %4, %5, %6 offen offset:3072 nt lds"
               :: "s"(m0v), "v"(v0), "v"(v1), "v"(v2), "v"(v3), "s"(srd), "s"(so), "i"(NFL - 1) : "memory");
}

template <class C>
__global__ void __launch_bounds__(NTHR, 1) fused_decode(const u16* xa, const u16* ak, const u16* av, const u16* wqa, const u16* woa,
                                                        const u16* xb, const u16* bk, const u16* bv, const u16* wqb, const u16* wob,
                                                        u16* out, unsigned char* ws) {
  constexpr int NFL = C::NFL, PARK = C::PARK;
  extern __shared__ __attribute__((aligned(16))) unsigned char lds_raw[];
  LAS unsigned char* lds = (LAS unsigned char*)lds_raw;
  const int wg = blockIdx.x, lane = threadIdx.x & 63, wave = __builtin_amdgcn_readfirstlane(threadIdx.x >> 6);
  const int xcd = wg & 7, jw = wg >> 3, b = jw >> 2, sp = jw & 3, h = 4 * xcd + sp;
  const int g = lane >> 4, n = lane & 15;
  const unsigned lds0 = (unsigned)(uintptr_t)lds;
  if (threadIdx.x < 16) *(volatile LAS unsigned*)(lds + 4 * lane) = 0u;
  asm volatile("s_waitcnt lgkmcnt(0)" :: "s"(wqb), "s"(wob), "s"(out), "s"(ws), "s"(xa) : "memory");
  const unsigned k0 = g * 256 + ((n ^ g) * 16);
#define SLOT_OFF(r_) (((((r_) & 0x70u) << 1) | ((r_) & 15u)) << 12)
  if (C::EARLY) {
#define E4(m_, so_) "s_mov_b32 m0, " m_ "\n\ts_mov_b32 %0, " so_ "\n\t" \
               "buffer_load_dwordx4 %2, %6, %0 offen nt lds\n\tbuffer_load_dwordx4 %3, %6, %0 offen offset:1024 nt lds\n\t" \
               "buffer_load_dwordx4 %4, %6, %0 offen offset:2048 nt lds\n\tbuffer_load_dwordx4 %5, %6, %0 offen offset:3072 nt lds\n\t"
    unsigned tmp;
    asm volatile("s_cmp_lg_u32 %1, 0\n\ts_cbranch_scc1 1f\n\t"
                 E4("%7", "0") E4("%7+4096", "65536") E4("%7+8192", "%8") E4("%7+12288", "%8+65536") E4("%7+16384", "%9") E4("%7+20480", "%9+65536")
                 "1:"
                 : "=&s"(tmp) : "s"(wave), "v"(k0), "v"(k0 ^ 64), "v"(k0 ^ 128), "v"(k0 ^ 192), "s"(mksrd(ak + (size_t)(b * NH + h) * SK * DH, SK * 256)),
                   "i"(L_RING), "i"(SLOT_OFF(C::MUL)), "i"(SLOT_OFF(2 * C::MUL)) : "memory", "scc");
  }
  if (C::EARLY > 1) {
    unsigned tmp;
    asm volatile("s_cmp_lt_u32 %1, 6\n\ts_cbranch_scc1 1f\n\t"
                 "s_mov_b32 m0, %4\n\ts_mov_b32 %0, 0\n\tbuffer_load_dwordx4 %2, %3, %0 offen lds\n\t"
                 "s_mov_b32 %0, 65536-1024\n\tbuffer_load_dwordx4 %2, %3, %0 offen offset:1024 lds\n\t"
                 "s_mov_b32 %0, 32768-2048\n\tbuffer_load_dwordx4 %2, %3, %0 offen offset:2048 lds\n\t"
                 "s_mov_b32 %0, 98304-3072\n\tbuffer_load_dwordx4 %2, %3, %0 offen offset:3072 lds\n\t"
                 "1:"
                 : "=&s"(tmp) : "s"(wave), "v"(lane * 16), "s"(mksrd(wqa + (size_t)(16 * jw + ((wave + 2) & 3)) * 4096 + xcd * 512, (C::DBG & (8 | 2)) ? 0 : 13 * 8192)),
                   "s"(L_S + ((wave + 2) & 3) * 4096) : "memory", "scc");
  }
#define START_BARRIER() do { __builtin_amdgcn_s_barrier(); CBAR(); } while (0)

  if (wave == 0) {
    __builtin_amdgcn_s_setprio(3);
    const size_t ua = (size_t)(b * NH + h) * SK * DH, ub = (size_t)(b * HKV + xcd) * SK * DH;
    if (C::EARLY) START_BARRIER();
    const unsigned vv = lane * 16;
    unsigned slot = lds0 + L_RING + (C::EARLY ? 3 * SLOT : 0);
    int limit = RING;
    u64 now = __builtin_amdgcn_s_memrealtime();
    unsigned base = (unsigned)now + C::PACE0, due = -(4 * C::PACE / 100);
    now = base + due;
    STAMP(0);
    int t = C::EARLY ? 3 : 0;
#pragma unroll 1
    for (int ph = 0; ph < 4; ++ph) {
      const v4i rs = mksrd(ph == 0 ? ak + ua : ph == 1 ? bk + ub : ph == 2 ? bv + ub : av + ua, SK * 256);
      const int tend = ph == 0 ? 128 : ph == 1 ? 160 : ph == 2 ? 192 : NSL;
      const unsigned v0 = ph < 2 ? k0 : vv, v1 = ph < 2 ? k0 ^ 64 : vv, v2 = ph < 2 ? k0 ^ 128 : vv, v3 = ph < 2 ? k0 ^ 192 : vv;
      unsigned r = ((ph == 1 || ph == 2) ? sp * 32 * C::MUL : t * C::MUL) & 127u;
      if (ph == 3) r = 0;
#pragma unroll 1
      while (t < tend) {
        if (!(C::DBG & 4) && t + 3 >= limit) {
#pragma unroll 1
          for (unsigned spn = 0; spn < SPIN_CAP * 8; ++spn) {
            const v4u d = *(volatile LAS v4u*)(lds + L_DONE);
            if (C::ANCH) { const unsigned nb = lds_ld(lds, L_BASE); base = nb ? nb : base; }
            const int c = min(min(4 * (int)d[0], 4 * (int)d[1] + 1), min(4 * (int)d[2] + 2, 4 * (int)d[3] + 3));
            limit = __builtin_amdgcn_readfirstlane(c) + RING;
            if (t + 3 < limit) break;
            __builtin_amdgcn_s_sleep(1);
          }
        }
#define RUN4(N_) do { constexpr int LAG0 = ((N_) + 4 + 7) / 8, LAG1 = ((N_) + 7) / 8;     \
        static_assert(LAG1 >= 3 && LAG1 <= 4, "the published count never goes down"); \
        _Pragma("unroll 1") for (const int te = (t | 3) + 1; t < te; ++t) { \
          const unsigned so = SLOT_OFF(r); \
          if (!C::EARLY && (N_) == NFL && t == 2) START_BARRIER(); \
          dma4<N_>(slot, v0, v1, v2, v3, rs, so); \
          if (LAG0 == LAG1) lds_st(lds, L_FULL, (unsigned)max(t - LAG0 + 1, 0)); \
          dma4<N_>(slot + 4096, v0, v1, v2, v3, rs, so + 65536u); \
          if (LAG0 != LAG1) lds_st(lds, L_FULL, (unsigned)max(t - LAG1 + 1, 0)); \
          slot += SLOT; if (slot >= lds0 + L_RING + RING * SLOT) slot = lds0 + L_RING; \
          r = (r + C::MUL) & 127u; } } while (0)
        if (C::PACE == 0) RUN4(NFL);
        else {
          asm volatile("" : "+s"(now));
          if (C::ANCH) lds_st(lds, L_IMPL, (unsigned)now - due);
          const int lag = (int)((unsigned)now - due - base); due += ph == 0 ? 4 * C::PACE / 100 : 4 * C::PACE2 / 100;
          asm volatile("s_memrealtime %0" : "=s"(now));
          if (C::SPRINT && lag + (int)due >= 32 * (4 * C::PACE / 100) + 48 * (4 * C::PACE2 / 100) - C::SPR0) RUN4(C::SPRINT ? C::SPRINT : 24);
          else if (lag >= -C::DN0) {
            if (lag < C::UP0 || C::NUP == 0) RUN4(NFL); else if (lag < C::UP0 + C::USTEP || C::NUP == 1) RUN4(NFL + 1);
            else if (lag < C::UP0 + 2 * C::USTEP || C::NUP == 2) RUN4(NFL + 2); else RUN4(NFL + 3);
          } else if (lag >= -C::DN0 - C::STEP || C::NDN == 1) RUN4(NFL - 1);
          else if (lag >= -C::DN0 - 2 * C::STEP || C::NDN == 2) RUN4(NFL - 2); else RUN4(NFL - 3);
        }
      }
    }
    asm volatile("s_waitcnt vmcnt(24)" ::: "memory"); lds_st(lds, L_FULL, NSL - 3);
    asm volatile("s_waitcnt vmcnt(16)" ::: "memory"); lds_st(lds, L_FULL, NSL - 2);
    asm volatile("s_waitcnt vmcnt(8)" ::: "memory");  lds_st(lds, L_FULL, NSL - 1);
    asm volatile("s_waitcnt vmcnt(0)" ::: "memory");  lds_st(lds, L_FULL, NSL);
    STAMP(1); STAMP_OUT(0, 2); STAMP_OUT(17, 26);
  } else if (wave >= 2 && wave < 6) {
    START_BARRIER();
    if (C::DBG & 8) return;
    const int w = wave - 2, jj = n >> 2;
    int slot = L_RING + w * SLOT; unsigned full = 0, cnt = 0; int t = w;
#define SLOT_WAIT() do { if (full <= (unsigned)t) full = lds_wait_ge<1>(lds, L_FULL, (unsigned)t + 1u); CBAR(); } while (0)
#define SLOT_DONE() do { CBAR(); ++cnt; lds_st(lds, L_DONE + 4 * w, cnt);     \
        CBAR(); slot += 4 * SLOT; if (slot >= L_RING + RING * SLOT) slot -= RING * SLOT; t += 4; } while (0)
#define K_READ(T_) do { SLOT_WAIT(); _Pragma("unroll") for (int m_ = 0; m_ < 8; ++m_) T_[m_] = *LDSP(v4u, ((slot + kb) ^ (64 * (m_ & 3))) + (m_ >> 2) * 4096); SLOT_DONE(); } while (0)
    const int sw = L_S + w * 4096;
    {
      const int kb = n * 256 + ((g ^ n) * 16);
      v4u q[4];
      float mx = -3.0e38f;
      const int sa0 = sw + ((n & 3) * 8 + g * 2 + (jj & 1)) * 16;
      int sa = sa0;
#define K_MATH(T_, act_, step_) do { f32x4 s0 = {0.f, 0.f, 0.f, 0.f}, s1 = {0.f, 0.f, 0.f, 0.f}; \
        _Pragma("unroll") for (int m = 0; m < 4; ++m) { s0 = mfma32(T_[m], q[m], s0); s1 = mfma32(T_[4 + m], q[m], s1); }       \
        mx = fmaxf(fmaxf(fmaxf(fmaxf(s0[0], s0[1]), fmaxf(s0[2], s0[3])), fmaxf(fmaxf(s1[0], s1[1]), fmaxf(s1[2], s1[3]))), mx); \
        if (act_) *LDSP(f32x4, sa) = (jj & 1) ? s1 : s0; \
        sa += (step_); } while (0)
      {
        v4u P[PARK][8];
#pragma unroll
        for (int i = 0; i < PARK; ++i) K_READ(P[i]);
        lds_wait_ge<1>(lds, L_QRDY, 1u);
#pragma unroll
        for (int m = 0; m < 4; ++m) q[m] = *LDSP(v4u, L_QIMG + g * 16 + 64 * m);
        if (w == 0) STAMP(2);
#pragma unroll
        for (int i = 0; i < PARK; ++i) K_MATH(P[i], (n & 11) == 0, 128);
      }
#pragma unroll 1
      for (int ph = 0; ph < 2; ++ph) {
        const bool act = ph == 1 ? n < 8 : (n & 11) == 0;
        const int sstep = ph == 1 ? 512 : 128, tend = ph == 0 ? 128 : 160;
#pragma unroll 1
        while (t < tend) { v4u T[8]; K_READ(T); K_MATH(T, act, sstep); }
        {
          lds_wait_ge<1>(lds, L_QRDY + 4, 1u);
          const float Mh = xg_max(mx);
          const float M = __builtin_bit_cast(float, __builtin_amdgcn_ds_bpermute(((lane >> 3) & 3) * 4, __builtin_bit_cast(int, Mh)));
          const float mc = -M * SC2;
          float l = 0.f;
          CBAR();
#pragma unroll
          for (int j = 0; j < 4; ++j) {
            const f32x4 s = *LDSP(f32x4, sw + 16 * lane + 1024 * j);
            const float p0 = ex2(fmaf(s[0], SC2, mc)), p1 = ex2(fmaf(s[1], SC2, mc)), p2 = ex2(fmaf(s[2], SC2, mc)), p3 = ex2(fmaf(s[3], SC2, mc));
            l += (p0 + p1) + (p2 + p3);
            *LDSP(v2u, L_P + ph * 8192 + w * 2048 + 8 * lane + 512 * j) = v2u{cvtpk(p0, p1), cvtpk(p2, p3)};
          }
          l += dpp<XOR1>(l); l += dpp<XOR2>(l); l += dpp<HMIR>(l); l = x32_sum(l);
          const float la = x16_sum(l + dpp<ROR8>(l));
          if ((lane & 39) == 0) *LDSP(f32x2, L_ML + ph * 128 + w * 32 + lane) = f32x2{M, ph == 1 ? l : la};
          lds_inc(lds, L_MLC, lane);
        }
        mx = -3.0e38f; sa = sa0;
#pragma unroll
        for (int m = 0; m < 4; ++m) q[m] = *LDSP(v4u, L_QIMG + 256 + (n & 3) * 256 + g * 16 + 64 * m);
      }
    }
    {
      const unsigned Z = 0x0C0C0C0Cu;
      const unsigned el0 = jj == 0 ? 0x0C0C0100u : jj == 1 ? 0x01000C0Cu : Z, el1 = jj == 2 ? 0x0C0C0100u : jj == 3 ? 0x01000C0Cu : Z;
      const unsigned eh0 = jj == 0 ? 0x0C0C0302u : jj == 1 ? 0x03020C0Cu : Z, eh1 = jj == 2 ? 0x0C0C0302u : jj == 3 ? 0x03020C0Cu : Z;
      const int vb = g * 1024 + n * 16;
#pragma unroll 1
      for (int ph = 1; ph >= 0; --ph) {
        if (ph == 0) {
          lds_wait_ge<1>(lds, L_MLC, 8u);
          f32x2 ml[4];
#pragma unroll
          for (int u = 0; u < 4; ++u) ml[u] = *LDSP(f32x2, L_ML + u * 32);
          const float M = fmaxf(fmaxf(ml[0][0], ml[1][0]), fmaxf(ml[2][0], ml[3][0]));
          float L = 0.f;
#pragma unroll
          for (int u = 0; u < 4; ++u) L = fmaf(ex2((ml[u][0] - M) * SC2), ml[u][1], L);
          *LDSP(float, L_WGT + 64 + 4 * w) = ex2(((*LDSP(f32x2, L_ML + w * 32))[0] - M) * SC2) / L;
        }
        const int tend = ph == 1 ? 192 : NSL, pstep = ph == 1 ? 256 : 64;
        int pa = L_P + ph * 8192 + w * 2048 + (ph == 1 ? (n & 3) * 64 : 0) + g * 16;
        f32x4 a0 = {0, 0, 0, 0}, a1 = {0, 0, 0, 0};
#pragma unroll 1
        while (t < tend) {
          const v4u pp = *LDSP(v4u, pa); pa += pstep;
          v2u bf[8];
#pragma unroll
          for (int i = 0; i < 8; ++i) { const unsigned pk = pp[i >> 1]; bf[i] = v2u{__builtin_amdgcn_perm(pk, pk, (i & 1) ? eh0 : el0), __builtin_amdgcn_perm(pk, pk, (i & 1) ? eh1 : el1)}; }
#pragma unroll
          for (int i = 0; i < 8; ++i) asm volatile("" : "+v"(bf[i]));
          SLOT_WAIT();
          v4u T[8];
#pragma unroll
          for (int i = 0; i < 8; ++i) T[i] = *LDSP(v4u, slot + vb + (i >> 2) * 4096 + (i & 3) * 256);
          SLOT_DONE();
#pragma unroll
          for (int i = 0; i < 8; ++i) { a0 = mfma16(v2u{T[i][0], T[i][1]}, bf[i], a0); a1 = mfma16(v2u{T[i][2], T[i][3]}, bf[i], a1); }
        }
        if ((n & 3) == 0) *LDSP(v4u, L_HREC + 4096 + w * 256 + jj * 64 + g * 16) = v4u{cvtpk(a0[0], a1[0]), cvtpk(a0[1], a1[1]), cvtpk(a0[2], a1[2]), cvtpk(a0[3], a1[3])};
        if (ph == 1) {
          const int ro = sw + (n & 3) * 512 + jj * 128 + g * 32;
          *LDSP(f32x4, ro) = f32x4{a0[0], a1[0], a0[1], a1[1]};
          *LDSP(f32x4, ro + 16) = f32x4{a0[2], a1[2], a0[3], a1[3]};
        }
        lds_inc(lds, ph == 1 ? L_BCNT : L_RCNT + 4, lane);
      }
    }
    if (w == 3) { STAMP(3); STAMP_OUT(2, 4); }
  } else if (wave >= 6) {
    const int w = wave - 6;
    if (C::EARLY > 1) { START_BARRIER(); asm volatile("s_waitcnt vmcnt(0)" ::: "memory"); lds_inc(lds, L_PFC, lane); if (C::DBG & 8) return; }
#pragma unroll 1
    for (int mat = C::EARLY > 1 ? 1 : 0; mat < 2; ++mat) {
      if (mat) lds_wait_ge<16>(lds, L_FULL, 32u);
      const __amdgpu_buffer_rsrc_t rw = mk((mat ? wqb : wqa) + (size_t)(16 * jw + w) * 4096 + xcd * 512, (C::DBG & (8 | 2)) ? 0 : 13 * 8192);
      v4u d[4];
#pragma unroll
      for (int i = 0; i < 4; ++i) { d[i] = __builtin_amdgcn_raw_buffer_load_b128(rw, lane * 16, ((i & 1) * 8 + (i >> 1) * 4) * 8192, 0);
        if (C::PF_SER && mat) { __builtin_amdgcn_sched_barrier(0); __builtin_amdgcn_s_sleep(C::PF_SER); __builtin_amdgcn_sched_barrier(0); } }
      if (!mat) { __builtin_amdgcn_sched_barrier(0); START_BARRIER(); __builtin_amdgcn_sched_barrier(0); }
#pragma unroll
      for (int i = 0; i < 4; ++i) asm volatile("" :: "v"(d[i]));
      if (C::DBG & 8) return;
    }
    if (C::DBG & 16) return;
    const __amdgpu_buffer_rsrc_t ry = mk(ws + W_YA, (C::DBG & 1024) ? 0 : W_END - W_YA);
    const int yo = ((b * 32 + h) * 512 + 128 * w + 8 * n + 2 * g) * 8;
    lds_wait_ge<16>(lds, L_FULL, 72u);
#pragma unroll 1
    for (int pass = 0; pass < 2; ++pass) {
      const __amdgpu_buffer_rsrc_t rw = mk((pass == 0 ? wob : woa) + (size_t)xcd * 512 * 512, (C::DBG & 1) ? 0 : 512 * 1024);
      if (C::WO_PF) {
        v4u d[4];
#pragma unroll
        for (int i = 0; i < 4; ++i) { d[i] = __builtin_amdgcn_raw_buffer_load_b128(rw, (2 * b + (i >> 1) + 16 * (i & 1)) * 4096 + lane * 16, sp * 131072 + w * 1024, 0);
          if (C::PF_SER && (i & 1)) { __builtin_amdgcn_sched_barrier(0); __builtin_amdgcn_s_sleep(2 * C::PF_SER); __builtin_amdgcn_sched_barrier(0); } }
#pragma unroll
        for (int i = 0; i < 4; ++i) asm volatile("" :: "v"(d[i]));
        lds_wait_ge<16>(lds, L_FULL, lds_ld(lds, L_FULL) + C::WO_PF_GAP);
      }
      const int wv = (pass == 0 ? w * 131072 + sp * 256 : sp * 131072 + w * 256) + g * 32768 + n * 16;
      v4u F[4][8];
#pragma unroll
      for (int s = 0; s < 4; ++s) {
#pragma unroll
        for (int jp = 0; jp < 4; ++jp) {
          const v4u w0 = __builtin_amdgcn_raw_buffer_load_b128(rw, wv, (8 * jp + s) * 1024, 0);
          const v4u w1 = __builtin_amdgcn_raw_buffer_load_b128(rw, wv, (8 * jp + 4 + s) * 1024, 0);
#pragma unroll
          for (int d = 0; d < 4; ++d) {
            unsigned lo = __builtin_amdgcn_perm(w1[d], w0[d], 0x05040100u), hi = __builtin_amdgcn_perm(w1[d], w0[d], 0x07060302u);
            asm volatile("" : "+v"(lo), "+v"(hi));
            F[s][2 * d][jp] = lo; F[s][2 * d + 1][jp] = hi;
          }
          __builtin_amdgcn_sched_barrier(0);
          if (C::WO_SLEEP) __builtin_amdgcn_s_sleep(C::WO_SLEEP);
          __builtin_amdgcn_sched_barrier(0);
        }
#pragma unroll
        for (int c = 0; c < 8; ++c) asm volatile("" : "+v"(F[s][c]));
      }
      const int hr = pass == 0 ? w : 4;
      lds_wait_ge<1>(lds, L_RCNT + 4 * pass, 4u);
      v4u of[4];
#pragma unroll
      for (int s = 0; s < 4; ++s) of[s] = *LDSP(v4u, L_HREC + hr * 1024 + (n & 3) * 256 + s * 64 + g * 16);
      const unsigned tag = *LDSP(unsigned, L_TAG);
      const f32x4 wt = *LDSP(f32x4, L_WGT + hr * 16);
      f32x4 da = {0, 0, 0, 0}, dc = {0, 0, 0, 0};
      __builtin_amdgcn_sched_barrier(0);
#pragma unroll
      for (int cp = 0; cp < 4; ++cp) {
        f32x4 d0 = {0, 0, 0, 0}, d1 = {0, 0, 0, 0};
#pragma unroll
        for (int s = 0; s < 4; ++s) { d0 = mfma32(of[s], F[s][2 * cp], d0); d1 = mfma32(of[s], F[s][2 * cp + 1], d1); }
#pragma unroll
        for (int r = 0; r < 4; ++r) { da[r] = (cp == 0 || g == cp) ? d0[r] : da[r]; dc[r] = (cp == 0 || g == cp) ? d1[r] : dc[r]; }
      }
#pragma unroll
      for (int i = 0; i < 18; ++i) __builtin_amdgcn_sched_group_barrier(0x8, 1, 0);
#pragma unroll
      for (int i = 0; i < 14; ++i) { __builtin_amdgcn_sched_group_barrier(0x8, 1, 0); __builtin_amdgcn_sched_group_barrier(0x2, 1, 0); }
      const float ya = fmaf(wt[0], da[0], wt[1] * da[1]) + fmaf(wt[2], da[2], wt[3] * da[3]), yc = fmaf(wt[0], dc[0], wt[1] * dc[1]) + fmaf(wt[2], dc[2], wt[3] * dc[3]);
      st_sc1(v4u{__float_as_uint(ya), tag, __float_as_uint(yc), tag}, ry, pass == 0 ? OOB : yo, 0);
      if (pass == 0) *LDSP(f32x2, L_YS + w * 512 + 8 * lane) = f32x2{ya, yc};
      lds_inc(lds, L_YPUB, lane);
      if (w == 0) STAMP(4 + pass);
    }
    if (w == 0) STAMP_OUT(4, 6);
  } else {
    if (C::DBG & 8) { START_BARRIER(); return; }
    unsigned G;
    asm volatile("global_load_dword %0, %1, %2 sc1" : "=v"(G) : "v"(W_EPOCH), "s"(ws) : "memory");
    const int cb = xcd * 32 + jw;
    const __amdgpu_buffer_rsrc_t rs = mk(ws, W_END);
    unsigned tag = 0;
    const int ta = L_P + (8 * g + (n >> 2)) * 32 + (n & 3) * 8;
#define AN_PUT(k_, at_) do { if ((C::ANCH >> (k_)) & 1) { lds_wait_ge<16>(lds, L_FULL, at_); const unsigned im = lds_ld(lds, L_IMPL); \
      st_sc1(v4u{im, tag, im, tag}, rs, (sp == 0 && lane == 0) ? W_AN + (k_) * 1024 + (b * 8 + xcd) * 16 : OOB, 0); } } while (0)
#define AN_GET(k_, at_) do { if ((C::ANCH >> (k_)) & 1) { lds_wait_ge<16>(lds, L_FULL, at_); const unsigned im = lds_ld(lds, L_IMPL); \
      const v4u a = __builtin_amdgcn_raw_buffer_load_b128(rs, W_AN + (k_) * 1024 + lane * 16, 0, 0); \
      const bool okv = a[1] == tag && a[3] == tag; const int cntv = __builtin_popcountll(__builtin_amdgcn_ballot_w64(okv)); \
      float dv = okv ? (float)(int)(a[0] - im) : 0.f; \
      dv += dpp<XOR1>(dv); dv += dpp<XOR2>(dv); dv += dpp<HMIR>(dv); dv += dpp<ROR8>(dv); dv = x32_sum(x16_sum(dv)); \
      if (cntv >= 32) lds_st(lds, L_BASE, im + (int)(dv / (float)cntv) + C::MARGIN); } } while (0)
    int it = -1; OPAQUE_S(it);
    v4u xf[16];
#pragma unroll 1
    for (; it < 2; ++it) {
      if (it != 0) {
        if (it == 1) { AN_PUT(0, 30u); AN_GET(0, 46u); lds_wait_ge<16>(lds, L_FULL, 52u); }
        const v4i rx = mksrd(it == 1 ? xb : xa, (C::DBG & 128) ? 0 : 8 * 512 * 2);
#pragma unroll
        for (int s = 0; s < 16; ++s) asm volatile("buffer_load_dwordx4 %0, %1, %2, 0 offen offset:%3" : "+v"(xf[s]) : "v"(n * 1024 + 16 * g), "s"(rx), "i"(64 * s) : "memory");
      }
      if (it < 0) START_BARRIER();
      if (it >= 0) {
        if (it == 0) { if (C::EARLY > 1) lds_wait_ge<1>(lds, L_PFC, 4u); lds_wait_ge<1>(lds, L_FULL, C::T_SLICE); }
        const v4i rw = mksrd((it ? wqb : wqa) + cb * 16, (C::DBG & 32) ? 0 : 512 * 8192 - cb * 32);
        const unsigned vo = (lane >> 1) * 8192 + (lane & 1) * 16;
#pragma unroll
        for (int i = 0; i < 16; ++i) {
          if (C::SL_NT) asm volatile("s_mov_b32 m0, %0\n\ts_nop 0\n\tbuffer_load_dwordx4 %1, %2, %3 offen nt lds" :: "s"(lds0 + L_P + i * 1024), "v"(vo), "s"(rw), "s"(i * 32 * 8192) : "memory");
          else asm volatile("s_mov_b32 m0, %0\n\ts_nop 0\n\tbuffer_load_dwordx4 %1, %2, %3 offen lds" :: "s"(lds0 + L_P + i * 1024), "v"(vo), "s"(rw), "s"(i * 32 * 8192) : "memory");
          if (it) __builtin_amdgcn_s_sleep(C::WQB_SLEEP); else if (C::WQA_SLEEP) __builtin_amdgcn_s_sleep(C::WQA_SLEEP);
        }
        asm volatile("s_waitcnt vmcnt(0)\n\tv_readfirstlane_b32 %0, %1\n\ts_nop 1" : "=s"(tag) : "v"(G) : "memory");
        tag += 1u;
        if (it == 0) STAMP(6);
      }
#pragma unroll
      for (int s = 0; s < 16; ++s) asm volatile("" : "+v"(xf[s]));
      f32x4 d0 = {0, 0, 0, 0}, d1 = {0, 0, 0, 0};
      {
        v2u t[16][2];
#pragma unroll
        for (int s = 0; s < 16; ++s)
#pragma unroll
          for (int u = 0; u < 2; ++u) t[s][u] = __builtin_bit_cast(v2u, __builtin_amdgcn_ds_read_tr16_b64_v4i16(LDSP(s16x4, ta + s * 1024 + u * 128)));
        __builtin_amdgcn_sched_barrier(0);
#pragma unroll
        for (int s = 0; s < 16; s += 2) {
          d0 = mfma32(v4u{t[s][0][0], t[s][0][1], t[s][1][0], t[s][1][1]}, xf[s], d0);
          d1 = mfma32(v4u{t[s + 1][0][0], t[s + 1][0][1], t[s + 1][1][0], t[s + 1][1][1]}, xf[s + 1], d1);
        }
      }
      d0 += d1;
      st_sc1(v4u{cvtpk(d0[0], d0[1]), tag, cvtpk(d0[2], d0[3]), tag}, rs, (it >= 0 && n < 8 && !(C::DBG & 64)) ? W_QG + (it * 256 + cb) * 512 + n * 64 + g * 16 : OOB, 0);
      if (it == 0) STAMP(7);
      if (it < 0) continue;
      lds_wait_ge<4>(lds, L_FULL, it ? 104u : C::T_GATHER);
      const int go = (C::DBG & (64 | 256)) ? OOB : it == 0 ? (lane < 32 ? W_QG + (h * 8 + (lane >> 2)) * 512 + b * 64 + (lane & 3) * 16 : OOB) : W_QG + (256 + xcd * 32 + (lane >> 2)) * 512 + b * 64 + (lane & 3) * 16;
      const u64 need = (C::DBG & (64 | 256)) ? 0ull : it == 0 ? 0xFFFFFFFFull : ~0ull;
#pragma unroll 1
      for (unsigned spn = 0; spn < SPIN_CAP; ++spn) {
        const v4u v = ld_sc1(rs, go, 0), u = ld_sc1(rs, it == 1 ? go : OOB, 16 * 512);
        if (it == 1 || lane < 32) *LDSP(v2u, L_QIMG + (it == 1 ? 256 : 0) + 8 * lane) = v2u{v[0], v[2]};
        if (it == 1) *LDSP(v2u, L_QIMG + 768 + 8 * lane) = v2u{u[0], u[2]};
        if ((__builtin_amdgcn_ballot_w64(v[1] != tag || v[3] != tag || (it == 1 && (u[1] != tag || u[3] != tag))) & need) == 0) break;
        __builtin_amdgcn_s_sleep(24);
      }
      CBAR();
      lds_st(lds, L_QRDY + 4 * it, 1u);
      if (it == 0) STAMP(8);
    }
    lds_st(lds, L_TAG, tag);
    AN_PUT(1, 112u); AN_GET(1, 128u); AN_PUT(2, 176u); AN_GET(2, 190u);
    lds_wait_ge<16>(lds, L_BCNT, 4u);
    if (!(C::DBG & 512)) {
      f32x2 ml[4]; f32x4 ac[4][2];
#pragma unroll
      for (int u = 0; u < 4; ++u) { ml[u] = *LDSP(f32x2, L_ML + 128 + u * 32 + g * 8); ac[u][0] = *LDSP(f32x4, L_S + u * 4096 + 32 * lane); ac[u][1] = *LDSP(f32x4, L_S + u * 4096 + 32 * lane + 16); }
      const float M = fmaxf(fmaxf(ml[0][0], ml[1][0]), fmaxf(ml[2][0], ml[3][0]));
      float L = 0.f; f32x4 o0 = {0, 0, 0, 0}, o1 = {0, 0, 0, 0};
#pragma unroll
      for (int u = 0; u < 4; ++u) { const float f = ex2((ml[u][0] - M) * SC2); L = fmaf(f, ml[u][1], L); o0 += f * ac[u][0]; o1 += f * ac[u][1]; }
      const float inv = 1.f / L; o0 *= inv; o1 *= inv;
      st_sc1(v4u{cvtpk(o0[0], o0[1]), cvtpk(o0[2], o0[3]), cvtpk(o1[0], o1[1]), cvtpk(o1[2], o1[3])}, rs, W_PO + wg * 1024 + 16 * lane, 0);
      asm volatile("s_waitcnt vmcnt(0)" ::: "memory");
      st_sc1(v4u{__float_as_uint(M), tag, __float_as_uint(L), tag}, rs, n == 0 ? W_ML + wg * 128 + g * 16 : OOB, 0);
    }
    lds_wait_ge<16>(lds, L_FULL, 192u + C::T_MLPOLL);
    v4u mlv = {0, 0, 0, 0};
#pragma unroll 1
    for (unsigned spn = 0; spn < SPIN_CAP; ++spn) {
      if (C::DBG & 512) break;
      mlv = ld_sc1(rs, lane < 16 ? W_ML + (xcd + 8 * (4 * b + (lane & 3))) * 128 + (lane >> 2) * 16 : OOB, 0);
      if ((__builtin_amdgcn_ballot_w64(mlv[1] != tag || mlv[3] != tag) & 0xFFFFu) == 0u) break;
      __builtin_amdgcn_s_sleep(64);
    }
    __builtin_amdgcn_fence(__ATOMIC_ACQUIRE, "wavefront");
    {
      v4u p[4];
#pragma unroll
      for (int u = 0; u < 4; ++u) p[u] = ld_sc1(rs, (C::DBG & 512) ? OOB : W_PO + (xcd + 32 * b) * 1024 + 16 * lane, u * 8192);
      const float m = __uint_as_float(mlv[0]);
      float M = fmaxf(m, dpp<XOR1>(m)); M = fmaxf(M, dpp<XOR2>(M));
      const float e = ex2((m - M) * SC2) * __uint_as_float(mlv[2]);
      float L = e + dpp<XOR1>(e); L += dpp<XOR2>(L);
      if (lane < 16) *LDSP(float, L_WGT + 4 * lane) = e / L;
#pragma unroll
      for (int u = 0; u < 4; ++u) *LDSP(v4u, L_HREC + g * 1024 + u * 256 + n * 16) = p[u];
      CBAR(); lds_st(lds, L_RCNT, 4u);
    }
    STAMP(9);
    if (C::DBG & (16 | 1024)) return;
    lds_wait_ge<16>(lds, L_YPUB, 4u);
    {
      const f32x2 yb = (*LDSP(f32x2, L_YS + 8 * lane) + *LDSP(f32x2, L_YS + 512 + 8 * lane)) + (*LDSP(f32x2, L_YS + 1024 + 8 * lane) + *LDSP(f32x2, L_YS + 1536 + 8 * lane));
      AN_PUT(3, 244u); AN_GET(3, 258u);
      lds_wait_ge<8>(lds, L_FULL, NSL - C::T_YB);
      st_sc1(v4u{__float_as_uint(yb[0]), tag, __float_as_uint(yb[1]), tag}, rs, W_YB + wg * 1024 + (8 * n + 2 * g) * 8, 0);
    }
#pragma unroll 1
    for (int pass = 0; pass < 2; ++pass) {
      if (pass == 0) lds_wait_ge<8>(lds, L_FULL, NSL - C::T_YBR);
      else {
        if (C::Y_GATE) {
          const unsigned bs = lds_ld(lds, L_BASE), gate = bs + 32 * (4 * C::PACE / 100) + 48 * (4 * C::PACE2 / 100) + C::Y_GATE;
#pragma unroll 1
          for (unsigned spn = 0; bs && spn < 64 && (int)((unsigned)__builtin_amdgcn_s_memrealtime() - gate) < 0; ++spn) __builtin_amdgcn_s_sleep(1);
        }
        lds_wait_ge<1>(lds, L_YPUB, 8u);
      }
      const int yo = pass == 0 ? W_YB + ((lane >> 3) + 8 * (4 * b + (h >> 3))) * 1024 + (h & 7) * 128 + (lane & 7) * 16
                               : W_YA + (b * 32 * 512 + (lane >> 3) * 512 + 16 * h + 2 * (lane & 7)) * 8;
      v4u y[4]; bool ok[4];
#pragma unroll
      for (int i = 0; i < 4; ++i) { y[i] = v4u{0, 0, 0, 0}; ok[i] = pass == 0 && i > 0; }
#pragma unroll 1
      for (unsigned spn = 0; spn < SPIN_CAP; ++spn) {
        v4u z[4];
#pragma unroll
        for (int i = 0; i < 4; ++i) z[i] = ld_sc1(rs, ok[i] ? OOB : yo, i * 8 * 512 * 8);
        bool all = true;
#pragma unroll
        for (int i = 0; i < 4; ++i) { if (!ok[i]) { y[i] = z[i]; ok[i] = z[i][1] == tag && z[i][3] == tag; } all &= ok[i]; }
        if (__builtin_amdgcn_ballot_w64(!all) == 0) break;
        if (C::Y_SLEEP) __builtin_amdgcn_s_sleep(C::Y_SLEEP);
      }
      float s0 = (__uint_as_float(y[0][0]) + __uint_as_float(y[1][0])) + (__uint_as_float(y[2][0]) + __uint_as_float(y[3][0]));
      float s1 = (__uint_as_float(y[0][2]) + __uint_as_float(y[1][2])) + (__uint_as_float(y[2][2]) + __uint_as_float(y[3][2]));
      s0 += dpp<ROR8>(s0); s1 += dpp<ROR8>(s1);
      s0 = x32_sum(x16_sum(s0)); s1 = x32_sum(x16_sum(s1));
      if (lane < 8) *(GAS unsigned*)(out + b * 1024 + (pass == 0 ? 512 : 0) + 16 * h + 2 * lane) = cvtpk(s0, s1);
      STAMP(10 + pass);
    }
    if (wg == 0 && lane == 0) __hip_atomic_store((GAS unsigned*)(ws + W_EPOCH), tag, RLX_AGENT);
    STAMP_OUT(6, 12);
  }
}

template <class C>
static void launch_cfg(void* const* d_in, void* d_out, void* d_ws, hipStream_t stream) {
  (void)hipFuncSetAttribute((const void*)fused_decode<C>, hipFuncAttributeMaxDynamicSharedMemorySize, LDS_BYTES);
  hipLaunchKernelGGL(fused_decode<C>, dim3(256), dim3(NTHR), LDS_BYTES, stream,
                     (const u16*)d_in[0], (const u16*)d_in[1], (const u16*)d_in[2], (const u16*)d_in[3], (const u16*)d_in[4],
                     (const u16*)d_in[5], (const u16*)d_in[6], (const u16*)d_in[7], (const u16*)d_in[8], (const u16*)d_in[9],
                     (u16*)d_out, (unsigned char*)d_ws);
}
extern "C" void kernel_launch(void* const* d_in, const int* in_sizes, int n_in, void* d_out, int out_size, void* d_ws, size_t ws_size, hipStream_t stream) {
  (void)in_sizes; (void)n_in; (void)out_size; (void)ws_size;
  launch_cfg<CfgDefault>(d_in, d_out, d_ws, stream);
}
```

```cpp
#include <hip/hip_runtime.h>
#include <hip/hip_bf16.h>
#include <stdint.h>

typedef unsigned v4u __attribute__((ext_vector_type(4)));
typedef unsigned v2u __attribute__((ext_vector_type(2)));
typedef int v4i __attribute__((ext_vector_type(4)));
typedef float f32x4 __attribute__((ext_vector_type(4)));
typedef float f32x2 __attribute__((ext_vector_type(2)));
typedef short s16x4 __attribute__((ext_vector_type(4)));
typedef __bf16 bf16x8 __attribute__((ext_vector_type(8)));
typedef __bf16 bf16x2 __attribute__((ext_vector_type(2)));
typedef unsigned short u16;
typedef unsigned long long u64;
#define GAS __attribute__((address_space(1)))
#define LAS __attribute__((address_space(3)))
#define RLX_AGENT __ATOMIC_RELAXED, __HIP_MEMORY_SCOPE_AGENT

struct CfgDefault {
  static constexpr int NFL = 20;
  static constexpr int PACE = 2900, PACE2 = 2800;
  static constexpr int UP0 = 60, DN0 = 40;
  static constexpr int PACE0 = 20;
  static constexpr int STEP = 50;
  static constexpr int NDN = 1, NUP = 2, USTEP = 80, PF_SER = 0, EARLY = 1, CODEPF = 140, BAR_AT = -2, LFIRST = 1;
  static constexpr int ANCH = 15, MARGIN = 20;
  static constexpr int T_YB = 12, T_YBR = 1, Y_SLEEP = 0, Y_GATE = 150, SPRINT = 24, SPR0 = 200;
  static constexpr int SL_NT = 1, WO_PF = 1, WO_PF_AUX = 0, WO_PF_GAP = 12;
  static constexpr int MUL = 11;
  static constexpr int WO_AUX = 0;
  static constexpr int WO_SLEEP = 0;
  static constexpr int PF_AUX = 0;
  static constexpr int T_SLICE = 5, T_GATHER = 16, T_MLPOLL = 24;
  static constexpr int WQA_SLEEP = 0, WQB_SLEEP = 12;
  static constexpr int PARK = 4;
  static constexpr int DBG = 0;
};

constexpr int SK = 4096, DH = 128, NH = 32, HKV = 8;
constexpr float SC2 = 0.088388347648318440f * 1.4426950408889634f;
constexpr int NWAVE = 10, NTHR = NWAVE * 64;
constexpr int NSL = 320;
constexpr int RING = 15, SLOT = 8192;
constexpr int L_FULL = 0, L_QRDY = 4  , L_BCNT = 12, L_DONE = 16, L_RCNT = 32  , L_TAG = 40, L_YPUB = 44, L_MLC = 48, L_IMPL = 52, L_BASE = 56, L_PFC = 60;
constexpr int L_QIMG = 64;
constexpr int L_WGT = L_QIMG + 1280;
constexpr int L_ML = L_WGT + 128;
constexpr int L_HREC = L_ML + 256;
constexpr int L_YS = 8192 + 16384 + 8192;
constexpr int L_S = 8192;
constexpr int L_P = L_S + 16384;
constexpr int L_RING = L_P + 16384;
constexpr int LDS_BYTES = 160 * 1024;
static_assert(L_HREC % 16 == 0 && L_HREC + 5 * 1024 <= 7168 && L_RING + RING * SLOT <= LDS_BYTES, "lds map");
constexpr int W_EPOCH = 0;
constexpr int W_ML = 4096;
constexpr int W_AN = 49152;
constexpr int W_QG = 65536;
constexpr int W_PO = W_QG + 2 * 256 * 512;
constexpr int W_YB = W_PO + 256 * 1024;
constexpr int W_YA = W_YB + 256 * 1024;
constexpr int W_END = W_YA + 8 * 32 * 512 * 8;
constexpr size_t W_STAMP = 64u << 20;
constexpr unsigned SPIN_CAP = 1u << 21;
constexpr int OOB = 0x7FFFFF00;

__device__ __forceinline__ unsigned cvtpk(float lo, float hi) { return __builtin_bit_cast(unsigned, __builtin_convertvector(f32x2{lo, hi}, bf16x2)); }
__device__ __forceinline__ __amdgpu_buffer_rsrc_t mk(const void* p, unsigned bytes) { return __builtin_amdgcn_make_buffer_rsrc((void*)p, 0, bytes, 0x00020000); }
__device__ __forceinline__ v4i mksrd(const void* p, unsigned bytes) { return v4i{(int)(uintptr_t)p, (int)((uintptr_t)p >> 32), (int)bytes, 0x00020000}; }
__device__ __forceinline__ f32x4 mfma32(v4u a, v4u b, f32x4 c) { return __builtin_amdgcn_mfma_f32_16x16x32_bf16(__builtin_bit_cast(bf16x8, a), __builtin_bit_cast(bf16x8, b), c, 0, 0, 0); }
__device__ __forceinline__ f32x4 mfma16(v2u a, v2u b, f32x4 c) { return __builtin_amdgcn_mfma_f32_16x16x16bf16_1k(__builtin_bit_cast(s16x4, a), __builtin_bit_cast(s16x4, b), c, 0, 0, 0); }
__device__ __forceinline__ float ex2(float x) { return __builtin_amdgcn_exp2f(x); }
__device__ __forceinline__ v4u ld_sc1(__amdgpu_buffer_rsrc_t r, int vo, int so) { return __builtin_amdgcn_raw_buffer_load_b128(r, vo, so, 16); }
__device__ __forceinline__ void st_sc1(v4u v, __amdgpu_buffer_rsrc_t r, int vo, int so) { __builtin_amdgcn_raw_buffer_store_b128(v, r, vo, so, 16); }
template <int CTRL> __device__ __forceinline__ float dpp(float x) { return __builtin_bit_cast(float, __builtin_amdgcn_mov_dpp(__builtin_bit_cast(int, x), CTRL, 0xf, 0xf, true)); }
constexpr int XOR1 = 0xB1, XOR2 = 0x4E, HMIR = 0x141, ROR8 = 0x128;
__device__ __forceinline__ float x16_sum(float x) { auto s = __builtin_amdgcn_permlane16_swap(__float_as_uint(x), __float_as_uint(x), false, false); return __uint_as_float(s[0]) + __uint_as_float(s[1]); }
__device__ __forceinline__ float x32_sum(float x) { auto s = __builtin_amdgcn_permlane32_swap(__float_as_uint(x), __float_as_uint(x), false, false); return __uint_as_float(s[0]) + __uint_as_float(s[1]); }
__device__ __forceinline__ float xg_max(float x) {
  auto s = __builtin_amdgcn_permlane16_swap(__float_as_uint(x), __float_as_uint(x), false, false);
  x = fmaxf(__uint_as_float(s[0]), __uint_as_float(s[1]));
  auto t = __builtin_amdgcn_permlane32_swap(__float_as_uint(x), __float_as_uint(x), false, false);
  return fmaxf(__uint_as_float(t[0]), __uint_as_float(t[1]));
}
#define CBAR() asm volatile("" ::: "memory")
#define OPAQUE_S(x) asm volatile("" : "+s"(x))
#define LDSP(T, off) ((LAS T*)(lds + (off)))
__device__ __forceinline__ unsigned lds_ld(LAS unsigned char* lds, int off) { return __builtin_amdgcn_readfirstlane(*(volatile LAS unsigned*)(lds + off)); }
__device__ __forceinline__ void lds_st(LAS unsigned char* lds, int off, unsigned v) { *(volatile LAS unsigned*)(lds + off) = v; }
template <int SLP> __device__ __forceinline__ unsigned lds_wait_ge(LAS unsigned char* lds, int off, unsigned want) {
  unsigned v = lds_ld(lds, off);
#pragma unroll 1
  for (unsigned sp = 0; v < want && sp < SPIN_CAP * 8; ++sp) { __builtin_amdgcn_s_sleep(SLP); v = lds_ld(lds, off); }
  CBAR();
  return v;
}
__device__ __forceinline__ void lds_inc(LAS unsigned char* lds, int off, int lane) {
  CBAR();
  if (lane == 0) __hip_atomic_fetch_add(LDSP(unsigned, off), 1u, __ATOMIC_RELAXED, __HIP_MEMORY_SCOPE_WORKGROUP);
  CBAR();
}
#define STAMP(i) do {} while (0)
#define STAMP_OUT(lo, hi) do {} while (0)

template <int NFL> __device__ __forceinline__ void dma4(unsigned m0v, unsigned v0, unsigned v1, unsigned v2, unsigned v3, v4i srd, unsigned so) {
  asm volatile("s_mov_b32 m0, %0\n\t"
               "s_waitcnt vmcnt(%7)\n\tbuffer_load_dwordx4 %1, %5, %6 offen nt lds\n\t"
               "s_waitcnt vmcnt(%7)\n\tbuffer_load_dwordx4 %2, %5, %6 offen offset:1024 nt lds\n\t"
               "s_waitcnt vmcnt(%7)\n\tbuffer_load_dwordx4 %3, %5, %6 offen offset:2048 nt lds\n\t"
               "s_waitcnt vmcnt(%7)\n\tbuffer_load_dwordx4 %4, %5, %6 offen offset:3072 nt lds"
               :: "s"(m0v), "v"(v0), "v"(v1), "v"(v2), "v"(v3), "s"(srd), "s"(so), "i"(NFL - 1) : "memory");
}

template <class C>
__global__ void __launch_bounds__(NTHR, 1) fused_decode(const u16* xa, const u16* ak, const u16* av, const u16* wqa, const u16* woa,
                                                        const u16* xb, const u16* bk, const u16* bv, const u16* wqb, const u16* wob,
                                                        u16* out, unsigned char* ws) {
  constexpr int NFL = C::NFL, PARK = C::PARK;
  u64 pc0;
  if (C::CODEPF) {
    unsigned tv;
    asm volatile("s_getpc_b64 %0\n\tv_min_u32 %1, %3, %2\n\ts_movk_i32 m0, 7936\n\tv_lshlrev_b32 %1, 7, %1\n\tglobal_load_lds_dword %1, %0"
                 : "=&s"(pc0), "=&v"(tv) : "v"(threadIdx.x), "i"(C::CODEPF - 1));
    __builtin_amdgcn_sched_barrier(0);
  }
  extern __shared__ __attribute__((aligned(16))) unsigned char lds_raw[];
  LAS unsigned char* lds = (LAS unsigned char*)lds_raw;
  const int wg = blockIdx.x, lane = threadIdx.x & 63, wave = __builtin_amdgcn_readfirstlane(threadIdx.x >> 6);
  const int xcd = wg & 7, jw = wg >> 3, b = jw >> 2, sp = jw & 3, h = 4 * xcd + sp;
  const int g = lane >> 4, n = lane & 15;
  const unsigned lds0 = (unsigned)(uintptr_t)lds;
  *(volatile LAS unsigned*)(lds + 4 * n) = 0u;
  asm volatile("s_waitcnt lgkmcnt(0)" :: "s"(wqb), "s"(wob), "s"(out), "s"(ws), "s"(xa) : "memory");
  if (C::BAR_AT == -2) __builtin_amdgcn_s_barrier();
  const unsigned k0 = g * 256 + ((n ^ g) * 16);
#define SLOT_OFF(r_) (((((r_) & 0x70u) << 1) | ((r_) & 15u)) << 12)
  if (C::EARLY) {
#define E4(m_, so_) "s_mov_b32 m0, " m_ "\n\ts_mov_b32 %0, " so_ "\n\t" \
               "buffer_load_dwordx4 %2, %6, %0 offen nt lds\n\tbuffer_load_dwordx4 %3, %6, %0 offen offset:1024 nt lds\n\t" \
               "buffer_load_dwordx4 %4, %6, %0 offen offset:2048 nt lds\n\tbuffer_load_dwordx4 %5, %6, %0 offen offset:3072 nt lds\n\t"
    unsigned tmp;
    asm volatile("s_cmp_lg_u32 %1, 0\n\ts_cbranch_scc1 1f\n\t"
                 E4("%7", "0") E4("%7+4096", "65536") E4("%7+8192", "%8") E4("%7+12288", "%8+65536") E4("%7+16384", "%9") E4("%7+20480", "%9+65536")
                 "1:"
                 : "=&s"(tmp) : "s"(wave), "v"(k0), "v"(k0 ^ 64), "v"(k0 ^ 128), "v"(k0 ^ 192), "s"(mksrd(ak + (size_t)(b * NH + h) * SK * DH, SK * 256)),
                   "i"(L_RING), "i"(SLOT_OFF(C::MUL)), "i"(SLOT_OFF(2 * C::MUL)) : "memory", "scc");
  }
  if (C::EARLY > 1) {
    unsigned tmp;
    asm volatile("s_cmp_lt_u32 %1, 6\n\ts_cbranch_scc1 1f\n\t"
                 "s_mov_b32 m0, %4\n\ts_mov_b32 %0, 0\n\tbuffer_load_dwordx4 %2, %3, %0 offen lds\n\t"
                 "s_mov_b32 %0, 65536-1024\n\tbuffer_load_dwordx4 %2, %3, %0 offen offset:1024 lds\n\t"
                 "s_mov_b32 %0, 32768-2048\n\tbuffer_load_dwordx4 %2, %3, %0 offen offset:2048 lds\n\t"
                 "s_mov_b32 %0, 98304-3072\n\tbuffer_load_dwordx4 %2, %3, %0 offen offset:3072 lds\n\t"
                 "1:"
                 : "=&s"(tmp) : "s"(wave), "v"(lane * 16), "s"(mksrd(wqa + (size_t)(16 * jw + ((wave + 2) & 3)) * 4096 + xcd * 512, (C::DBG & (8 | 2)) ? 0 : 13 * 8192)),
                   "s"(L_S + ((wave + 2) & 3) * 4096) : "memory", "scc");
  }
#define START_BARRIER() do { if (C::BAR_AT != -2) __builtin_amdgcn_s_barrier(); CBAR(); } while (0)

  if (__builtin_expect(wave == 0, C::LFIRST)) {
    __builtin_amdgcn_s_setprio(3);
    const size_t ua = (size_t)(b * NH + h) * SK * DH, ub = (size_t)(b * HKV + xcd) * SK * DH;
    if (C::BAR_AT < 0) START_BARRIER();
    const unsigned vv = lane * 16;
    unsigned slot = lds0 + L_RING + (C::EARLY ? 3 * SLOT : 0);
    int limit = RING;
    u64 now = __builtin_amdgcn_s_memrealtime();
    unsigned base = (unsigned)now + C::PACE0, due = -(4 * C::PACE / 100);
    now = base + due;
    int t = C::EARLY ? 3 : 0;
#pragma unroll 1
    for (int ph = 0; ph < 4; ++ph) {
      const v4i rs = mksrd(ph == 0 ? ak + ua : ph == 1 ? bk + ub : ph == 2 ? bv + ub : av + ua, SK * 256);
      const int tend = ph == 0 ? 128 : ph == 1 ? 160 : ph == 2 ? 192 : NSL;
      const unsigned v0 = ph < 2 ? k0 : vv, v1 = ph < 2 ? k0 ^ 64 : vv, v2 = ph < 2 ? k0 ^ 128 : vv, v3 = ph < 2 ? k0 ^ 192 : vv;
      unsigned r = ((ph == 1 || ph == 2) ? sp * 32 * C::MUL : t * C::MUL) & 127u;
      if (ph == 3) r = 0;
#pragma unroll 1
      while (t < tend) {
        if (!(C::DBG & 4) && t + 3 >= limit) {
#pragma unroll 1
          for (unsigned spn = 0; spn < SPIN_CAP * 8; ++spn) {
            const v4u d = *(volatile LAS v4u*)(lds + L_DONE);
            if (C::ANCH) { const unsigned nb = lds_ld(lds, L_BASE); base = nb ? nb : base; }
            const int c = min(min(4 * (int)d[0], 4 * (int)d[1] + 1), min(4 * (int)d[2] + 2, 4 * (int)d[3] + 3));
            limit = __builtin_amdgcn_readfirstlane(c) + RING;
            if (t + 3 < limit) break;
            __builtin_amdgcn_s_sleep(1);
          }
        }
#define RUN4(N_) do { constexpr int LAG0 = ((N_) + 4 + 7) / 8, LAG1 = ((N_) + 7) / 8;     \
        static_assert(LAG1 >= 3 && LAG1 <= 4, "the published count never goes down"); \
        _Pragma("unroll 1") for (const int te = (t | 3) + 1; t < te; ++t) { \
          const unsigned so = SLOT_OFF(r); \
          if (C::BAR_AT >= 0 && t == C::BAR_AT) START_BARRIER(); \
          \
          dma4<N_>(slot, v0, v1, v2, v3, rs, so); \
          if (LAG0 == LAG1) lds_st(lds, L_FULL, (unsigned)max(t - LAG0 + 1, 0)); \
          dma4<N_>(slot + 4096, v0, v1, v2, v3, rs, so + 65536u); \
          if (LAG0 != LAG1) lds_st(lds, L_FULL, (unsigned)max(t - LAG1 + 1, 0)); \
          slot += SLOT; if (slot >= lds0 + L_RING + RING * SLOT) slot = lds0 + L_RING; \
          r = (r + C::MUL) & 127u; } } while (0)
        if (C::PACE == 0) RUN4(NFL);
        else {
          asm volatile("" : "+s"(now));
          if (C::ANCH) lds_st(lds, L_IMPL, (unsigned)now - due);
          const int lag = (int)((unsigned)now - due - base); due += ph == 0 ? 4 * C::PACE / 100 : 4 * C::PACE2 / 100;
          asm volatile("s_memrealtime %0" : "=s"(now));
          if (C::SPRINT && lag + (int)due >= 32 * (4 * C::PACE / 100) + 48 * (4 * C::PACE2 / 100) - C::SPR0) RUN4(C::SPRINT ? C::SPRINT : 24);
          else if (lag >= -C::DN0) {
            if (lag < C::UP0 || C::NUP == 0) RUN4(NFL); else if (lag < C::UP0 + C::USTEP || C::NUP == 1) RUN4(NFL + 1);
            else if (lag < C::UP0 + 2 * C::USTEP || C::NUP == 2) RUN4(NFL + 2); else RUN4(NFL + 3);
          } else if (lag >= -C::DN0 - C::STEP || C::NDN == 1) RUN4(NFL - 1);
          else if (lag >= -C::DN0 - 2 * C::STEP || C::NDN == 2) RUN4(NFL - 2); else RUN4(NFL - 3);
        }
      }
    }
    asm volatile("s_waitcnt vmcnt(24)" ::: "memory"); lds_st(lds, L_FULL, NSL - 3);
    asm volatile("s_waitcnt vmcnt(16)" ::: "memory"); lds_st(lds, L_FULL, NSL - 2);
    asm volatile("s_waitcnt vmcnt(8)" ::: "memory");  lds_st(lds, L_FULL, NSL - 1);
    asm volatile("s_waitcnt vmcnt(0)" ::: "memory");  lds_st(lds, L_FULL, NSL);
    STAMP(1); STAMP_OUT(0, 2); STAMP_OUT(13, 16); STAMP_OUT(17, 26);
  } else if (wave >= 2 && wave < 6) {
    START_BARRIER();
    if (C::DBG & 8) return;
    const int w = wave - 2, jj = n >> 2;
    int slot = L_RING + w * SLOT; unsigned full = 0, cnt = 0; int t = w;
#define SLOT_WAIT() do { if (full <= (unsigned)t) full = lds_wait_ge<1>(lds, L_FULL, (unsigned)t + 1u); CBAR(); } while (0)
#define SLOT_DONE() do { CBAR(); ++cnt; lds_st(lds, L_DONE + 4 * w, cnt);     \
        CBAR(); slot += 4 * SLOT; if (slot >= L_RING + RING * SLOT) slot -= RING * SLOT; t += 4; } while (0)
#define K_READ(T_) do { SLOT_WAIT(); _Pragma("unroll") for (int m_ = 0; m_ < 8; ++m_) T_[m_] = *LDSP(v4u, ((slot + kb) ^ (64 * (m_ & 3))) + (m_ >> 2) * 4096); SLOT_DONE(); } while (0)
    const int sw = L_S + w * 4096;
    {
      const int kb = n * 256 + ((g ^ n) * 16);
      v4u q[4];
      float mx = -3.0e38f;
      const int sa0 = sw + ((n & 3) * 8 + g * 2 + (jj & 1)) * 16;
      int sa = sa0;
#define K_MATH(T_, act_, step_) do { f32x4 s0 = {0.f, 0.f, 0.f, 0.f}, s1 = {0.f, 0.f, 0.f, 0.f}; \
        _Pragma("unroll") for (int m = 0; m < 4; ++m) { s0 = mfma32(T_[m], q[m], s0); s1 = mfma32(T_[4 + m], q[m], s1); }       \
        mx = fmaxf(fmaxf(fmaxf(fmaxf(s0[0], s0[1]), fmaxf(s0[2], s0[3])), fmaxf(fmaxf(s1[0], s1[1]), fmaxf(s1[2], s1[3]))), mx); \
        if (act_) *LDSP(f32x4, sa) = (jj & 1) ? s1 : s0; \
        sa += (step_); } while (0)
      {
        v4u P[PARK][8];
#pragma unroll
        for (int i = 0; i < PARK; ++i) K_READ(P[i]);
        lds_wait_ge<1>(lds, L_QRDY, 1u);
#pragma unroll
        for (int m = 0; m < 4; ++m) q[m] = *LDSP(v4u, L_QIMG + g * 16 + 64 * m);
        if (w == 0) STAMP(2);
#pragma unroll
        for (int i = 0; i < PARK; ++i) K_MATH(P[i], (n & 11) == 0, 128);
      }
#pragma unroll 1
      for (int ph = 0; ph < 2; ++ph) {
        const bool act = ph == 1 ? n < 8 : (n & 11) == 0;
        const int sstep = ph == 1 ? 512 : 128, tend = ph == 0 ? 128 : 160;
#pragma unroll 1
        while (t < tend) { v4u T[8]; K_READ(T); K_MATH(T, act, sstep); }
        {
          lds_wait_ge<1>(lds, L_QRDY + 4, 1u);
          const float Mh = xg_max(mx);
          const float M = __builtin_bit_cast(float, __builtin_amdgcn_ds_bpermute(((lane >> 3) & 3) * 4, __builtin_bit_cast(int, Mh)));
          const float mc = -M * SC2;
          float l = 0.f;
          CBAR();
#pragma unroll
          for (int j = 0; j < 4; ++j) {
            const f32x4 s = *LDSP(f32x4, sw + 16 * lane + 1024 * j);
            const float p0 = ex2(fmaf(s[0], SC2, mc)), p1 = ex2(fmaf(s[1], SC2, mc)), p2 = ex2(fmaf(s[2], SC2, mc)), p3 = ex2(fmaf(s[3], SC2, mc));
            l += (p0 + p1) + (p2 + p3);
            *LDSP(v2u, L_P + ph * 8192 + w * 2048 + 8 * lane + 512 * j) = v2u{cvtpk(p0, p1), cvtpk(p2, p3)};
          }
          l += dpp<XOR1>(l); l += dpp<XOR2>(l); l += dpp<HMIR>(l); l = x32_sum(l);
          const float la = x16_sum(l + dpp<ROR8>(l));
          if ((lane & 39) == 0) *LDSP(f32x2, L_ML + ph * 128 + w * 32 + lane) = f32x2{M, ph == 1 ? l : la};
          lds_inc(lds, L_MLC, lane);
        }
        mx = -3.0e38f; sa = sa0;
#pragma unroll
        for (int m = 0; m < 4; ++m) q[m] = *LDSP(v4u, L_QIMG + 256 + (n & 3) * 256 + g * 16 + 64 * m);
      }
    }
    {
      const unsigned Z = 0x0C0C0C0Cu;
      const unsigned el0 = jj == 0 ? 0x0C0C0100u : jj == 1 ? 0x01000C0Cu : Z, el1 = jj == 2 ? 0x0C0C0100u : jj == 3 ? 0x01000C0Cu : Z;
      const unsigned eh0 = jj == 0 ? 0x0C0C0302u : jj == 1 ? 0x03020C0Cu : Z, eh1 = jj == 2 ? 0x0C0C0302u : jj == 3 ? 0x03020C0Cu : Z;
      const int vb = g * 1024 + n * 16;
#pragma unroll 1
      for (int ph = 1; ph >= 0; --ph) {
        if (ph == 0) {
          lds_wait_ge<1>(lds, L_MLC, 8u);
          f32x2 ml[4];
#pragma unroll
          for (int u = 0; u < 4; ++u) ml[u] = *LDSP(f32x2, L_ML + u * 32);
          const float M = fmaxf(fmaxf(ml[0][0], ml[1][0]), fmaxf(ml[2][0], ml[3][0]));
          float L = 0.f;
#pragma unroll
          for (int u = 0; u < 4; ++u) L = fmaf(ex2((ml[u][0] - M) * SC2), ml[u][1], L);
          *LDSP(float, L_WGT + 64 + 4 * w) = ex2(((*LDSP(f32x2, L_ML + w * 32))[0] - M) * SC2) / L;
        }
        const int tend = ph == 1 ? 192 : NSL, pstep = ph == 1 ? 256 : 64;
        int pa = L_P + ph * 8192 + w * 2048 + (ph == 1 ? (n & 3) * 64 : 0) + g * 16;
        f32x4 a0 = {0, 0, 0, 0}, a1 = {0, 0, 0, 0};
#pragma unroll 1
        while (t < tend) {
          const v4u pp = *LDSP(v4u, pa); pa += pstep;
          v2u bf[8];
#pragma unroll
          for (int i = 0; i < 8; ++i) { const unsigned pk = pp[i >> 1]; bf[i] = v2u{__builtin_amdgcn_perm(pk, pk, (i & 1) ? eh0 : el0), __builtin_amdgcn_perm(pk, pk, (i & 1) ? eh1 : el1)}; }
#pragma unroll
          for (int i = 0; i < 8; ++i) asm volatile("" : "+v"(bf[i]));
          SLOT_WAIT();
          v4u T[8];
#pragma unroll
          for (int i = 0; i < 8; ++i) T[i] = *LDSP(v4u, slot + vb + (i >> 2) * 4096 + (i & 3) * 256);
          SLOT_DONE();
#pragma unroll
          for (int i = 0; i < 8; ++i) { a0 = mfma16(v2u{T[i][0], T[i][1]}, bf[i], a0); a1 = mfma16(v2u{T[i][2], T[i][3]}, bf[i], a1); }
        }
        if ((n & 3) == 0) *LDSP(v4u, L_HREC + 4096 + w * 256 + jj * 64 + g * 16) = v4u{cvtpk(a0[0], a1[0]), cvtpk(a0[1], a1[1]), cvtpk(a0[2], a1[2]), cvtpk(a0[3], a1[3])};
        if (ph == 1) {
          const int ro = sw + (n & 3) * 512 + jj * 128 + g * 32;
          *LDSP(f32x4, ro) = f32x4{a0[0], a1[0], a0[1], a1[1]};
          *LDSP(f32x4, ro + 16) = f32x4{a0[2], a1[2], a0[3], a1[3]};
        }
        lds_inc(lds, ph == 1 ? L_BCNT : L_RCNT + 4, lane);
      }
    }
    if (w == 3) { STAMP(3); STAMP_OUT(2, 4); }
  } else if (wave >= 6) {
    const int w = wave - 6;
    if (C::EARLY > 1) { START_BARRIER(); asm volatile("s_waitcnt vmcnt(0)" ::: "memory"); lds_inc(lds, L_PFC, lane); if (C::DBG & 8) return; }
#pragma unroll 1
    for (int mat = C::EARLY > 1 ? 1 : 0; mat < 2; ++mat) {
      if (mat) lds_wait_ge<16>(lds, L_FULL, 32u);
      const __amdgpu_buffer_rsrc_t rw = mk((mat ? wqb : wqa) + (size_t)(16 * jw + w) * 4096 + xcd * 512, (C::DBG & (8 | 2)) ? 0 : 13 * 8192);
      v4u d[4];
#pragma unroll
      for (int i = 0; i < 4; ++i) { d[i] = __builtin_amdgcn_raw_buffer_load_b128(rw, lane * 16, ((i & 1) * 8 + (i >> 1) * 4) * 8192, 0);
        if (C::PF_SER && mat) { __builtin_amdgcn_sched_barrier(0); __builtin_amdgcn_s_sleep(C::PF_SER); __builtin_amdgcn_sched_barrier(0); } }
      if (!mat) { __builtin_amdgcn_sched_barrier(0); START_BARRIER(); __builtin_amdgcn_sched_barrier(0); }
#pragma unroll
      for (int i = 0; i < 4; ++i) asm volatile("" :: "v"(d[i]));
      if (C::DBG & 8) return;
    }
    if (C::DBG & 16) return;
    const __amdgpu_buffer_rsrc_t ry = mk(ws + W_YA, (C::DBG & 1024) ? 0 : W_END - W_YA);
    const int yo = ((b * 32 + h) * 512 + 128 * w + 8 * n + 2 * g) * 8;
    lds_wait_ge<16>(lds, L_FULL, 72u);
#pragma unroll 1
    for (int pass = 0; pass < 2; ++pass) {
      const __amdgpu_buffer_rsrc_t rw = mk((pass == 0 ? wob : woa) + (size_t)xcd * 512 * 512, (C::DBG & 1) ? 0 : 512 * 1024);
      if (C::WO_PF) {
        v4u d[4];
#pragma unroll
        for (int i = 0; i < 4; ++i) { d[i] = __builtin_amdgcn_raw_buffer_load_b128(rw, (2 * b + (i >> 1) + 16 * (i & 1)) * 4096 + lane * 16, sp * 131072 + w * 1024, 0);
          if (C::PF_SER && (i & 1)) { __builtin_amdgcn_sched_barrier(0); __builtin_amdgcn_s_sleep(2 * C::PF_SER); __builtin_amdgcn_sched_barrier(0); } }
#pragma unroll
        for (int i = 0; i < 4; ++i) asm volatile("" :: "v"(d[i]));
        lds_wait_ge<16>(lds, L_FULL, lds_ld(lds, L_FULL) + C::WO_PF_GAP);
      }
      const int wv = (pass == 0 ? w * 131072 + sp * 256 : sp * 131072 + w * 256) + g * 32768 + n * 16;
      v4u F[4][8];
#pragma unroll
      for (int s = 0; s < 4; ++s) {
#pragma unroll
        for (int jp = 0; jp < 4; ++jp) {
          const v4u w0 = __builtin_amdgcn_raw_buffer_load_b128(rw, wv, (8 * jp + s) * 1024, 0);
          const v4u w1 = __builtin_amdgcn_raw_buffer_load_b128(rw, wv, (8 * jp + 4 + s) * 1024, 0);
#pragma unroll
          for (int d = 0; d < 4; ++d) {
            unsigned lo = __builtin_amdgcn_perm(w1[d], w0[d], 0x05040100u), hi = __builtin_amdgcn_perm(w1[d], w0[d], 0x07060302u);
            asm volatile("" : "+v"(lo), "+v"(hi));
            F[s][2 * d][jp] = lo; F[s][2 * d + 1][jp] = hi;
          }
          __builtin_amdgcn_sched_barrier(0);
          if (C::WO_SLEEP) __builtin_amdgcn_s_sleep(C::WO_SLEEP);
          __builtin_amdgcn_sched_barrier(0);
        }
#pragma unroll
        for (int c = 0; c < 8; ++c) asm volatile("" : "+v"(F[s][c]));
      }
      const int hr = pass == 0 ? w : 4;
      lds_wait_ge<1>(lds, L_RCNT + 4 * pass, 4u);
      v4u of[4];
#pragma unroll
      for (int s = 0; s < 4; ++s) of[s] = *LDSP(v4u, L_HREC + hr * 1024 + (n & 3) * 256 + s * 64 + g * 16);
      const unsigned tag = *LDSP(unsigned, L_TAG);
      const f32x4 wt = *LDSP(f32x4, L_WGT + hr * 16);
      f32x4 da = {0, 0, 0, 0}, dc = {0, 0, 0, 0};
      __builtin_amdgcn_sched_barrier(0);
#pragma unroll
      for (int cp = 0; cp < 4; ++cp) {
        f32x4 d0 = {0, 0, 0, 0}, d1 = {0, 0, 0, 0};
#pragma unroll
        for (int s = 0; s < 4; ++s) { d0 = mfma32(of[s], F[s][2 * cp], d0); d1 = mfma32(of[s], F[s][2 * cp + 1], d1); }
#pragma unroll
        for (int r = 0; r < 4; ++r) { da[r] = (cp == 0 || g == cp) ? d0[r] : da[r]; dc[r] = (cp == 0 || g == cp) ? d1[r] : dc[r]; }
      }
#pragma unroll
      for (int i = 0; i < 18; ++i) __builtin_amdgcn_sched_group_barrier(0x8, 1, 0);
#pragma unroll
      for (int i = 0; i < 14; ++i) { __builtin_amdgcn_sched_group_barrier(0x8, 1, 0); __builtin_amdgcn_sched_group_barrier(0x2, 1, 0); }
      const float ya = fmaf(wt[0], da[0], wt[1] * da[1]) + fmaf(wt[2], da[2], wt[3] * da[3]), yc = fmaf(wt[0], dc[0], wt[1] * dc[1]) + fmaf(wt[2], dc[2], wt[3] * dc[3]);
      st_sc1(v4u{__float_as_uint(ya), tag, __float_as_uint(yc), tag}, ry, pass == 0 ? OOB : yo, 0);
      if (pass == 0) *LDSP(f32x2, L_YS + w * 512 + 8 * lane) = f32x2{ya, yc};
      lds_inc(lds, L_YPUB, lane);
      if (w == 0) STAMP(4 + pass);
    }
    if (w == 0) STAMP_OUT(4, 6);
  } else {
    if (C::DBG & 8) { START_BARRIER(); return; }
    unsigned G;
    asm volatile("global_load_dword %0, %1, %2 sc1" : "=v"(G) : "v"(W_EPOCH), "s"(ws) : "memory");
    const int cb = xcd * 32 + jw;
    const __amdgpu_buffer_rsrc_t rs = mk(ws, W_END);
    unsigned tag = 0;
    const int ta = L_P + (8 * g + (n >> 2)) * 32 + (n & 3) * 8;
#define AN_PUT(k_, at_) do { if ((C::ANCH >> (k_)) & 1) { lds_wait_ge<16>(lds, L_FULL, at_); const unsigned im = lds_ld(lds, L_IMPL); \
      st_sc1(v4u{im, tag, im, tag}, rs, (sp == 0 && lane == 0) ? W_AN + (k_) * 1024 + (b * 8 + xcd) * 16 : OOB, 0); } } while (0)
#define AN_GET(k_, at_) do { if ((C::ANCH >> (k_)) & 1) { lds_wait_ge<16>(lds, L_FULL, at_); const unsigned im = lds_ld(lds, L_IMPL); \
      const v4u a = __builtin_amdgcn_raw_buffer_load_b128(rs, W_AN + (k_) * 1024 + lane * 16, 0, 0); \
      const bool okv = a[1] == tag && a[3] == tag; const int cntv = __builtin_popcountll(__builtin_amdgcn_ballot_w64(okv)); \
      float dv = okv ? (float)(int)(a[0] - im) : 0.f; \
      dv += dpp<XOR1>(dv); dv += dpp<XOR2>(dv); dv += dpp<HMIR>(dv); dv += dpp<ROR8>(dv); dv = x32_sum(x16_sum(dv)); \
      if (cntv >= 32) lds_st(lds, L_BASE, im + (int)(dv / (float)cntv) + C::MARGIN); } } while (0)
    int it = -1; OPAQUE_S(it);
    v4u xf[16];
#pragma unroll 1
    for (; it < 2; ++it) {
      if (it != 0) {
        if (it == 1) { AN_PUT(0, 30u); AN_GET(0, 46u); lds_wait_ge<16>(lds, L_FULL, 52u); }
        const v4i rx = mksrd(it == 1 ? xb : xa, (C::DBG & 128) ? 0 : 8 * 512 * 2);
#pragma unroll
        for (int s = 0; s < 16; ++s) asm volatile("buffer_load_dwordx4 %0, %1, %2, 0 offen offset:%3" : "+v"(xf[s]) : "v"(n * 1024 + 16 * g), "s"(rx), "i"(64 * s) : "memory");
      }
      if (it < 0) START_BARRIER();
      if (it >= 0) {
        if (it == 0) { if (C::EARLY > 1) lds_wait_ge<1>(lds, L_PFC, 4u); lds_wait_ge<1>(lds, L_FULL, C::T_SLICE); }
        const v4i rw = mksrd((it ? wqb : wqa) + cb * 16, (C::DBG & 32) ? 0 : 512 * 8192 - cb * 32);
        const unsigned vo = (lane >> 1) * 8192 + (lane & 1) * 16;
#pragma unroll
        for (int i = 0; i < 16; ++i) {
          if (C::SL_NT) asm volatile("s_mov_b32 m0, %0\n\ts_nop 0\n\tbuffer_load_dwordx4 %1, %2, %3 offen nt lds" :: "s"(lds0 + L_P + i * 1024), "v"(vo), "s"(rw), "s"(i * 32 * 8192) : "memory");
          else asm volatile("s_mov_b32 m0, %0\n\ts_nop 0\n\tbuffer_load_dwordx4 %1, %2, %3 offen lds" :: "s"(lds0 + L_P + i * 1024), "v"(vo), "s"(rw), "s"(i * 32 * 8192) : "memory");
          if (it) __builtin_amdgcn_s_sleep(C::WQB_SLEEP); else if (C::WQA_SLEEP) __builtin_amdgcn_s_sleep(C::WQA_SLEEP);
        }
        asm volatile("s_waitcnt vmcnt(0)\n\tv_readfirstlane_b32 %0, %1\n\ts_nop 1" : "=s"(tag) : "v"(G) : "memory");
        tag += 1u;
        if (it == 0) STAMP(6);
      }
#pragma unroll
      for (int s = 0; s < 16; ++s) asm volatile("" : "+v"(xf[s]));
      f32x4 d0 = {0, 0, 0, 0}, d1 = {0, 0, 0, 0};
      {
        v2u t[16][2];
#pragma unroll
        for (int s = 0; s < 16; ++s)
#pragma unroll
          for (int u = 0; u < 2; ++u) t[s][u] = __builtin_bit_cast(v2u, __builtin_amdgcn_ds_read_tr16_b64_v4i16(LDSP(s16x4, ta + s * 1024 + u * 128)));
        __builtin_amdgcn_sched_barrier(0);
#pragma unroll
        for (int s = 0; s < 16; s += 2) {
          d0 = mfma32(v4u{t[s][0][0], t[s][0][1], t[s][1][0], t[s][1][1]}, xf[s], d0);
          d1 = mfma32(v4u{t[s + 1][0][0], t[s + 1][0][1], t[s + 1][1][0], t[s + 1][1][1]}, xf[s + 1], d1);
        }
      }
      d0 += d1;
      st_sc1(v4u{cvtpk(d0[0], d0[1]), tag, cvtpk(d0[2], d0[3]), tag}, rs, (it >= 0 && n < 8 && !(C::DBG & 64)) ? W_QG + (it * 256 + cb) * 512 + n * 64 + g * 16 : OOB, 0);
      if (it == 0) STAMP(7);
      if (it < 0) continue;
      lds_wait_ge<4>(lds, L_FULL, it ? 104u : C::T_GATHER);
      const int go = (C::DBG & (64 | 256)) ? OOB : it == 0 ? (lane < 32 ? W_QG + (h * 8 + (lane >> 2)) * 512 + b * 64 + (lane & 3) * 16 : OOB) : W_QG + (256 + xcd * 32 + (lane >> 2)) * 512 + b * 64 + (lane & 3) * 16;
      const u64 need = (C::DBG & (64 | 256)) ? 0ull : it == 0 ? 0xFFFFFFFFull : ~0ull;
#pragma unroll 1
      for (unsigned spn = 0; spn < SPIN_CAP; ++spn) {
        const v4u v = ld_sc1(rs, go, 0), u = ld_sc1(rs, it == 1 ? go : OOB, 16 * 512);
        if (it == 1 || lane < 32) *LDSP(v2u, L_QIMG + (it == 1 ? 256 : 0) + 8 * lane) = v2u{v[0], v[2]};
        if (it == 1) *LDSP(v2u, L_QIMG + 768 + 8 * lane) = v2u{u[0], u[2]};
        if ((__builtin_amdgcn_ballot_w64(v[1] != tag || v[3] != tag || (it == 1 && (u[1] != tag || u[3] != tag))) & need) == 0) break;
        __builtin_amdgcn_s_sleep(24);
      }
      CBAR();
      lds_st(lds, L_QRDY + 4 * it, 1u);
      if (it == 0) STAMP(8);
    }
    lds_st(lds, L_TAG, tag);
    AN_PUT(1, 112u); AN_GET(1, 128u); AN_PUT(2, 176u); AN_GET(2, 190u);
    lds_wait_ge<16>(lds, L_BCNT, 4u);
    if (!(C::DBG & 512)) {
      f32x2 ml[4]; f32x4 ac[4][2];
#pragma unroll
      for (int u = 0; u < 4; ++u) { ml[u] = *LDSP(f32x2, L_ML + 128 + u * 32 + g * 8); ac[u][0] = *LDSP(f32x4, L_S + u * 4096 + 32 * lane); ac[u][1] = *LDSP(f32x4, L_S + u * 4096 + 32 * lane + 16); }
      const float M = fmaxf(fmaxf(ml[0][0], ml[1][0]), fmaxf(ml[2][0], ml[3][0]));
      float L = 0.f; f32x4 o0 = {0, 0, 0, 0}, o1 = {0, 0, 0, 0};
#pragma unroll
      for (int u = 0; u < 4; ++u) { const float f = ex2((ml[u][0] - M) * SC2); L = fmaf(f, ml[u][1], L); o0 += f * ac[u][0]; o1 += f * ac[u][1]; }
      const float inv = 1.f / L; o0 *= inv; o1 *= inv;
      st_sc1(v4u{cvtpk(o0[0], o0[1]), cvtpk(o0[2], o0[3]), cvtpk(o1[0], o1[1]), cvtpk(o1[2], o1[3])}, rs, W_PO + wg * 1024 + 16 * lane, 0);
      asm volatile("s_waitcnt vmcnt(0)" ::: "memory");
      st_sc1(v4u{__float_as_uint(M), tag, __float_as_uint(L), tag}, rs, n == 0 ? W_ML + wg * 128 + g * 16 : OOB, 0);
    }
    lds_wait_ge<16>(lds, L_FULL, 192u + C::T_MLPOLL);
    v4u mlv = {0, 0, 0, 0};
#pragma unroll 1
    for (unsigned spn = 0; spn < SPIN_CAP; ++spn) {
      if (C::DBG & 512) break;
      mlv = ld_sc1(rs, lane < 16 ? W_ML + (xcd + 8 * (4 * b + (lane & 3))) * 128 + (lane >> 2) * 16 : OOB, 0);
      if ((__builtin_amdgcn_ballot_w64(mlv[1] != tag || mlv[3] != tag) & 0xFFFFu) == 0u) break;
      __builtin_amdgcn_s_sleep(64);
    }
    __builtin_amdgcn_fence(__ATOMIC_ACQUIRE, "wavefront");
    {
      v4u p[4];
#pragma unroll
      for (int u = 0; u < 4; ++u) p[u] = ld_sc1(rs, (C::DBG & 512) ? OOB : W_PO + (xcd + 32 * b) * 1024 + 16 * lane, u * 8192);
      const float m = __uint_as_float(mlv[0]);
      float M = fmaxf(m, dpp<XOR1>(m)); M = fmaxf(M, dpp<XOR2>(M));
      const float e = ex2((m - M) * SC2) * __uint_as_float(mlv[2]);
      float L = e + dpp<XOR1>(e); L += dpp<XOR2>(L);
      if (lane < 16) *LDSP(float, L_WGT + 4 * lane) = e / L;
#pragma unroll
      for (int u = 0; u < 4; ++u) *LDSP(v4u, L_HREC + g * 1024 + u * 256 + n * 16) = p[u];
      CBAR(); lds_st(lds, L_RCNT, 4u);
    }
    STAMP(9);
    if (C::DBG & (16 | 1024)) return;
    lds_wait_ge<16>(lds, L_YPUB, 4u);
    {
      const f32x2 yb = (*LDSP(f32x2, L_YS + 8 * lane) + *LDSP(f32x2, L_YS + 512 + 8 * lane)) + (*LDSP(f32x2, L_YS + 1024 + 8 * lane) + *LDSP(f32x2, L_YS + 1536 + 8 * lane));
      AN_PUT(3, 244u); AN_GET(3, 258u);
      lds_wait_ge<8>(lds, L_FULL, NSL - C::T_YB);
      st_sc1(v4u{__float_as_uint(yb[0]), tag, __float_as_uint(yb[1]), tag}, rs, W_YB + wg * 1024 + (8 * n + 2 * g) * 8, 0);
    }
#pragma unroll 1
    for (int pass = 0; pass < 2; ++pass) {
      if (pass == 0) lds_wait_ge<8>(lds, L_FULL, NSL - C::T_YBR);
      else {
        if (C::Y_GATE) {
          const unsigned bs = lds_ld(lds, L_BASE), gate = bs + 32 * (4 * C::PACE / 100) + 48 * (4 * C::PACE2 / 100) + C::Y_GATE;
#pragma unroll 1
          for (unsigned spn = 0; bs && spn < 64 && (int)((unsigned)__builtin_amdgcn_s_memrealtime() - gate) < 0; ++spn) __builtin_amdgcn_s_sleep(1);
        }
        lds_wait_ge<1>(lds, L_YPUB, 8u);
      }
      const int yo = pass == 0 ? W_YB + ((lane >> 3) + 8 * (4 * b + (h >> 3))) * 1024 + (h & 7) * 128 + (lane & 7) * 16
                               : W_YA + (b * 32 * 512 + (lane >> 3) * 512 + 16 * h + 2 * (lane & 7)) * 8;
      v4u y[4]; bool ok[4];
#pragma unroll
      for (int i = 0; i < 4; ++i) { y[i] = v4u{0, 0, 0, 0}; ok[i] = pass == 0 && i > 0; }
#pragma unroll 1
      for (unsigned spn = 0; spn < SPIN_CAP; ++spn) {
        v4u z[4];
#pragma unroll
        for (int i = 0; i < 4; ++i) z[i] = ld_sc1(rs, ok[i] ? OOB : yo, i * 8 * 512 * 8);
        bool all = true;
#pragma unroll
        for (int i = 0; i < 4; ++i) { if (!ok[i]) { y[i] = z[i]; ok[i] = z[i][1] == tag && z[i][3] == tag; } all &= ok[i]; }
        if (__builtin_amdgcn_ballot_w64(!all) == 0) break;
        if (C::Y_SLEEP) __builtin_amdgcn_s_sleep(C::Y_SLEEP);
      }
      float s0 = (__uint_as_float(y[0][0]) + __uint_as_float(y[1][0])) + (__uint_as_float(y[2][0]) + __uint_as_float(y[3][0]));
      float s1 = (__uint_as_float(y[0][2]) + __uint_as_float(y[1][2])) + (__uint_as_float(y[2][2]) + __uint_as_float(y[3][2]));
      s0 += dpp<ROR8>(s0); s1 += dpp<ROR8>(s1);
      s0 = x32_sum(x16_sum(s0)); s1 = x32_sum(x16_sum(s1));
      if (lane < 8) *(GAS unsigned*)(out + b * 1024 + (pass == 0 ? 512 : 0) + 16 * h + 2 * lane) = cvtpk(s0, s1);
      STAMP(10 + pass);
    }
    if (wg == 0 && lane == 0) __hip_atomic_store((GAS unsigned*)(ws + W_EPOCH), tag, RLX_AGENT);
    STAMP_OUT(6, 12);
  }
}

template <class C>
static void launch_cfg(void* const* d_in, void* d_out, void* d_ws, hipStream_t stream) {
  (void)hipFuncSetAttribute((const void*)fused_decode<C>, hipFuncAttributeMaxDynamicSharedMemorySize, LDS_BYTES);
  hipLaunchKernelGGL(fused_decode<C>, dim3(256), dim3(NTHR), LDS_BYTES, stream,
                     (const u16*)d_in[0], (const u16*)d_in[1], (const u16*)d_in[2], (const u16*)d_in[3], (const u16*)d_in[4],
                     (const u16*)d_in[5], (const u16*)d_in[6], (const u16*)d_in[7], (const u16*)d_in[8], (const u16*)d_in[9],
                     (u16*)d_out, (unsigned char*)d_ws);
}
extern "C" void kernel_launch(void* const* d_in, const int* in_sizes, int n_in, void* d_out, int out_size, void* d_ws, size_t ws_size, hipStream_t stream) {
  (void)in_sizes; (void)n_in; (void)out_size; (void)ws_size;
  launch_cfg<CfgDefault>(d_in, d_out, d_ws, stream);
}
```

```cpp
#include <hip/hip_runtime.h>
#include <hip/hip_bf16.h>
#include <stdint.h>

typedef unsigned v4u __attribute__((ext_vector_type(4)));
typedef unsigned v2u __attribute__((ext_vector_type(2)));
typedef int v4i __attribute__((ext_vector_type(4)));
typedef float f32x4 __attribute__((ext_vector_type(4)));
typedef float f32x2 __attribute__((ext_vector_type(2)));
typedef short s16x4 __attribute__((ext_vector_type(4)));
typedef __bf16 bf16x8 __attribute__((ext_vector_type(8)));
typedef __bf16 bf16x2 __attribute__((ext_vector_type(2)));
typedef unsigned short u16;
typedef unsigned long long u64;
#define GAS __attribute__((address_space(1)))
#define LAS __attribute__((address_space(3)))

struct CfgDefault {
  static constexpr int NFL = 21;
  static constexpr int MUL = 11;
  static constexpr int PACE = 116, PACE2 = 112;
  static constexpr int PACE0 = 20, MARGIN = 20;
  static constexpr int DN = 40, U1 = 60, U2 = 110, U3 = 160, U4 = 220;
  static constexpr int T2 = 264, GAIN2 = 3;
  static constexpr int ANCH = 15;
  static constexpr int CODE_LINES = 143;
  static constexpr int T_SLICE = 3, T_GATHER = 17, T_ML = 216, T_YB = 314;
  static constexpr int TIGHT = 1, HALFV = 1, Q1 = 1;
  static constexpr int DBG = 0;
};

constexpr int SK = 4096, DH = 128, NH = 32, HKV = 8;
constexpr float SC2 = 0.088388347648318440f * 1.4426950408889634f;
constexpr int NWAVE = 10, NTHR = NWAVE * 64;
constexpr int NSL = 320;
constexpr int PARK = 4;
constexpr int RING = 15, SLOT = 8192;
constexpr int L_FULL = 0  , L_QRDY = 4  , L_BCNT = 12, L_DONE = 16  , L_RCNT = 32  , L_YPUB = 40  ,
              L_TAG = 48, L_MLC = 52, L_IMPL = 56, L_BASE = 60;
constexpr int L_QIMG = 64;
constexpr int L_WGT = L_QIMG + 1280;
constexpr int L_ML = L_WGT + 128;
constexpr int L_HREC = L_ML + 256;
constexpr int L_JUNK = 7936;
constexpr int L_S = 8192;
constexpr int L_P = L_S + 16384;
constexpr int L_YS = L_P + 8192;
constexpr int L_RING = L_P + 16384;
constexpr int LDS_BYTES = 160 * 1024;
static_assert(L_HREC % 16 == 0 && L_HREC + 5 * 1024 <= 7168 && L_RING + RING * SLOT <= LDS_BYTES, "lds map");
constexpr int W_EPOCH = 0;
constexpr int W_ML = 4096;
constexpr int W_AN = 49152;
constexpr int W_QG = 65536;
constexpr int W_PO = W_QG + 2 * 256 * 512;
constexpr int W_YB = W_PO + 256 * 1024;
constexpr int W_YA = W_YB + 256 * 1024;
constexpr int W_END = W_YA + 8 * 32 * 512 * 8;
constexpr size_t W_STAMP = 64u << 20;
constexpr unsigned SPIN_CAP = 1u << 20;
constexpr int OOB = 0x7FFFFF00;

__device__ __forceinline__ unsigned cvtpk(float lo, float hi) { return __builtin_bit_cast(unsigned, __builtin_convertvector(f32x2{lo, hi}, bf16x2)); }
__device__ __forceinline__ __amdgpu_buffer_rsrc_t mk(const void* p, unsigned bytes) { return __builtin_amdgcn_make_buffer_rsrc((void*)p, 0, bytes, 0x00020000); }
__device__ __forceinline__ v4i mksrd(const void* p, unsigned bytes) { return v4i{(int)(uintptr_t)p, (int)((uintptr_t)p >> 32), (int)bytes, 0x00020000}; }
__device__ __forceinline__ f32x4 mfma32(v4u a, v4u b, f32x4 c) { return __builtin_amdgcn_mfma_f32_16x16x32_bf16(__builtin_bit_cast(bf16x8, a), __builtin_bit_cast(bf16x8, b), c, 0, 0, 0); }
__device__ __forceinline__ f32x4 mfma16(v2u a, v2u b, f32x4 c) { return __builtin_amdgcn_mfma_f32_16x16x16bf16_1k(__builtin_bit_cast(s16x4, a), __builtin_bit_cast(s16x4, b), c, 0, 0, 0); }
__device__ __forceinline__ float ex2(float x) { return __builtin_amdgcn_exp2f(x); }
__device__ __forceinline__ v4u ld128(__amdgpu_buffer_rsrc_t r, int vo, int so) { return __builtin_amdgcn_raw_buffer_load_b128(r, vo, so, 0); }
__device__ __forceinline__ v4u ld_sc1(__amdgpu_buffer_rsrc_t r, int vo, int so) { return __builtin_amdgcn_raw_buffer_load_b128(r, vo, so, 16); }
__device__ __forceinline__ void st_sc1(v4u v, __amdgpu_buffer_rsrc_t r, int vo, int so) { __builtin_amdgcn_raw_buffer_store_b128(v, r, vo, so, 16); }
template <int CTRL> __device__ __forceinline__ float dpp(float x) { return __builtin_bit_cast(float, __builtin_amdgcn_mov_dpp(__builtin_bit_cast(int, x), CTRL, 0xf, 0xf, true)); }
constexpr int XOR1 = 0xB1, XOR2 = 0x4E, HMIR = 0x141, ROR8 = 0x128;
__device__ __forceinline__ float x16_sum(float x) { auto s = __builtin_amdgcn_permlane16_swap(__float_as_uint(x), __float_as_uint(x), false, false); return __uint_as_float(s[0]) + __uint_as_float(s[1]); }
__device__ __forceinline__ float x32_sum(float x) { auto s = __builtin_amdgcn_permlane32_swap(__float_as_uint(x), __float_as_uint(x), false, false); return __uint_as_float(s[0]) + __uint_as_float(s[1]); }
__device__ __forceinline__ float xg_max(float x) {
  auto s = __builtin_amdgcn_permlane16_swap(__float_as_uint(x), __float_as_uint(x), false, false);
  x = fmaxf(__uint_as_float(s[0]), __uint_as_float(s[1]));
  auto t = __builtin_amdgcn_permlane32_swap(__float_as_uint(x), __float_as_uint(x), false, false);
  return fmaxf(__uint_as_float(t[0]), __uint_as_float(t[1]));
}
#define CBAR() asm volatile("" ::: "memory")
#define LDSP(T, off) ((LAS T*)(lds + (off)))
__device__ __forceinline__ unsigned lds_ld(LAS unsigned char* lds, int off) { return __builtin_amdgcn_readfirstlane(*(volatile LAS unsigned*)(lds + off)); }
__device__ __forceinline__ void lds_st(LAS unsigned char* lds, int off, unsigned v) { *(volatile LAS unsigned*)(lds + off) = v; }
template <int SLP> __device__ __forceinline__ unsigned lds_wait_ge(LAS unsigned char* lds, int off, unsigned want) {
  unsigned v = lds_ld(lds, off);
#pragma unroll 1
  for (unsigned sp = 0; v < want && sp < SPIN_CAP * 32 / (SLP + 1); ++sp) { if (SLP) __builtin_amdgcn_s_sleep(SLP); v = lds_ld(lds, off); }
  CBAR();
  return v;
}
template <int SLP> __device__ __forceinline__ void wait_slots(LAS unsigned char* lds, unsigned slots) { lds_wait_ge<SLP>(lds, L_FULL, 2 * slots); }
__device__ __forceinline__ void lds_inc(LAS unsigned char* lds, int off, int lane) {
  CBAR();
  if (lane == 0) __hip_atomic_fetch_add(LDSP(unsigned, off), 1u, __ATOMIC_RELAXED, __HIP_MEMORY_SCOPE_WORKGROUP);
  CBAR();
}
#define STAMP(i) do {} while (0)
#define STAMP_OUT(lo, hi) do {} while (0)

template <int N> __device__ __forceinline__ void dma4(unsigned m0v, unsigned v0, unsigned v1, unsigned v2, unsigned v3, v4i srd, unsigned so) {
  asm volatile("s_mov_b32 m0, %0\n\t"
               "s_waitcnt vmcnt(%7)\n\tbuffer_load_dwordx4 %1, %5, %6 offen nt lds\n\t"
               "s_waitcnt vmcnt(%7)\n\tbuffer_load_dwordx4 %2, %5, %6 offen offset:1024 nt lds\n\t"
               "s_waitcnt vmcnt(%7)\n\tbuffer_load_dwordx4 %3, %5, %6 offen offset:2048 nt lds\n\t"
               "s_waitcnt vmcnt(%7)\n\tbuffer_load_dwordx4 %4, %5, %6 offen offset:3072 nt lds"
               :: "s"(m0v), "v"(v0), "v"(v1), "v"(v2), "v"(v3), "s"(srd), "s"(so), "i"(N - 1) : "memory");
}

template <class C>
__global__ void __launch_bounds__(NTHR, 1) fused_decode(const u16* xa, const u16* ak, const u16* av, const u16* wqa, const u16* woa,
                                                        const u16* xb, const u16* bk, const u16* bv, const u16* wqb, const u16* wob,
                                                        u16* out, unsigned char* ws) {
  constexpr int NFL = C::NFL;
  {
    u64 pc; unsigned tv;
    asm volatile("s_getpc_b64 %0\n\tv_min_u32 %1, %3, %2\n\ts_movk_i32 m0, %4\n\tv_lshlrev_b32 %1, 7, %1\n\tglobal_load_lds_dword %1, %0"
                 : "=&s"(pc), "=&v"(tv) : "v"(threadIdx.x), "i"(C::CODE_LINES - 1), "i"(L_JUNK));
    __builtin_amdgcn_sched_barrier(0);
  }
  extern __shared__ __attribute__((aligned(16))) unsigned char lds_raw[];
  LAS unsigned char* lds = (LAS unsigned char*)lds_raw;
  const int wg = blockIdx.x, lane = threadIdx.x & 63, wave = __builtin_amdgcn_readfirstlane(threadIdx.x >> 6);
  const int xcd = wg & 7, jw = wg >> 3, b = jw >> 2, sp = jw & 3, h = 4 * xcd + sp;
  const int g = lane >> 4, n = lane & 15;
  *(volatile LAS unsigned*)(lds + 4 * n) = 0u;
  asm volatile("s_waitcnt lgkmcnt(0)" :: "s"(wqb), "s"(wob), "s"(out), "s"(ws), "s"(xa) : "memory");
  __builtin_amdgcn_s_barrier();
  const unsigned k0 = g * 256 + ((n ^ g) * 16);
#define SLOT_OFF(r_) (((((r_) & 0x70u) << 1) | ((r_) & 15u)) << 12)
  {
#define E4(m_, so_) "s_mov_b32 m0, " m_ "\n\ts_mov_b32 %0, " so_ "\n\t" \
               "buffer_load_dwordx4 %2, %6, %0 offen nt lds\n\tbuffer_load_dwordx4 %3, %6, %0 offen offset:1024 nt lds\n\t" \
               "buffer_load_dwordx4 %4, %6, %0 offen offset:2048 nt lds\n\tbuffer_load_dwordx4 %5, %6, %0 offen offset:3072 nt lds\n\t"
    unsigned tmp;
    asm volatile("s_cmp_lg_u32 %1, 0\n\ts_cbranch_scc1 1f\n\t"
                 E4("%7", "0") E4("%7+4096", "65536") E4("%7+8192", "%8") E4("%7+12288", "%8+65536") E4("%7+16384", "%9") E4("%7+20480", "%9+65536")
                 "1:"
                 : "=&s"(tmp) : "s"(wave), "v"(k0), "v"(k0 ^ 64), "v"(k0 ^ 128), "v"(k0 ^ 192), "s"(mksrd(ak + (size_t)(b * NH + h) * SK * DH, SK * 256)),
                   "i"(L_RING), "i"(SLOT_OFF(C::MUL)), "i"(SLOT_OFF(2 * C::MUL)) : "memory", "scc");
  }

  if (wave == 0) {
    __builtin_amdgcn_s_setprio(3);
    const size_t ua = (size_t)(b * NH + h) * SK * DH, ub = (size_t)(b * HKV + xcd) * SK * DH;
    const unsigned vv = lane * 16;
    unsigned slot = L_RING + 3 * SLOT;
    int limit = RING;
    u64 now = __builtin_amdgcn_s_memrealtime();
    unsigned base = (unsigned)now + C::PACE0, due = -C::PACE;
    now = base + due;
    int t = 3;
#pragma unroll 1
    for (int ph = 0; ph < 4; ++ph) {
      const v4i rs = mksrd(ph == 0 ? ak + ua : ph == 1 ? bk + ub : ph == 2 ? bv + ub : av + ua, SK * 256);
      const int tend = ph == 0 ? 128 : ph == 1 ? 160 : ph == 2 ? 192 : NSL;
      const unsigned v0 = ph < 2 ? k0 : vv, v1 = ph < 2 ? k0 ^ 64 : vv, v2 = ph < 2 ? k0 ^ 128 : vv, v3 = ph < 2 ? k0 ^ 192 : vv;
      unsigned r = (ph == 0 ? 3 * C::MUL : ph == 3 ? 0 : sp * 32 * C::MUL) & 127u;
#pragma unroll 1
      while (t < tend) {
        if (!(C::DBG & 4) && t + 3 >= limit) {
#pragma unroll 1
          for (unsigned spn = 0; spn < SPIN_CAP * 16; ++spn) {
            const v4u d = *(volatile LAS v4u*)(lds + L_DONE);
            const unsigned nb = lds_ld(lds, L_BASE); base = nb ? nb : base;
            const int c = min(min(4 * (int)d[0], 4 * (int)d[1] + 1), min(4 * (int)d[2] + 2, 4 * (int)d[3] + 3));
            limit = __builtin_amdgcn_readfirstlane(c) + RING;
            if (t + 3 < limit) break;
            __builtin_amdgcn_s_sleep(1);
          }
        }
#define RUN4(N_) do { static_assert(((N_) + 3) / 4 >= 5 && ((N_) + 3) / 4 <= 7, "the published count never goes down"); \
        _Pragma("unroll 1") for (const int te = (t | 3) + 1; t < te; ++t) { \
          const unsigned so = SLOT_OFF(r); \
          dma4<N_>(slot, v0, v1, v2, v3, rs, so); \
          dma4<N_>(slot + 4096, v0, v1, v2, v3, rs, so + 65536u); \
          lds_st(lds, L_FULL, (unsigned)(2 * t + 2 - ((N_) + 3) / 4)); \
          slot += SLOT; if (slot >= L_RING + RING * SLOT) slot = L_RING; \
          r = (r + C::MUL) & 127u; } } while (0)
        asm volatile("" : "+s"(now));
        lds_st(lds, L_IMPL, (unsigned)now - due);
        const int lagr = (C::DBG & 2) ? 0 : (int)((unsigned)now - due - base), lag = t >= C::T2 ? lagr * C::GAIN2 : lagr;
        due += ph == 0 ? C::PACE : C::PACE2;
        asm volatile("s_memrealtime %0" : "=s"(now));
        if (lag < C::U1) { if (lag >= -C::DN) RUN4(NFL); else RUN4(NFL - 1); }
        else if (lagr < C::U3) { if (lag < C::U2) RUN4(NFL + 1); else RUN4(NFL + 2); }
        else if (lagr < C::U4) RUN4(NFL + 3); else RUN4(NFL + 5);
      }
    }
    asm volatile("s_waitcnt vmcnt(16)" ::: "memory"); lds_st(lds, L_FULL, 2 * NSL - 4);
    asm volatile("s_waitcnt vmcnt(12)" ::: "memory"); lds_st(lds, L_FULL, 2 * NSL - 3);
    asm volatile("s_waitcnt vmcnt(8)" ::: "memory");  lds_st(lds, L_FULL, 2 * NSL - 2);
    asm volatile("s_waitcnt vmcnt(4)" ::: "memory");  lds_st(lds, L_FULL, 2 * NSL - 1);
    asm volatile("s_waitcnt vmcnt(0)" ::: "memory");  lds_st(lds, L_FULL, 2 * NSL);
    STAMP(1); STAMP_OUT(0, 2); STAMP_OUT(17, 26);
  } else if (wave >= 2 && wave < 6) {
    if (C::DBG & 8) return;
    const int w = wave - 2, jj = n >> 2;
    int slot = L_RING + w * SLOT; unsigned full = 0, cnt = 0; int t = w;
#define HALF_WAIT(k_, slp_) do { if (full < (unsigned)(2 * t + (k_))) full = lds_wait_ge<slp_>(lds, L_FULL, (unsigned)(2 * t + (k_))); CBAR(); } while (0)
#define SLOT_DONE() do { CBAR(); ++cnt; lds_st(lds, L_DONE + 4 * w, cnt);     \
        CBAR(); slot += 4 * SLOT; if (slot >= L_RING + RING * SLOT) slot -= RING * SLOT; t += 4; } while (0)
#define K_READ(T_) do { HALF_WAIT(2, 1); _Pragma("unroll") for (int m_ = 0; m_ < 8; ++m_) T_[m_] = *LDSP(v4u, ((slot + kb) ^ (64 * (m_ & 3))) + (m_ >> 2) * 4096); SLOT_DONE(); } while (0)
    const int sw = L_S + w * 4096;
    {
      const int kb = n * 256 + ((g ^ n) * 16);
      v4u q[4];
      float mx = -3.0e38f;
      const int sa0 = sw + ((n & 3) * 8 + g * 2 + (jj & 1)) * 16;
      int sa = sa0;
#define K_MATH(T_, act_, step_) do { f32x4 s0 = {0.f, 0.f, 0.f, 0.f}, s1 = {0.f, 0.f, 0.f, 0.f}; \
        _Pragma("unroll") for (int m = 0; m < 4; ++m) { s0 = mfma32(T_[m], q[m], s0); s1 = mfma32(T_[4 + m], q[m], s1); }       \
        mx = fmaxf(fmaxf(fmaxf(fmaxf(s0[0], s0[1]), fmaxf(s0[2], s0[3])), fmaxf(fmaxf(s1[0], s1[1]), fmaxf(s1[2], s1[3]))), mx); \
        if (act_) *LDSP(f32x4, sa) = (jj & 1) ? s1 : s0; \
        sa += (step_); } while (0)
      {
        v4u P[PARK][8];
#pragma unroll
        for (int i = 0; i < PARK; ++i) K_READ(P[i]);
        lds_wait_ge<1>(lds, L_QRDY, 1u);
#pragma unroll
        for (int m = 0; m < 4; ++m) q[m] = *LDSP(v4u, L_QIMG + g * 16 + 64 * m);
#pragma unroll
        for (int i = 0; i < PARK; ++i) K_MATH(P[i], (n & 11) == 0, 128);
      }
#pragma unroll 1
      for (int ph = 0; ph < 2; ++ph) {
        const bool act = ph == 1 ? n < 8 : (n & 11) == 0;
        const int sstep = ph == 1 ? 512 : 128, tend = ph == 0 ? 128 : 160;
#pragma unroll 1
        while (t < tend) { v4u T[8]; K_READ(T); K_MATH(T, act, sstep); }
        lds_wait_ge<1>(lds, L_QRDY + 4, 1u);
        const float Mh = xg_max(mx);
        const float M = __builtin_bit_cast(float, __builtin_amdgcn_ds_bpermute(((lane >> 3) & 3) * 4, __builtin_bit_cast(int, Mh)));
        const float mc = -M * SC2;
        float l = 0.f;
        CBAR();
#pragma unroll
        for (int j = 0; j < 4; ++j) {
          const f32x4 s = *LDSP(f32x4, sw + 16 * lane + 1024 * j);
          const float p0 = ex2(fmaf(s[0], SC2, mc)), p1 = ex2(fmaf(s[1], SC2, mc)), p2 = ex2(fmaf(s[2], SC2, mc)), p3 = ex2(fmaf(s[3], SC2, mc));
          l += (p0 + p1) + (p2 + p3);
          *LDSP(v2u, L_P + ph * 8192 + w * 2048 + 8 * lane + 512 * j) = v2u{cvtpk(p0, p1), cvtpk(p2, p3)};
        }
        l += dpp<XOR1>(l); l += dpp<XOR2>(l); l += dpp<HMIR>(l); l = x32_sum(l);
        const float la = x16_sum(l + dpp<ROR8>(l));
        if ((lane & 39) == 0) *LDSP(f32x2, L_ML + ph * 128 + w * 32 + lane) = f32x2{M, ph == 1 ? l : la};
        lds_inc(lds, L_MLC, lane);
        mx = -3.0e38f; sa = sa0;
#pragma unroll
        for (int m = 0; m < 4; ++m) q[m] = *LDSP(v4u, L_QIMG + 256 + (n & 3) * 256 + g * 16 + 64 * m);
      }
    }
    {
      const unsigned Z = 0x0C0C0C0Cu;
      const unsigned el0 = jj == 0 ? 0x0C0C0100u : jj == 1 ? 0x01000C0Cu : Z, el1 = jj == 2 ? 0x0C0C0100u : jj == 3 ? 0x01000C0Cu : Z;
      const unsigned eh0 = jj == 0 ? 0x0C0C0302u : jj == 1 ? 0x03020C0Cu : Z, eh1 = jj == 2 ? 0x0C0C0302u : jj == 3 ? 0x03020C0Cu : Z;
      const int vb = g * 1024 + n * 16;
#pragma unroll 1
      for (int ph = 1; ph >= 0; --ph) {
        if (ph == 0) {
          lds_wait_ge<1>(lds, L_MLC, 8u);
          f32x2 ml[4];
#pragma unroll
          for (int u = 0; u < 4; ++u) ml[u] = *LDSP(f32x2, L_ML + u * 32);
          const float M = fmaxf(fmaxf(ml[0][0], ml[1][0]), fmaxf(ml[2][0], ml[3][0]));
          float L = 0.f;
#pragma unroll
          for (int u = 0; u < 4; ++u) L = fmaf(ex2((ml[u][0] - M) * SC2), ml[u][1], L);
          *LDSP(float, L_WGT + 64 + 4 * w) = ex2(((*LDSP(f32x2, L_ML + w * 32))[0] - M) * SC2) / L;
        }
        const int tend = ph == 1 ? 192 : NSL, pstep = ph == 1 ? 256 : 64;
        int pa = L_P + ph * 8192 + w * 2048 + (ph == 1 ? (n & 3) * 64 : 0) + g * 16;
        f32x4 a0 = {0, 0, 0, 0}, a1 = {0, 0, 0, 0};
#pragma unroll 1
        while (t < tend) {
          const v4u pp = *LDSP(v4u, pa); pa += pstep;
          v2u bf[8];
#pragma unroll
          for (int i = 0; i < 8; ++i) { const unsigned pk = pp[i >> 1]; bf[i] = v2u{__builtin_amdgcn_perm(pk, pk, (i & 1) ? eh0 : el0), __builtin_amdgcn_perm(pk, pk, (i & 1) ? eh1 : el1)}; }
#pragma unroll
          for (int i = 0; i < 8; ++i) asm volatile("" : "+v"(bf[i]));
          v4u T[8];
          HALF_WAIT(C::HALFV ? 1 : 2, 1);
#pragma unroll
          for (int i = 0; i < 4; ++i) T[i] = *LDSP(v4u, slot + vb + i * 256);
          if (C::HALFV) {
#pragma unroll
            for (int i = 0; i < 4; ++i) { a0 = mfma16(v2u{T[i][0], T[i][1]}, bf[i], a0); a1 = mfma16(v2u{T[i][2], T[i][3]}, bf[i], a1); }
            asm volatile("" : "+v"(a0), "+v"(a1));
            HALF_WAIT(2, C::TIGHT ? 0 : 1);
          }
#pragma unroll
          for (int i = 4; i < 8; ++i) T[i] = *LDSP(v4u, slot + vb + 4096 + (i & 3) * 256);
          SLOT_DONE();
#pragma unroll
          for (int i = C::HALFV ? 4 : 0; i < 8; ++i) { a0 = mfma16(v2u{T[i][0], T[i][1]}, bf[i], a0); a1 = mfma16(v2u{T[i][2], T[i][3]}, bf[i], a1); }
        }
        if ((n & 3) == 0) *LDSP(v4u, L_HREC + 4096 + w * 256 + jj * 64 + g * 16) = v4u{cvtpk(a0[0], a1[0]), cvtpk(a0[1], a1[1]), cvtpk(a0[2], a1[2]), cvtpk(a0[3], a1[3])};
        if (ph == 1) {
          const int ro = sw + (n & 3) * 512 + jj * 128 + g * 32;
          *LDSP(f32x4, ro) = f32x4{a0[0], a1[0], a0[1], a1[1]};
          *LDSP(f32x4, ro + 16) = f32x4{a0[2], a1[2], a0[3], a1[3]};
        }
        lds_inc(lds, ph == 1 ? L_BCNT : L_RCNT + 4, lane);
      }
    }
    if (w == 3) { STAMP(3); STAMP_OUT(3, 4); }
  } else if (wave >= 6) {
    if (C::DBG & 8) return;
    const int w = wave - 6;
#pragma unroll 1
    for (int mat = 0; mat < 2; ++mat) {
      if (mat) wait_slots<16>(lds, 32u);
      const __amdgpu_buffer_rsrc_t rw = mk((mat ? wqb : wqa) + (size_t)(16 * jw + w) * 4096 + xcd * 512, 13 * 8192);
      v4u d[4];
#pragma unroll
      for (int i = 0; i < 4; ++i) d[i] = ld128(rw, lane * 16, ((i & 1) * 8 + (i >> 1) * 4) * 8192);
#pragma unroll
      for (int i = 0; i < 4; ++i) asm volatile("" :: "v"(d[i]));
    }
    const __amdgpu_buffer_rsrc_t ry = mk(ws + W_YA, W_END - W_YA);
    const int yo = ((b * 32 + h) * 512 + 128 * w + 8 * n + 2 * g) * 8;
    wait_slots<16>(lds, 72u);
#pragma unroll 1
    for (int pass = 0; pass < 2; ++pass) {
      const __amdgpu_buffer_rsrc_t rw = mk((pass == 0 ? wob : woa) + (size_t)xcd * 512 * 512, 512 * 1024);
      {
        v4u d[4];
#pragma unroll
        for (int i = 0; i < 4; ++i) d[i] = ld128(rw, (2 * b + (i >> 1) + 16 * (i & 1)) * 4096 + lane * 16, sp * 131072 + w * 1024);
#pragma unroll
        for (int i = 0; i < 4; ++i) asm volatile("" :: "v"(d[i]));
        lds_wait_ge<16>(lds, L_FULL, min(lds_ld(lds, L_FULL) + 24u, 2u * NSL));
      }
      const int wv = (pass == 0 ? w * 131072 + sp * 256 : sp * 131072 + w * 256) + g * 32768 + n * 16;
      v4u F[4][8];
#pragma unroll
      for (int s = 0; s < 4; ++s) {
#pragma unroll
        for (int jp = 0; jp < 4; ++jp) {
          const v4u w0 = ld128(rw, wv, (8 * jp + s) * 1024), w1 = ld128(rw, wv, (8 * jp + 4 + s) * 1024);
#pragma unroll
          for (int d = 0; d < 4; ++d) {
            unsigned lo = __builtin_amdgcn_perm(w1[d], w0[d], 0x05040100u), hi = __builtin_amdgcn_perm(w1[d], w0[d], 0x07060302u);
            asm volatile("" : "+v"(lo), "+v"(hi));
            F[s][2 * d][jp] = lo; F[s][2 * d + 1][jp] = hi;
          }
          __builtin_amdgcn_sched_barrier(0);
        }
#pragma unroll
        for (int c = 0; c < 8; ++c) asm volatile("" : "+v"(F[s][c]));
      }
      const int hr = pass == 0 ? w : 4;
      if (C::TIGHT) lds_wait_ge<4>(lds, L_RCNT + 4 * pass, 2u);
      lds_wait_ge<C::TIGHT ? 0 : 1>(lds, L_RCNT + 4 * pass, 4u);
      v4u of[4];
#pragma unroll
      for (int s = 0; s < 4; ++s) of[s] = *LDSP(v4u, L_HREC + hr * 1024 + (n & 3) * 256 + s * 64 + g * 16);
      const unsigned tag = *LDSP(unsigned, L_TAG);
      const f32x4 wt = *LDSP(f32x4, L_WGT + hr * 16);
      f32x4 da = {0, 0, 0, 0}, dc = {0, 0, 0, 0};
      __builtin_amdgcn_sched_barrier(0);
#pragma unroll
      for (int cp = 0; cp < 4; ++cp) {
        f32x4 d0 = {0, 0, 0, 0}, d1 = {0, 0, 0, 0};
#pragma unroll
        for (int s = 0; s < 4; ++s) { d0 = mfma32(of[s], F[s][2 * cp], d0); d1 = mfma32(of[s], F[s][2 * cp + 1], d1); }
#pragma unroll
        for (int r = 0; r < 4; ++r) { da[r] = (cp == 0 || g == cp) ? d0[r] : da[r]; dc[r] = (cp == 0 || g == cp) ? d1[r] : dc[r]; }
      }
#pragma unroll
      for (int i = 0; i < 18; ++i) __builtin_amdgcn_sched_group_barrier(0x8, 1, 0);
#pragma unroll
      for (int i = 0; i < 14; ++i) { __builtin_amdgcn_sched_group_barrier(0x8, 1, 0); __builtin_amdgcn_sched_group_barrier(0x2, 1, 0); }
      const float ya = fmaf(wt[0], da[0], wt[1] * da[1]) + fmaf(wt[2], da[2], wt[3] * da[3]), yc = fmaf(wt[0], dc[0], wt[1] * dc[1]) + fmaf(wt[2], dc[2], wt[3] * dc[3]);
      st_sc1(v4u{__float_as_uint(ya), tag, __float_as_uint(yc), tag}, ry, pass == 0 ? OOB : yo, 0);
      if (pass == 0) *LDSP(f32x2, L_YS + w * 512 + 8 * lane) = f32x2{ya, yc};
      lds_inc(lds, L_YPUB + 4 * pass, lane);
      if (w == 0) STAMP(4 + pass);
    }
    if (w == 0) STAMP_OUT(4, 6);
  } else {
    if (C::DBG & 8) return;
    const unsigned tag = __builtin_amdgcn_readfirstlane(__hip_atomic_load((GAS unsigned*)(ws + W_EPOCH), __ATOMIC_RELAXED, __HIP_MEMORY_SCOPE_AGENT)) + 1u;
    lds_st(lds, L_TAG, tag);
    const __amdgpu_buffer_rsrc_t rs = mk(ws, W_END);
#define AN_PUT(k_, at_) do { if (((C::ANCH >> (k_)) & 1) && sp == 0) { wait_slots<16>(lds, at_); const unsigned im = lds_ld(lds, L_IMPL); \
      st_sc1(v4u{im, tag, im, tag}, rs, lane == 0 ? W_AN + (k_) * 1024 + (b * 8 + xcd) * 16 : OOB, 0); } } while (0)
#define AN_GET(k_, at_) do { if ((C::ANCH >> (k_)) & 1) { wait_slots<16>(lds, at_); const unsigned im = lds_ld(lds, L_IMPL); \
      const v4u a = ld128(rs, W_AN + (k_) * 1024 + lane * 16, 0); \
      const bool okv = a[1] == tag && a[3] == tag; const int cntv = __builtin_popcountll(__builtin_amdgcn_ballot_w64(okv)); \
      float dv = okv ? (float)(int)(a[0] - im) : 0.f; \
      dv += dpp<XOR1>(dv); dv += dpp<XOR2>(dv); dv += dpp<HMIR>(dv); dv += dpp<ROR8>(dv); dv = x32_sum(x16_sum(dv)); \
      if (cntv >= 32) lds_st(lds, L_BASE, im + (int)(dv / (float)cntv) + C::MARGIN); } } while (0)
#pragma unroll 1
    for (int it = 0; it < 2; ++it) {
      if (it == 1) { AN_PUT(0, 30u); AN_GET(0, 46u); }
      if (C::Q1 ? (jw >> 4) == it : true) {
        const int cb = C::Q1 ? xcd * 32 + 2 * (jw & 15) : xcd * 32 + jw;
        v4u xf[16];
        const __amdgpu_buffer_rsrc_t rx = mk(it ? xb : xa, 8 * 512 * 2);
        if (it) wait_slots<16>(lds, 52u);
#pragma unroll
        for (int s = 0; s < 16; ++s) xf[s] = ld128(rx, (C::Q1 ? n & 7 : n) * 1024 + 16 * g + 64 * s, 0);
        if (!it) wait_slots<1>(lds, C::T_SLICE);
        const v4i rw = mksrd((it ? wqb : wqa) + cb * 16, 512 * 8192 - cb * 32);
        f32x4 d0 = {0, 0, 0, 0}, d1 = {0, 0, 0, 0};
        if (C::Q1) {
          const unsigned vo = (lane >> 2) * 8192 + (lane & 3) * 16;
#pragma unroll
          for (int hf = 0; hf < 2; ++hf) {
            if (it || hf == 0) {
#pragma unroll 1
              for (int i = 0; i < (it ? 16 : 32); ++i) {
                asm volatile("s_mov_b32 m0, %0\n\ts_nop 0\n\tbuffer_load_dwordx4 %1, %2, %3 offen nt lds" :: "s"((it ? L_P : L_S) + i * 1024), "v"(vo), "s"(rw), "s"((hf * 16 + i) * 16 * 8192) : "memory");
                if (it) __builtin_amdgcn_s_sleep(6);
              }
              asm volatile("s_waitcnt vmcnt(0)" ::: "memory");
            }
            const int ta = ((it || hf) ? L_P : L_S) + (8 * g + (n >> 2)) * 64 + (n & 3) * 8;
            v2u t[8][4];
#pragma unroll
            for (int s = 0; s < 8; ++s)
#pragma unroll
              for (int u = 0; u < 4; ++u) t[s][u] = __builtin_bit_cast(v2u, __builtin_amdgcn_ds_read_tr16_b64_v4i16(LDSP(s16x4, ta + s * 2048 + (u & 1) * 256 + (u >> 1) * 32)));
            __builtin_amdgcn_sched_barrier(0);
#pragma unroll
            for (int s = 0; s < 8; ++s) {
              d0 = mfma32(v4u{t[s][0][0], t[s][0][1], t[s][1][0], t[s][1][1]}, xf[8 * hf + s], d0);
              d1 = mfma32(v4u{t[s][2][0], t[s][2][1], t[s][3][0], t[s][3][1]}, xf[8 * hf + s], d1);
            }
            asm volatile("" : "+v"(d0), "+v"(d1) :: "memory");
          }
          const f32x4 dd = n < 8 ? d0 : d1;
          st_sc1(v4u{cvtpk(dd[0], dd[1]), tag, cvtpk(dd[2], dd[3]), tag}, rs, W_QG + (it * 256 + cb) * 512 + n * 64 + g * 16, 0);
        } else {
          const unsigned vo = (lane >> 1) * 8192 + (lane & 1) * 16;
#pragma unroll 1
          for (int i = 0; i < 16; ++i) {
            asm volatile("s_mov_b32 m0, %0\n\ts_nop 0\n\tbuffer_load_dwordx4 %1, %2, %3 offen nt lds" :: "s"(L_P + i * 1024), "v"(vo), "s"(rw), "s"(i * 32 * 8192) : "memory");
            if (it) __builtin_amdgcn_s_sleep(12);
          }
          asm volatile("s_waitcnt vmcnt(0)" ::: "memory");
          const int ta = L_P + (8 * g + (n >> 2)) * 32 + (n & 3) * 8;
          v2u t[16][2];
#pragma unroll
          for (int s = 0; s < 16; ++s)
#pragma unroll
            for (int u = 0; u < 2; ++u) t[s][u] = __builtin_bit_cast(v2u, __builtin_amdgcn_ds_read_tr16_b64_v4i16(LDSP(s16x4, ta + s * 1024 + u * 128)));
          __builtin_amdgcn_sched_barrier(0);
#pragma unroll
          for (int s = 0; s < 16; s += 2) {
            d0 = mfma32(v4u{t[s][0][0], t[s][0][1], t[s][1][0], t[s][1][1]}, xf[s], d0);
            d1 = mfma32(v4u{t[s + 1][0][0], t[s + 1][0][1], t[s + 1][1][0], t[s + 1][1][1]}, xf[s + 1], d1);
          }
          d0 += d1;
          st_sc1(v4u{cvtpk(d0[0], d0[1]), tag, cvtpk(d0[2], d0[3]), tag}, rs, n < 8 ? W_QG + (it * 256 + cb) * 512 + n * 64 + g * 16 : OOB, 0);
        }
        if (it == 0) STAMP(7);
      }
      wait_slots<4>(lds, it ? 104u : C::T_GATHER);
      const int go = it == 0 ? (lane < 32 ? W_QG + (h * 8 + (lane >> 2)) * 512 + b * 64 + (lane & 3) * 16 : OOB) : W_QG + (256 + xcd * 32 + (lane >> 2)) * 512 + b * 64 + (lane & 3) * 16;
      const u64 need = it == 0 ? 0xFFFFFFFFull : ~0ull;
#pragma unroll 1
      for (unsigned spn = 0; spn < SPIN_CAP; ++spn) {
        const v4u v = ld_sc1(rs, go, 0), u = ld_sc1(rs, it == 1 ? go : OOB, 16 * 512);
        if (it == 1 || lane < 32) *LDSP(v2u, L_QIMG + (it == 1 ? 256 : 0) + 8 * lane) = v2u{v[0], v[2]};
        if (it == 1) *LDSP(v2u, L_QIMG + 768 + 8 * lane) = v2u{u[0], u[2]};
        if ((__builtin_amdgcn_ballot_w64(v[1] != tag || v[3] != tag || (it == 1 && (u[1] != tag || u[3] != tag))) & need) == 0) break;
        __builtin_amdgcn_s_sleep(24);
      }
      CBAR();
      lds_st(lds, L_QRDY + 4 * it, 1u);
      if (it == 0) STAMP(8);
    }
    AN_PUT(1, 112u); AN_GET(1, 128u); AN_PUT(2, 176u); AN_GET(2, 190u);
    lds_wait_ge<16>(lds, L_BCNT, 4u);
    {
      f32x2 ml[4]; f32x4 ac[4][2];
#pragma unroll
      for (int u = 0; u < 4; ++u) { ml[u] = *LDSP(f32x2, L_ML + 128 + u * 32 + g * 8); ac[u][0] = *LDSP(f32x4, L_S + u * 4096 + 32 * lane); ac[u][1] = *LDSP(f32x4, L_S + u * 4096 + 32 * lane + 16); }
      const float M = fmaxf(fmaxf(ml[0][0], ml[1][0]), fmaxf(ml[2][0], ml[3][0]));
      float L = 0.f; f32x4 o0 = {0, 0, 0, 0}, o1 = {0, 0, 0, 0};
#pragma unroll
      for (int u = 0; u < 4; ++u) { const float f = ex2((ml[u][0] - M) * SC2); L = fmaf(f, ml[u][1], L); o0 += f * ac[u][0]; o1 += f * ac[u][1]; }
      const float inv = 1.f / L; o0 *= inv; o1 *= inv;
      st_sc1(v4u{cvtpk(o0[0], o0[1]), cvtpk(o0[2], o0[3]), cvtpk(o1[0], o1[1]), cvtpk(o1[2], o1[3])}, rs, W_PO + wg * 1024 + 16 * lane, 0);
      asm volatile("s_waitcnt vmcnt(0)" ::: "memory");
      st_sc1(v4u{__float_as_uint(M), tag, __float_as_uint(L), tag}, rs, n == 0 ? W_ML + wg * 128 + g * 16 : OOB, 0);
    }
    wait_slots<16>(lds, C::T_ML);
    v4u mlv = {0, 0, 0, 0};
#pragma unroll 1
    for (unsigned spn = 0; spn < SPIN_CAP; ++spn) {
      mlv = ld_sc1(rs, lane < 16 ? W_ML + (xcd + 8 * (4 * b + (lane & 3))) * 128 + (lane >> 2) * 16 : OOB, 0);
      if ((__builtin_amdgcn_ballot_w64(mlv[1] != tag || mlv[3] != tag) & 0xFFFFu) == 0u) break;
      __builtin_amdgcn_s_sleep(64);
    }
    __builtin_amdgcn_fence(__ATOMIC_ACQUIRE, "wavefront");
    {
      v4u p[4];
#pragma unroll
      for (int u = 0; u < 4; ++u) p[u] = ld_sc1(rs, W_PO + (xcd + 32 * b) * 1024 + 16 * lane, u * 8192);
      const float m = __uint_as_float(mlv[0]);
      float M = fmaxf(m, dpp<XOR1>(m)); M = fmaxf(M, dpp<XOR2>(M));
      const float e = ex2((m - M) * SC2) * __uint_as_float(mlv[2]);
      float L = e + dpp<XOR1>(e); L += dpp<XOR2>(L);
      if (lane < 16) *LDSP(float, L_WGT + 4 * lane) = e / L;
#pragma unroll
      for (int u = 0; u < 4; ++u) *LDSP(v4u, L_HREC + g * 1024 + u * 256 + n * 16) = p[u];
      CBAR(); lds_st(lds, L_RCNT, 4u);
    }
    lds_wait_ge<16>(lds, L_YPUB, 4u);
    {
      const f32x2 yb = (*LDSP(f32x2, L_YS + 8 * lane) + *LDSP(f32x2, L_YS + 512 + 8 * lane)) + (*LDSP(f32x2, L_YS + 1024 + 8 * lane) + *LDSP(f32x2, L_YS + 1536 + 8 * lane));
      AN_PUT(3, 244u); AN_GET(3, 258u);
      wait_slots<8>(lds, C::T_YB);
      st_sc1(v4u{__float_as_uint(yb[0]), tag, __float_as_uint(yb[1]), tag}, rs, W_YB + wg * 1024 + (8 * n + 2 * g) * 8, 0);
    }
#pragma unroll 1
    for (int pass = 0; pass < 2; ++pass) {
      if (pass == 0) wait_slots<8>(lds, NSL - 1); else lds_wait_ge<1>(lds, L_YPUB + 4, 4u);
      const int yo = pass == 0 ? W_YB + ((lane >> 3) + 8 * (4 * b + (h >> 3))) * 1024 + (h & 7) * 128 + (lane & 7) * 16
                               : W_YA + (b * 32 * 512 + (lane >> 3) * 512 + 16 * h + 2 * (lane & 7)) * 8;
      v4u y[4]; bool ok[4];
#pragma unroll
      for (int i = 0; i < 4; ++i) { y[i] = v4u{0, 0, 0, 0}; ok[i] = pass == 0 && i > 0; }
#pragma unroll 1
      for (unsigned spn = 0; spn < SPIN_CAP; ++spn) {
        v4u z[4];
#pragma unroll
        for (int i = 0; i < 4; ++i) z[i] = ld_sc1(rs, ok[i] ? OOB : yo, i * 8 * 512 * 8);
        bool all = true;
#pragma unroll
        for (int i = 0; i < 4; ++i) { if (!ok[i]) { y[i] = z[i]; ok[i] = z[i][1] == tag && z[i][3] == tag; } all &= ok[i]; }
        if (__builtin_amdgcn_ballot_w64(!all) == 0) break;
        __builtin_amdgcn_s_sleep(2);
      }
      float s0 = (__uint_as_float(y[0][0]) + __uint_as_float(y[1][0])) + (__uint_as_float(y[2][0]) + __uint_as_float(y[3][0]));
      float s1 = (__uint_as_float(y[0][2]) + __uint_as_float(y[1][2])) + (__uint_as_float(y[2][2]) + __uint_as_float(y[3][2]));
      s0 += dpp<ROR8>(s0); s1 += dpp<ROR8>(s1);
      s0 = x32_sum(x16_sum(s0)); s1 = x32_sum(x16_sum(s1));
      if (lane < 8) *(GAS unsigned*)(out + b * 1024 + (pass == 0 ? 512 : 0) + 16 * h + 2 * lane) = cvtpk(s0, s1);
      STAMP(10 + pass);
    }
    if (wg == 0 && lane == 0) __hip_atomic_store((GAS unsigned*)(ws + W_EPOCH), tag, __ATOMIC_RELAXED, __HIP_MEMORY_SCOPE_AGENT);
    STAMP_OUT(7, 9); STAMP_OUT(10, 12);
  }
}

template <class C>
static void launch_cfg(void* const* d_in, void* d_out, void* d_ws, hipStream_t stream) {
  (void)hipFuncSetAttribute((const void*)fused_decode<C>, hipFuncAttributeMaxDynamicSharedMemorySize, LDS_BYTES);
  hipLaunchKernelGGL(fused_decode<C>, dim3(256), dim3(NTHR), LDS_BYTES, stream,
                     (const u16*)d_in[0], (const u16*)d_in[1], (const u16*)d_in[2], (const u16*)d_in[3], (const u16*)d_in[4],
                     (const u16*)d_in[5], (const u16*)d_in[6], (const u16*)d_in[7], (const u16*)d_in[8], (const u16*)d_in[9],
                     (u16*)d_out, (unsigned char*)d_ws);
}
extern "C" void kernel_launch(void* const* d_in, const int* in_sizes, int n_in, void* d_out, int out_size, void* d_ws, size_t ws_size, hipStream_t stream) {
  (void)in_sizes; (void)n_in; (void)out_size; (void)ws_size;
  launch_cfg<CfgDefault>(d_in, d_out, d_ws, stream);
}
```

```cpp
#include <hip/hip_runtime.h>
#include <hip/hip_bf16.h>
#include <stdint.h>

typedef unsigned v4u __attribute__((ext_vector_type(4)));
typedef unsigned v2u __attribute__((ext_vector_type(2)));
typedef int v4i __attribute__((ext_vector_type(4)));
typedef float f32x4 __attribute__((ext_vector_type(4)));
typedef float f32x2 __attribute__((ext_vector_type(2)));
typedef short s16x4 __attribute__((ext_vector_type(4)));
typedef __bf16 bf16x8 __attribute__((ext_vector_type(8)));
typedef __bf16 bf16x2 __attribute__((ext_vector_type(2)));
typedef unsigned short u16;
typedef unsigned long long u64;
#define GAS __attribute__((address_space(1)))
#define LAS __attribute__((address_space(3)))

struct CfgDefault {
  static constexpr int NFL = 20;
  static constexpr int MUL = 11;
  static constexpr int PACE = 116, PACE2 = 112;
  static constexpr int PACE0 = 20, MARGIN = 20;
  static constexpr int DN = 40, U1 = 60, U2 = 110, U3 = 160, U4 = 220;
  static constexpr int T2 = 264, GAIN2 = 3;
  static constexpr int ANCH = 15;
  static constexpr int CODE_LINES = 144;
  static constexpr int T_SLICE = 3, T_GATHER = 17, T_ML = 216, T_YB = 314;
  static constexpr int TIGHT = 1, HALFV = 1, Q1 = 1;
  static constexpr int DBG = 0;
};

constexpr int SK = 4096, DH = 128, NH = 32, HKV = 8;
constexpr float SC2 = 0.088388347648318440f * 1.4426950408889634f;
constexpr int NWAVE = 10, NTHR = NWAVE * 64;
constexpr int NSL = 320;
constexpr int PARK = 4;
constexpr int RING = 15, SLOT = 8192;
constexpr int L_FULL = 0  , L_QRDY = 4  , L_BCNT = 12, L_DONE = 16  , L_RCNT = 32  , L_YPUB = 40  ,
              L_TAG = 48, L_MLC = 52, L_IMPL = 56, L_BASE = 60;
constexpr int L_QIMG = 64;
constexpr int L_WGT = L_QIMG + 1280;
constexpr int L_ML = L_WGT + 128;
constexpr int L_HREC = L_ML + 256;
constexpr int L_JUNK = 7936;
constexpr int L_S = 8192;
constexpr int L_P = L_S + 16384;
constexpr int L_YS = L_P + 8192;
constexpr int L_RING = L_P + 16384;
constexpr int LDS_BYTES = 160 * 1024;
static_assert(L_HREC % 16 == 0 && L_HREC + 5 * 1024 <= 7168 && L_RING + RING * SLOT <= LDS_BYTES, "lds map");
constexpr int W_EPOCH = 0;
constexpr int W_ML = 4096;
constexpr int W_AN = 49152;
constexpr int W_QG = 65536;
constexpr int W_PO = W_QG + 2 * 256 * 512;
constexpr int W_YB = W_PO + 256 * 1024;
constexpr int W_YA = W_YB + 256 * 1024;
constexpr int W_END = W_YA + 8 * 32 * 512 * 8;
constexpr size_t W_STAMP = 64u << 20;
constexpr unsigned SPIN_CAP = 1u << 20;
constexpr int OOB = 0x7FFFFF00;

__device__ __forceinline__ unsigned cvtpk(float lo, float hi) { return __builtin_bit_cast(unsigned, __builtin_convertvector(f32x2{lo, hi}, bf16x2)); }
__device__ __forceinline__ __amdgpu_buffer_rsrc_t mk(const void* p, unsigned bytes) { return __builtin_amdgcn_make_buffer_rsrc((void*)p, 0, bytes, 0x00020000); }
__device__ __forceinline__ v4i mksrd(const void* p, unsigned bytes) { return v4i{(int)(uintptr_t)p, (int)((uintptr_t)p >> 32), (int)bytes, 0x00020000}; }
__device__ __forceinline__ f32x4 mfma32(v4u a, v4u b, f32x4 c) { return __builtin_amdgcn_mfma_f32_16x16x32_bf16(__builtin_bit_cast(bf16x8, a), __builtin_bit_cast(bf16x8, b), c, 0, 0, 0); }
__device__ __forceinline__ f32x4 mfma16(v2u a, v2u b, f32x4 c) { return __builtin_amdgcn_mfma_f32_16x16x16bf16_1k(__builtin_bit_cast(s16x4, a), __builtin_bit_cast(s16x4, b), c, 0, 0, 0); }
__device__ __forceinline__ float ex2(float x) { return __builtin_amdgcn_exp2f(x); }
__device__ __forceinline__ v4u ld128(__amdgpu_buffer_rsrc_t r, int vo, int so) { return __builtin_amdgcn_raw_buffer_load_b128(r, vo, so, 0); }
__device__ __forceinline__ v4u ld_sc1(__amdgpu_buffer_rsrc_t r, int vo, int so) { return __builtin_amdgcn_raw_buffer_load_b128(r, vo, so, 16); }
__device__ __forceinline__ void st_sc1(v4u v, __amdgpu_buffer_rsrc_t r, int vo, int so) { __builtin_amdgcn_raw_buffer_store_b128(v, r, vo, so, 16); }
template <int CTRL> __device__ __forceinline__ float dpp(float x) { return __builtin_bit_cast(float, __builtin_amdgcn_mov_dpp(__builtin_bit_cast(int, x), CTRL, 0xf, 0xf, true)); }
constexpr int XOR1 = 0xB1, XOR2 = 0x4E, HMIR = 0x141, ROR8 = 0x128;
__device__ __forceinline__ float x16_sum(float x) { auto s = __builtin_amdgcn_permlane16_swap(__float_as_uint(x), __float_as_uint(x), false, false); return __uint_as_float(s[0]) + __uint_as_float(s[1]); }
__device__ __forceinline__ float x32_sum(float x) { auto s = __builtin_amdgcn_permlane32_swap(__float_as_uint(x), __float_as_uint(x), false, false); return __uint_as_float(s[0]) + __uint_as_float(s[1]); }
__device__ __forceinline__ float xg_max(float x) {
  auto s = __builtin_amdgcn_permlane16_swap(__float_as_uint(x), __float_as_uint(x), false, false);
  x = fmaxf(__uint_as_float(s[0]), __uint_as_float(s[1]));
  auto t = __builtin_amdgcn_permlane32_swap(__float_as_uint(x), __float_as_uint(x), false, false);
  return fmaxf(__uint_as_float(t[0]), __uint_as_float(t[1]));
}
#define CBAR() asm volatile("" ::: "memory")
#define LDSP(T, off) ((LAS T*)(lds + (off)))
__device__ __forceinline__ unsigned lds_ld(LAS unsigned char* lds, int off) { return __builtin_amdgcn_readfirstlane(*(volatile LAS unsigned*)(lds + off)); }
__device__ __forceinline__ void lds_st(LAS unsigned char* lds, int off, unsigned v) { *(volatile LAS unsigned*)(lds + off) = v; }
template <int SLP> __device__ __forceinline__ unsigned lds_wait_ge(LAS unsigned char* lds, int off, unsigned want) {
  unsigned v = lds_ld(lds, off);
#pragma unroll 1
  for (unsigned sp = 0; v < want && sp < SPIN_CAP * 32 / (SLP + 1); ++sp) { if (SLP) __builtin_amdgcn_s_sleep(SLP); v = lds_ld(lds, off); }
  CBAR();
  return v;
}
template <int SLP> __device__ __forceinline__ void wait_slots(LAS unsigned char* lds, unsigned slots) { lds_wait_ge<SLP>(lds, L_FULL, 2 * slots); }
__device__ __forceinline__ void lds_inc(LAS unsigned char* lds, int off, int lane) {
  CBAR();
  if (lane == 0) __hip_atomic_fetch_add(LDSP(unsigned, off), 1u, __ATOMIC_RELAXED, __HIP_MEMORY_SCOPE_WORKGROUP);
  CBAR();
}
#define STAMP(i) do {} while (0)
#define STAMP_OUT(lo, hi) do {} while (0)

template <int N> __device__ __forceinline__ void dma4(unsigned m0v, unsigned v0, unsigned v1, unsigned v2, unsigned v3, v4i srd, unsigned so) {
  asm volatile("s_mov_b32 m0, %0\n\t"
               "s_waitcnt vmcnt(%7)\n\tbuffer_load_dwordx4 %1, %5, %6 offen nt lds\n\t"
               "s_waitcnt vmcnt(%7)\n\tbuffer_load_dwordx4 %2, %5, %6 offen offset:1024 nt lds\n\t"
               "s_waitcnt vmcnt(%7)\n\tbuffer_load_dwordx4 %3, %5, %6 offen offset:2048 nt lds\n\t"
               "s_waitcnt vmcnt(%7)\n\tbuffer_load_dwordx4 %4, %5, %6 offen offset:3072 nt lds"
               :: "s"(m0v), "v"(v0), "v"(v1), "v"(v2), "v"(v3), "s"(srd), "s"(so), "i"(N - 1) : "memory");
}

template <class C>
__global__ void __launch_bounds__(NTHR, 1) fused_decode(const u16* xa, const u16* ak, const u16* av, const u16* wqa, const u16* woa,
                                                        const u16* xb, const u16* bk, const u16* bv, const u16* wqb, const u16* wob,
                                                        u16* out, unsigned char* ws) {
  u64 tstart; asm volatile("s_memrealtime %0" : "=s"(tstart) :: "memory");
  constexpr int NFL = C::NFL;
  {
    u64 pc; unsigned tv;
    asm volatile("s_getpc_b64 %0\n\tv_min_u32 %1, %3, %2\n\ts_movk_i32 m0, %4\n\tv_lshlrev_b32 %1, 7, %1\n\tglobal_load_lds_dword %1, %0"
                 : "=&s"(pc), "=&v"(tv) : "v"(threadIdx.x), "i"(C::CODE_LINES - 1), "i"(L_JUNK));
    __builtin_amdgcn_sched_barrier(0);
  }
  extern __shared__ __attribute__((aligned(16))) unsigned char lds_raw[];
  LAS unsigned char* lds = (LAS unsigned char*)lds_raw;
  const int wg = blockIdx.x, lane = threadIdx.x & 63, wave = __builtin_amdgcn_readfirstlane(threadIdx.x >> 6);
  const int xcd = wg & 7, jw = wg >> 3, b = jw >> 2, sp = jw & 3, h = 4 * xcd + sp;
  const int g = lane >> 4, n = lane & 15;
  *(volatile LAS unsigned*)(lds + 4 * n) = 0u;
  asm volatile("s_waitcnt lgkmcnt(0)" : "+s"(tstart) : "s"(wqb), "s"(wob), "s"(out), "s"(ws), "s"(xa) : "memory");
  __builtin_amdgcn_s_barrier();
  const unsigned k0 = g * 256 + ((n ^ g) * 16);
#define SLOT_OFF(r_) (((((r_) & 0x70u) << 1) | ((r_) & 15u)) << 12)
  {
#define E4(m_, so_) "s_mov_b32 m0, " m_ "\n\ts_mov_b32 %0, " so_ "\n\t" \
               "buffer_load_dwordx4 %2, %6, %0 offen nt lds\n\tbuffer_load_dwordx4 %3, %6, %0 offen offset:1024 nt lds\n\t" \
               "buffer_load_dwordx4 %4, %6, %0 offen offset:2048 nt lds\n\tbuffer_load_dwordx4 %5, %6, %0 offen offset:3072 nt lds\n\t"
    unsigned tmp;
    asm volatile("s_cmp_lg_u32 %1, 0\n\ts_cbranch_scc1 1f\n\t"
                 E4("%7", "0") E4("%7+4096", "65536") E4("%7+8192", "%8") E4("%7+12288", "%8+65536") E4("%7+16384", "%9") E4("%7+20480", "%9+65536")
                 "1:"
                 : "=&s"(tmp) : "s"(wave), "v"(k0), "v"(k0 ^ 64), "v"(k0 ^ 128), "v"(k0 ^ 192), "s"(mksrd(ak + (size_t)(b * NH + h) * SK * DH, SK * 256)),
                   "i"(L_RING), "i"(SLOT_OFF(C::MUL)), "i"(SLOT_OFF(2 * C::MUL)) : "memory", "scc");
  }

  if (wave == 0) {
    __builtin_amdgcn_s_setprio(3);
    const size_t ua = (size_t)(b * NH + h) * SK * DH, ub = (size_t)(b * HKV + xcd) * SK * DH;
    const unsigned vv = lane * 16;
    unsigned slot = L_RING + 3 * SLOT;
    int limit = RING;
    u64 now = __builtin_amdgcn_s_memrealtime();
    unsigned base = (unsigned)now + C::PACE0, due = -C::PACE;
    now = base + due;
    int t = 3;
#pragma unroll 1
    for (int ph = 0; ph < 4; ++ph) {
      const v4i rs = mksrd(ph == 0 ? ak + ua : ph == 1 ? bk + ub : ph == 2 ? bv + ub : av + ua, SK * 256);
      const int tend = ph == 0 ? 128 : ph == 1 ? 160 : ph == 2 ? 192 : NSL;
      const unsigned v0 = ph < 2 ? k0 : vv, v1 = ph < 2 ? k0 ^ 64 : vv, v2 = ph < 2 ? k0 ^ 128 : vv, v3 = ph < 2 ? k0 ^ 192 : vv;
      unsigned r = (ph == 0 ? 3 * C::MUL : ph == 3 ? 0 : sp * 32 * C::MUL) & 127u;
#pragma unroll 1
      while (t < tend) {
        if (!(C::DBG & 4) && t + 3 >= limit) {
#pragma unroll 1
          for (unsigned spn = 0; spn < SPIN_CAP * 16; ++spn) {
            const v4u d = *(volatile LAS v4u*)(lds + L_DONE);
            const unsigned nb = lds_ld(lds, L_BASE); base = nb ? nb : base;
            const int c = min(min(4 * (int)d[0], 4 * (int)d[1] + 1), min(4 * (int)d[2] + 2, 4 * (int)d[3] + 3));
            limit = __builtin_amdgcn_readfirstlane(c) + RING;
            if (t + 3 < limit) break;
            __builtin_amdgcn_s_sleep(1);
          }
        }
        if (t == 4) { lds_st(lds, 7168, (unsigned)__builtin_amdgcn_s_memrealtime() - (unsigned)tstart); }
#define RUN4(N_) do { static_assert(((N_) + 3) / 4 >= 5 && ((N_) + 3) / 4 <= 7, "the published count never goes down"); \
        _Pragma("unroll 1") for (const int te = (t | 3) + 1; t < te; ++t) { \
          const unsigned so = SLOT_OFF(r); \
          dma4<N_>(slot, v0, v1, v2, v3, rs, so); \
          dma4<N_>(slot + 4096, v0, v1, v2, v3, rs, so + 65536u); \
          lds_st(lds, L_FULL, (unsigned)(2 * t + 2 - ((N_) + 3) / 4)); \
          slot += SLOT; if (slot >= L_RING + RING * SLOT) slot = L_RING; \
          r = (r + C::MUL) & 127u; } } while (0)
        asm volatile("" : "+s"(now));
        lds_st(lds, L_IMPL, (unsigned)now - due);
        const int lagr = (C::DBG & 2) ? 0 : (int)((unsigned)now - due - base), lag = t >= C::T2 ? lagr * C::GAIN2 : lagr;
        due += ph == 0 ? C::PACE : C::PACE2;
        asm volatile("s_memrealtime %0" : "=s"(now));
        if (lag < C::U1) { if (lag >= -C::DN) RUN4(NFL); else RUN4(NFL - 1); }
        else if (lagr < C::U3) { if (lag < C::U2) RUN4(NFL + 1); else RUN4(NFL + 2); }
        else if (lagr < C::U4) RUN4(NFL + 3); else RUN4(NFL + 5);
      }
    }
    asm volatile("s_waitcnt vmcnt(16)" ::: "memory"); lds_st(lds, L_FULL, 2 * NSL - 4);
    asm volatile("s_waitcnt vmcnt(12)" ::: "memory"); lds_st(lds, L_FULL, 2 * NSL - 3);
    asm volatile("s_waitcnt vmcnt(8)" ::: "memory");  lds_st(lds, L_FULL, 2 * NSL - 2);
    asm volatile("s_waitcnt vmcnt(4)" ::: "memory");  lds_st(lds, L_FULL, 2 * NSL - 1);
    asm volatile("s_waitcnt vmcnt(0)" ::: "memory");  lds_st(lds, L_FULL, 2 * NSL);
    STAMP(1); STAMP_OUT(0, 2); STAMP_OUT(17, 26);
  } else if (wave >= 2 && wave < 6) {
    if (C::DBG & 8) return;
    const int w = wave - 2, jj = n >> 2;
    int slot = L_RING + w * SLOT; unsigned full = 0, cnt = 0; int t = w;
#define HALF_WAIT(k_, slp_) do { if (full < (unsigned)(2 * t + (k_))) full = lds_wait_ge<slp_>(lds, L_FULL, (unsigned)(2 * t + (k_))); CBAR(); } while (0)
#define SLOT_DONE() do { CBAR(); ++cnt; lds_st(lds, L_DONE + 4 * w, cnt);     \
        CBAR(); slot += 4 * SLOT; if (slot >= L_RING + RING * SLOT) slot -= RING * SLOT; t += 4; } while (0)
#define K_READ(T_) do { HALF_WAIT(2, 1); _Pragma("unroll") for (int m_ = 0; m_ < 8; ++m_) T_[m_] = *LDSP(v4u, ((slot + kb) ^ (64 * (m_ & 3))) + (m_ >> 2) * 4096); SLOT_DONE(); } while (0)
    const int sw = L_S + w * 4096;
    {
      const int kb = n * 256 + ((g ^ n) * 16);
      v4u q[4];
      float mx = -3.0e38f;
      const int sa0 = sw + ((n & 3) * 8 + g * 2 + (jj & 1)) * 16;
      int sa = sa0;
#define K_MATH(T_, act_, step_) do { f32x4 s0 = {0.f, 0.f, 0.f, 0.f}, s1 = {0.f, 0.f, 0.f, 0.f}; \
        _Pragma("unroll") for (int m = 0; m < 4; ++m) { s0 = mfma32(T_[m], q[m], s0); s1 = mfma32(T_[4 + m], q[m], s1); }       \
        mx = fmaxf(fmaxf(fmaxf(fmaxf(s0[0], s0[1]), fmaxf(s0[2], s0[3])), fmaxf(fmaxf(s1[0], s1[1]), fmaxf(s1[2], s1[3]))), mx); \
        if (act_) *LDSP(f32x4, sa) = (jj & 1) ? s1 : s0; \
        sa += (step_); } while (0)
      {
        v4u P[PARK][8];
#pragma unroll
        for (int i = 0; i < PARK; ++i) K_READ(P[i]);
        lds_wait_ge<1>(lds, L_QRDY, 1u);
#pragma unroll
        for (int m = 0; m < 4; ++m) q[m] = *LDSP(v4u, L_QIMG + g * 16 + 64 * m);
#pragma unroll
        for (int i = 0; i < PARK; ++i) K_MATH(P[i], (n & 11) == 0, 128);
      }
#pragma unroll 1
      for (int ph = 0; ph < 2; ++ph) {
        const bool act = ph == 1 ? n < 8 : (n & 11) == 0;
        const int sstep = ph == 1 ? 512 : 128, tend = ph == 0 ? 128 : 160;
#pragma unroll 1
        while (t < tend) { v4u T[8]; K_READ(T); K_MATH(T, act, sstep); }
        lds_wait_ge<1>(lds, L_QRDY + 4, 1u);
        const float Mh = xg_max(mx);
        const float M = __builtin_bit_cast(float, __builtin_amdgcn_ds_bpermute(((lane >> 3) & 3) * 4, __builtin_bit_cast(int, Mh)));
        const float mc = -M * SC2;
        float l = 0.f;
        CBAR();
#pragma unroll
        for (int j = 0; j < 4; ++j) {
          const f32x4 s = *LDSP(f32x4, sw + 16 * lane + 1024 * j);
          const float p0 = ex2(fmaf(s[0], SC2, mc)), p1 = ex2(fmaf(s[1], SC2, mc)), p2 = ex2(fmaf(s[2], SC2, mc)), p3 = ex2(fmaf(s[3], SC2, mc));
          l += (p0 + p1) + (p2 + p3);
          *LDSP(v2u, L_P + ph * 8192 + w * 2048 + 8 * lane + 512 * j) = v2u{cvtpk(p0, p1), cvtpk(p2, p3)};
        }
        l += dpp<XOR1>(l); l += dpp<XOR2>(l); l += dpp<HMIR>(l); l = x32_sum(l);
        const float la = x16_sum(l + dpp<ROR8>(l));
        if ((lane & 39) == 0) *LDSP(f32x2, L_ML + ph * 128 + w * 32 + lane) = f32x2{M, ph == 1 ? l : la};
        lds_inc(lds, L_MLC, lane);
        mx = -3.0e38f; sa = sa0;
#pragma unroll
        for (int m = 0; m < 4; ++m) q[m] = *LDSP(v4u, L_QIMG + 256 + (n & 3) * 256 + g * 16 + 64 * m);
      }
    }
    {
      const unsigned Z = 0x0C0C0C0Cu;
      const unsigned el0 = jj == 0 ? 0x0C0C0100u : jj == 1 ? 0x01000C0Cu : Z, el1 = jj == 2 ? 0x0C0C0100u : jj == 3 ? 0x01000C0Cu : Z;
      const unsigned eh0 = jj == 0 ? 0x0C0C0302u : jj == 1 ? 0x03020C0Cu : Z, eh1 = jj == 2 ? 0x0C0C0302u : jj == 3 ? 0x03020C0Cu : Z;
      const int vb = g * 1024 + n * 16;
#pragma unroll 1
      for (int ph = 1; ph >= 0; --ph) {
        if (ph == 0) {
          lds_wait_ge<1>(lds, L_MLC, 8u);
          f32x2 ml[4];
#pragma unroll
          for (int u = 0; u < 4; ++u) ml[u] = *LDSP(f32x2, L_ML + u * 32);
          const float M = fmaxf(fmaxf(ml[0][0], ml[1][0]), fmaxf(ml[2][0], ml[3][0]));
          float L = 0.f;
#pragma unroll
          for (int u = 0; u < 4; ++u) L = fmaf(ex2((ml[u][0] - M) * SC2), ml[u][1], L);
          *LDSP(float, L_WGT + 64 + 4 * w) = ex2(((*LDSP(f32x2, L_ML + w * 32))[0] - M) * SC2) / L;
        }
        const int tend = ph == 1 ? 192 : NSL, pstep = ph == 1 ? 256 : 64;
        int pa = L_P + ph * 8192 + w * 2048 + (ph == 1 ? (n & 3) * 64 : 0) + g * 16;
        f32x4 a0 = {0, 0, 0, 0}, a1 = {0, 0, 0, 0};
#pragma unroll 1
        while (t < tend) {
          const v4u pp = *LDSP(v4u, pa); pa += pstep;
          v2u bf[8];
#pragma unroll
          for (int i = 0; i < 8; ++i) { const unsigned pk = pp[i >> 1]; bf[i] = v2u{__builtin_amdgcn_perm(pk, pk, (i & 1) ? eh0 : el0), __builtin_amdgcn_perm(pk, pk, (i & 1) ? eh1 : el1)}; }
#pragma unroll
          for (int i = 0; i < 8; ++i) asm volatile("" : "+v"(bf[i]));
          v4u T[8];
          HALF_WAIT(C::HALFV ? 1 : 2, 1);
#pragma unroll
          for (int i = 0; i < 4; ++i) T[i] = *LDSP(v4u, slot + vb + i * 256);
          if (C::HALFV) {
#pragma unroll
            for (int i = 0; i < 4; ++i) { a0 = mfma16(v2u{T[i][0], T[i][1]}, bf[i], a0); a1 = mfma16(v2u{T[i][2], T[i][3]}, bf[i], a1); }
            asm volatile("" : "+v"(a0), "+v"(a1));
            HALF_WAIT(2, C::TIGHT ? 0 : 1);
          }
#pragma unroll
          for (int i = 4; i < 8; ++i) T[i] = *LDSP(v4u, slot + vb + 4096 + (i & 3) * 256);
          SLOT_DONE();
#pragma unroll
          for (int i = C::HALFV ? 4 : 0; i < 8; ++i) { a0 = mfma16(v2u{T[i][0], T[i][1]}, bf[i], a0); a1 = mfma16(v2u{T[i][2], T[i][3]}, bf[i], a1); }
        }
        if ((n & 3) == 0) *LDSP(v4u, L_HREC + 4096 + w * 256 + jj * 64 + g * 16) = v4u{cvtpk(a0[0], a1[0]), cvtpk(a0[1], a1[1]), cvtpk(a0[2], a1[2]), cvtpk(a0[3], a1[3])};
        if (ph == 1) {
          const int ro = sw + (n & 3) * 512 + jj * 128 + g * 32;
          *LDSP(f32x4, ro) = f32x4{a0[0], a1[0], a0[1], a1[1]};
          *LDSP(f32x4, ro + 16) = f32x4{a0[2], a1[2], a0[3], a1[3]};
        }
        lds_inc(lds, ph == 1 ? L_BCNT : L_RCNT + 4, lane);
      }
    }
    if (w == 3) { STAMP(3); STAMP_OUT(3, 4); }
  } else if (wave >= 6) {
    if (C::DBG & 8) return;
    const int w = wave - 6;
#pragma unroll 1
    for (int mat = 0; mat < 2; ++mat) {
      if (mat) wait_slots<16>(lds, 32u);
      const __amdgpu_buffer_rsrc_t rw = mk((mat ? wqb : wqa) + (size_t)(16 * jw + w) * 4096 + xcd * 512, 13 * 8192);
      v4u d[4];
#pragma unroll
      for (int i = 0; i < 4; ++i) d[i] = ld128(rw, lane * 16, ((i & 1) * 8 + (i >> 1) * 4) * 8192);
#pragma unroll
      for (int i = 0; i < 4; ++i) asm volatile("" :: "v"(d[i]));
    }
    const __amdgpu_buffer_rsrc_t ry = mk(ws + W_YA, W_END - W_YA);
    const int yo = ((b * 32 + h) * 512 + 128 * w + 8 * n + 2 * g) * 8;
    wait_slots<16>(lds, 72u);
#pragma unroll 1
    for (int pass = 0; pass < 2; ++pass) {
      const __amdgpu_buffer_rsrc_t rw = mk((pass == 0 ? wob : woa) + (size_t)xcd * 512 * 512, 512 * 1024);
      {
        v4u d[4];
#pragma unroll
        for (int i = 0; i < 4; ++i) d[i] = ld128(rw, (2 * b + (i >> 1) + 16 * (i & 1)) * 4096 + lane * 16, sp * 131072 + w * 1024);
#pragma unroll
        for (int i = 0; i < 4; ++i) asm volatile("" :: "v"(d[i]));
        lds_wait_ge<16>(lds, L_FULL, min(lds_ld(lds, L_FULL) + 24u, 2u * NSL));
      }
      const int wv = (pass == 0 ? w * 131072 + sp * 256 : sp * 131072 + w * 256) + g * 32768 + n * 16;
      v4u F[4][8];
#pragma unroll
      for (int s = 0; s < 4; ++s) {
#pragma unroll
        for (int jp = 0; jp < 4; ++jp) {
          const v4u w0 = ld128(rw, wv, (8 * jp + s) * 1024), w1 = ld128(rw, wv, (8 * jp + 4 + s) * 1024);
#pragma unroll
          for (int d = 0; d < 4; ++d) {
            unsigned lo = __builtin_amdgcn_perm(w1[d], w0[d], 0x05040100u), hi = __builtin_amdgcn_perm(w1[d], w0[d], 0x07060302u);
            asm volatile("" : "+v"(lo), "+v"(hi));
            F[s][2 * d][jp] = lo; F[s][2 * d + 1][jp] = hi;
          }
          __builtin_amdgcn_sched_barrier(0);
        }
#pragma unroll
        for (int c = 0; c < 8; ++c) asm volatile("" : "+v"(F[s][c]));
      }
      const int hr = pass == 0 ? w : 4;
      if (C::TIGHT) lds_wait_ge<4>(lds, L_RCNT + 4 * pass, 2u);
      lds_wait_ge<C::TIGHT ? 0 : 1>(lds, L_RCNT + 4 * pass, 4u);
      v4u of[4];
#pragma unroll
      for (int s = 0; s < 4; ++s) of[s] = *LDSP(v4u, L_HREC + hr * 1024 + (n & 3) * 256 + s * 64 + g * 16);
      const unsigned tag = *LDSP(unsigned, L_TAG);
      const f32x4 wt = *LDSP(f32x4, L_WGT + hr * 16);
      f32x4 da = {0, 0, 0, 0}, dc = {0, 0, 0, 0};
      __builtin_amdgcn_sched_barrier(0);
#pragma unroll
      for (int cp = 0; cp < 4; ++cp) {
        f32x4 d0 = {0, 0, 0, 0}, d1 = {0, 0, 0, 0};
#pragma unroll
        for (int s = 0; s < 4; ++s) { d0 = mfma32(of[s], F[s][2 * cp], d0); d1 = mfma32(of[s], F[s][2 * cp + 1], d1); }
#pragma unroll
        for (int r = 0; r < 4; ++r) { da[r] = (cp == 0 || g == cp) ? d0[r] : da[r]; dc[r] = (cp == 0 || g == cp) ? d1[r] : dc[r]; }
      }
#pragma unroll
      for (int i = 0; i < 18; ++i) __builtin_amdgcn_sched_group_barrier(0x8, 1, 0);
#pragma unroll
      for (int i = 0; i < 14; ++i) { __builtin_amdgcn_sched_group_barrier(0x8, 1, 0); __builtin_amdgcn_sched_group_barrier(0x2, 1, 0); }
      const float ya = fmaf(wt[0], da[0], wt[1] * da[1]) + fmaf(wt[2], da[2], wt[3] * da[3]), yc = fmaf(wt[0], dc[0], wt[1] * dc[1]) + fmaf(wt[2], dc[2], wt[3] * dc[3]);
      st_sc1(v4u{__float_as_uint(ya), tag, __float_as_uint(yc), tag}, ry, pass == 0 ? OOB : yo, 0);
      if (pass == 0) *LDSP(f32x2, L_YS + w * 512 + 8 * lane) = f32x2{ya, yc};
      lds_inc(lds, L_YPUB + 4 * pass, lane);
      if (w == 0) STAMP(4 + pass);
    }
    if (w == 0) STAMP_OUT(4, 6);
  } else {
    if (C::DBG & 8) return;
    const unsigned tag = __builtin_amdgcn_readfirstlane(__hip_atomic_load((GAS unsigned*)(ws + W_EPOCH), __ATOMIC_RELAXED, __HIP_MEMORY_SCOPE_AGENT)) + 1u;
    lds_st(lds, L_TAG, tag);
    const __amdgpu_buffer_rsrc_t rs = mk(ws, W_END);
#define AN_PUT(k_, at_) do { if (((C::ANCH >> (k_)) & 1) && sp == 0) { wait_slots<16>(lds, at_); const unsigned im = lds_ld(lds, L_IMPL); \
      st_sc1(v4u{im, tag, im, tag}, rs, lane == 0 ? W_AN + (k_) * 1024 + (b * 8 + xcd) * 16 : OOB, 0); } } while (0)
#define AN_GET(k_, at_) do { if ((C::ANCH >> (k_)) & 1) { wait_slots<16>(lds, at_); const unsigned im = lds_ld(lds, L_IMPL); \
      const v4u a = ld128(rs, W_AN + (k_) * 1024 + lane * 16, 0); \
      const bool okv = a[1] == tag && a[3] == tag; const int cntv = __builtin_popcountll(__builtin_amdgcn_ballot_w64(okv)); \
      float dv = okv ? (float)(int)(a[0] - im) : 0.f; \
      dv += dpp<XOR1>(dv); dv += dpp<XOR2>(dv); dv += dpp<HMIR>(dv); dv += dpp<ROR8>(dv); dv = x32_sum(x16_sum(dv)); \
      if (cntv >= 32) lds_st(lds, L_BASE, im + (int)(dv / (float)cntv) + C::MARGIN); } } while (0)
#pragma unroll 1
    for (int it = 0; it < 2; ++it) {
      if (it == 1) { AN_PUT(0, 30u); AN_GET(0, 46u); }
      if (C::Q1 ? (jw >> 4) == it : true) {
        const int cb = C::Q1 ? xcd * 32 + 2 * (jw & 15) : xcd * 32 + jw;
        v4u xf[16];
        const __amdgpu_buffer_rsrc_t rx = mk(it ? xb : xa, 8 * 512 * 2);
        if (it) wait_slots<16>(lds, 52u);
#pragma unroll
        for (int s = 0; s < 16; ++s) xf[s] = ld128(rx, (C::Q1 ? n & 7 : n) * 1024 + 16 * g + 64 * s, 0);
        if (!it) wait_slots<1>(lds, C::T_SLICE);
        const v4i rw = mksrd((it ? wqb : wqa) + cb * 16, 512 * 8192 - cb * 32);
        f32x4 d0 = {0, 0, 0, 0}, d1 = {0, 0, 0, 0};
        if (C::Q1) {
          const unsigned vo = (lane >> 2) * 8192 + (lane & 3) * 16;
#pragma unroll
          for (int hf = 0; hf < 2; ++hf) {
            if (it || hf == 0) {
#pragma unroll 1
              for (int i = 0; i < (it ? 16 : 32); ++i) {
                asm volatile("s_mov_b32 m0, %0\n\ts_nop 0\n\tbuffer_load_dwordx4 %1, %2, %3 offen nt lds" :: "s"((it ? L_P : L_S) + i * 1024), "v"(vo), "s"(rw), "s"((hf * 16 + i) * 16 * 8192) : "memory");
                if (it) __builtin_amdgcn_s_sleep(6);
              }
              asm volatile("s_waitcnt vmcnt(0)" ::: "memory");
            }
            const int ta = ((it || hf) ? L_P : L_S) + (8 * g + (n >> 2)) * 64 + (n & 3) * 8;
            v2u t[8][4];
#pragma unroll
            for (int s = 0; s < 8; ++s)
#pragma unroll
              for (int u = 0; u < 4; ++u) t[s][u] = __builtin_bit_cast(v2u, __builtin_amdgcn_ds_read_tr16_b64_v4i16(LDSP(s16x4, ta + s * 2048 + (u & 1) * 256 + (u >> 1) * 32)));
            __builtin_amdgcn_sched_barrier(0);
#pragma unroll
            for (int s = 0; s < 8; ++s) {
              d0 = mfma32(v4u{t[s][0][0], t[s][0][1], t[s][1][0], t[s][1][1]}, xf[8 * hf + s], d0);
              d1 = mfma32(v4u{t[s][2][0], t[s][2][1], t[s][3][0], t[s][3][1]}, xf[8 * hf + s], d1);
            }
            asm volatile("" : "+v"(d0), "+v"(d1) :: "memory");
          }
          const f32x4 dd = n < 8 ? d0 : d1;
          st_sc1(v4u{cvtpk(dd[0], dd[1]), tag, cvtpk(dd[2], dd[3]), tag}, rs, W_QG + (it * 256 + cb) * 512 + n * 64 + g * 16, 0);
        } else {
          const unsigned vo = (lane >> 1) * 8192 + (lane & 1) * 16;
#pragma unroll 1
          for (int i = 0; i < 16; ++i) {
            asm volatile("s_mov_b32 m0, %0\n\ts_nop 0\n\tbuffer_load_dwordx4 %1, %2, %3 offen nt lds" :: "s"(L_P + i * 1024), "v"(vo), "s"(rw), "s"(i * 32 * 8192) : "memory");
            if (it) __builtin_amdgcn_s_sleep(12);
          }
          asm volatile("s_waitcnt vmcnt(0)" ::: "memory");
          const int ta = L_P + (8 * g + (n >> 2)) * 32 + (n & 3) * 8;
          v2u t[16][2];
#pragma unroll
          for (int s = 0; s < 16; ++s)
#pragma unroll
            for (int u = 0; u < 2; ++u) t[s][u] = __builtin_bit_cast(v2u, __builtin_amdgcn_ds_read_tr16_b64_v4i16(LDSP(s16x4, ta + s * 1024 + u * 128)));
          __builtin_amdgcn_sched_barrier(0);
#pragma unroll
          for (int s = 0; s < 16; s += 2) {
            d0 = mfma32(v4u{t[s][0][0], t[s][0][1], t[s][1][0], t[s][1][1]}, xf[s], d0);
            d1 = mfma32(v4u{t[s + 1][0][0], t[s + 1][0][1], t[s + 1][1][0], t[s + 1][1][1]}, xf[s + 1], d1);
          }
          d0 += d1;
          st_sc1(v4u{cvtpk(d0[0], d0[1]), tag, cvtpk(d0[2], d0[3]), tag}, rs, n < 8 ? W_QG + (it * 256 + cb) * 512 + n * 64 + g * 16 : OOB, 0);
        }
        if (it == 0) STAMP(7);
      }
      wait_slots<4>(lds, it ? 104u : C::T_GATHER);
      const int go = it == 0 ? (lane < 32 ? W_QG + (h * 8 + (lane >> 2)) * 512 + b * 64 + (lane & 3) * 16 : OOB) : W_QG + (256 + xcd * 32 + (lane >> 2)) * 512 + b * 64 + (lane & 3) * 16;
      const u64 need = it == 0 ? 0xFFFFFFFFull : ~0ull;
#pragma unroll 1
      for (unsigned spn = 0; spn < SPIN_CAP; ++spn) {
        const v4u v = ld_sc1(rs, go, 0), u = ld_sc1(rs, it == 1 ? go : OOB, 16 * 512);
        if (it == 1 || lane < 32) *LDSP(v2u, L_QIMG + (it == 1 ? 256 : 0) + 8 * lane) = v2u{v[0], v[2]};
        if (it == 1) *LDSP(v2u, L_QIMG + 768 + 8 * lane) = v2u{u[0], u[2]};
        if ((__builtin_amdgcn_ballot_w64(v[1] != tag || v[3] != tag || (it == 1 && (u[1] != tag || u[3] != tag))) & need) == 0) break;
        __builtin_amdgcn_s_sleep(24);
      }
      CBAR();
      lds_st(lds, L_QRDY + 4 * it, 1u);
      if (it == 0) STAMP(8);
    }
    AN_PUT(1, 112u); AN_GET(1, 128u); AN_PUT(2, 176u); AN_GET(2, 190u);
    lds_wait_ge<16>(lds, L_BCNT, 4u);
    {
      f32x2 ml[4]; f32x4 ac[4][2];
#pragma unroll
      for (int u = 0; u < 4; ++u) { ml[u] = *LDSP(f32x2, L_ML + 128 + u * 32 + g * 8); ac[u][0] = *LDSP(f32x4, L_S + u * 4096 + 32 * lane); ac[u][1] = *LDSP(f32x4, L_S + u * 4096 + 32 * lane + 16); }
      const float M = fmaxf(fmaxf(ml[0][0], ml[1][0]), fmaxf(ml[2][0], ml[3][0]));
      float L = 0.f; f32x4 o0 = {0, 0, 0, 0}, o1 = {0, 0, 0, 0};
#pragma unroll
      for (int u = 0; u < 4; ++u) { const float f = ex2((ml[u][0] - M) * SC2); L = fmaf(f, ml[u][1], L); o0 += f * ac[u][0]; o1 += f * ac[u][1]; }
      const float inv = 1.f / L; o0 *= inv; o1 *= inv;
      st_sc1(v4u{cvtpk(o0[0], o0[1]), cvtpk(o0[2], o0[3]), cvtpk(o1[0], o1[1]), cvtpk(o1[2], o1[3])}, rs, W_PO + wg * 1024 + 16 * lane, 0);
      asm volatile("s_waitcnt vmcnt(0)" ::: "memory");
      st_sc1(v4u{__float_as_uint(M), tag, __float_as_uint(L), tag}, rs, n == 0 ? W_ML + wg * 128 + g * 16 : OOB, 0);
    }
    wait_slots<16>(lds, C::T_ML);
    v4u mlv = {0, 0, 0, 0};
#pragma unroll 1
    for (unsigned spn = 0; spn < SPIN_CAP; ++spn) {
      mlv = ld_sc1(rs, lane < 16 ? W_ML + (xcd + 8 * (4 * b + (lane & 3))) * 128 + (lane >> 2) * 16 : OOB, 0);
      if ((__builtin_amdgcn_ballot_w64(mlv[1] != tag || mlv[3] != tag) & 0xFFFFu) == 0u) break;
      __builtin_amdgcn_s_sleep(64);
    }
    __builtin_amdgcn_fence(__ATOMIC_ACQUIRE, "wavefront");
    {
      v4u p[4];
#pragma unroll
      for (int u = 0; u < 4; ++u) p[u] = ld_sc1(rs, W_PO + (xcd + 32 * b) * 1024 + 16 * lane, u * 8192);
      const float m = __uint_as_float(mlv[0]);
      float M = fmaxf(m, dpp<XOR1>(m)); M = fmaxf(M, dpp<XOR2>(M));
      const float e = ex2((m - M) * SC2) * __uint_as_float(mlv[2]);
      float L = e + dpp<XOR1>(e); L += dpp<XOR2>(L);
      if (lane < 16) *LDSP(float, L_WGT + 4 * lane) = e / L;
#pragma unroll
      for (int u = 0; u < 4; ++u) *LDSP(v4u, L_HREC + g * 1024 + u * 256 + n * 16) = p[u];
      CBAR(); lds_st(lds, L_RCNT, 4u);
    }
    lds_wait_ge<16>(lds, L_YPUB, 4u);
    {
      const f32x2 yb = (*LDSP(f32x2, L_YS + 8 * lane) + *LDSP(f32x2, L_YS + 512 + 8 * lane)) + (*LDSP(f32x2, L_YS + 1024 + 8 * lane) + *LDSP(f32x2, L_YS + 1536 + 8 * lane));
      AN_PUT(3, 244u); AN_GET(3, 258u);
      wait_slots<8>(lds, C::T_YB);
      st_sc1(v4u{__float_as_uint(yb[0]), tag, __float_as_uint(yb[1]), tag}, rs, W_YB + wg * 1024 + (8 * n + 2 * g) * 8, 0);
    }
#pragma unroll 1
    for (int pass = 0; pass < 2; ++pass) {
      if (pass == 0) wait_slots<8>(lds, NSL - 1); else lds_wait_ge<1>(lds, L_YPUB + 4, 4u);
      const int yo = pass == 0 ? W_YB + ((lane >> 3) + 8 * (4 * b + (h >> 3))) * 1024 + (h & 7) * 128 + (lane & 7) * 16
                               : W_YA + (b * 32 * 512 + (lane >> 3) * 512 + 16 * h + 2 * (lane & 7)) * 8;
      v4u y[4]; bool ok[4];
#pragma unroll
      for (int i = 0; i < 4; ++i) { y[i] = v4u{0, 0, 0, 0}; ok[i] = pass == 0 && i > 0; }
#pragma unroll 1
      for (unsigned spn = 0; spn < SPIN_CAP; ++spn) {
        v4u z[4];
#pragma unroll
        for (int i = 0; i < 4; ++i) z[i] = ld_sc1(rs, ok[i] ? OOB : yo, i * 8 * 512 * 8);
        bool all = true;
#pragma unroll
        for (int i = 0; i < 4; ++i) { if (!ok[i]) { y[i] = z[i]; ok[i] = z[i][1] == tag && z[i][3] == tag; } all &= ok[i]; }
        if (__builtin_amdgcn_ballot_w64(!all) == 0) break;
        __builtin_amdgcn_s_sleep(2);
      }
      float s0 = (__uint_as_float(y[0][0]) + __uint_as_float(y[1][0])) + (__uint_as_float(y[2][0]) + __uint_as_float(y[3][0]));
      float s1 = (__uint_as_float(y[0][2]) + __uint_as_float(y[1][2])) + (__uint_as_float(y[2][2]) + __uint_as_float(y[3][2]));
      s0 += dpp<ROR8>(s0); s1 += dpp<ROR8>(s1);
      s0 = x32_sum(x16_sum(s0)); s1 = x32_sum(x16_sum(s1));
      if (lane < 8) *(GAS unsigned*)(out + b * 1024 + (pass == 0 ? 512 : 0) + 16 * h + 2 * lane) = cvtpk(s0, s1);
      STAMP(10 + pass);
    }
    if (wg == 0 && lane == 0) __hip_atomic_store((GAS unsigned*)(ws + W_EPOCH), tag, __ATOMIC_RELAXED, __HIP_MEMORY_SCOPE_AGENT);
    { const unsigned tm = lds_ld(lds, 7168); if (true) while ((unsigned)__builtin_amdgcn_s_memrealtime() - (unsigned)tstart < 15000 + 10 * tm) __builtin_amdgcn_s_sleep(1); }
    STAMP_OUT(7, 9); STAMP_OUT(10, 12);
  }
}

template <class C>
static void launch_cfg(void* const* d_in, void* d_out, void* d_ws, hipStream_t stream) {
  (void)hipFuncSetAttribute((const void*)fused_decode<C>, hipFuncAttributeMaxDynamicSharedMemorySize, LDS_BYTES);
  hipLaunchKernelGGL(fused_decode<C>, dim3(256), dim3(NTHR), LDS_BYTES, stream,
                     (const u16*)d_in[0], (const u16*)d_in[1], (const u16*)d_in[2], (const u16*)d_in[3], (const u16*)d_in[4],
                     (const u16*)d_in[5], (const u16*)d_in[6], (const u16*)d_in[7], (const u16*)d_in[8], (const u16*)d_in[9],
                     (u16*)d_out, (unsigned char*)d_ws);
}
extern "C" void kernel_launch(void* const* d_in, const int* in_sizes, int n_in, void* d_out, int out_size, void* d_ws, size_t ws_size, hipStream_t stream) {
  (void)in_sizes; (void)n_in; (void)out_size; (void)ws_size;
  launch_cfg<CfgDefault>(d_in, d_out, d_ws, stream);
}
```

```cpp
#include <hip/hip_runtime.h>
#include <hip/hip_bf16.h>
#include <stdint.h>

typedef unsigned v4u __attribute__((ext_vector_type(4)));
typedef unsigned v2u __attribute__((ext_vector_type(2)));
typedef int v4i __attribute__((ext_vector_type(4)));
typedef float f32x4 __attribute__((ext_vector_type(4)));
typedef float f32x2 __attribute__((ext_vector_type(2)));
typedef short s16x4 __attribute__((ext_vector_type(4)));
typedef __bf16 bf16x8 __attribute__((ext_vector_type(8)));
typedef __bf16 bf16x2 __attribute__((ext_vector_type(2)));
typedef unsigned short u16;
typedef unsigned long long u64;
#define GAS __attribute__((address_space(1)))
#define LAS __attribute__((address_space(3)))

struct C {
  static constexpr int NFL = 20;
  static constexpr int MUL = 11;
  static constexpr int PACE = 119, PACE2 = 112;
  static constexpr int PACE0 = 20, MARGIN = 20;
  static constexpr int DN = 40, U1 = 60, U2 = 110, U3 = 160, U4 = 220;
  static constexpr int T2 = 264, GAIN2 = 3;
  static constexpr int CODE_LINES = 144;
  static constexpr int T_SLICE = 3, T_GATHER = 17, T_ML = 216, T_YB = 314;
};

constexpr int SK = 4096, DH = 128, NH = 32, HKV = 8;
constexpr float SC2 = 0.088388347648318440f * 1.4426950408889634f;
constexpr int NWAVE = 10, NTHR = NWAVE * 64;
constexpr int NSL = 320;
constexpr int PARK = 4;
constexpr int RING = 15, SLOT = 8192;
constexpr int L_FULL = 0  , L_QRDY = 4  , L_BCNT = 12, L_DONE = 16  , L_RCNT = 32  , L_YPUB = 40  ,
              L_TAG = 48, L_MLC = 52, L_IMPL = 56, L_BASE = 60;
constexpr int L_QIMG = 64;
constexpr int L_WGT = L_QIMG + 1280;
constexpr int L_ML = L_WGT + 128;
constexpr int L_HREC = L_ML + 256;
constexpr int L_JUNK = 7936;
constexpr int L_S = 8192;
constexpr int L_P = L_S + 16384;
constexpr int L_YS = L_P + 8192;
constexpr int L_RING = L_P + 16384;
constexpr int LDS_BYTES = 160 * 1024;
static_assert(L_HREC % 16 == 0 && L_HREC + 5 * 1024 <= 7168 && L_RING + RING * SLOT <= LDS_BYTES, "lds map");
constexpr int W_EPOCH = 0;
constexpr int W_ML = 4096;
constexpr int W_AN = 49152;
constexpr int W_QG = 65536;
constexpr int W_PO = W_QG + 2 * 256 * 512;
constexpr int W_YB = W_PO + 256 * 1024;
constexpr int W_YA = W_YB + 256 * 1024;
constexpr int W_END = W_YA + 8 * 32 * 512 * 8;
constexpr unsigned SPIN_CAP = 1u << 20;
constexpr int OOB = 0x7FFFFF00;

__device__ __forceinline__ unsigned cvtpk(float lo, float hi) { return __builtin_bit_cast(unsigned, __builtin_convertvector(f32x2{lo, hi}, bf16x2)); }
__device__ __forceinline__ __amdgpu_buffer_rsrc_t mk(const void* p, unsigned bytes) { return __builtin_amdgcn_make_buffer_rsrc((void*)p, 0, bytes, 0x00020000); }
__device__ __forceinline__ v4i mksrd(const void* p, unsigned bytes) { return v4i{(int)(uintptr_t)p, (int)((uintptr_t)p >> 32), (int)bytes, 0x00020000}; }
__device__ __forceinline__ f32x4 mfma32(v4u a, v4u b, f32x4 c) { return __builtin_amdgcn_mfma_f32_16x16x32_bf16(__builtin_bit_cast(bf16x8, a), __builtin_bit_cast(bf16x8, b), c, 0, 0, 0); }
__device__ __forceinline__ f32x4 mfma16(v2u a, v2u b, f32x4 c) { return __builtin_amdgcn_mfma_f32_16x16x16bf16_1k(__builtin_bit_cast(s16x4, a), __builtin_bit_cast(s16x4, b), c, 0, 0, 0); }
__device__ __forceinline__ float ex2(float x) { return __builtin_amdgcn_exp2f(x); }
__device__ __forceinline__ v4u ld128(__amdgpu_buffer_rsrc_t r, int vo, int so) { return __builtin_amdgcn_raw_buffer_load_b128(r, vo, so, 0); }
__device__ __forceinline__ v4u ld_sc1(__amdgpu_buffer_rsrc_t r, int vo, int so) { return __builtin_amdgcn_raw_buffer_load_b128(r, vo, so, 16); }
__device__ __forceinline__ void st_sc1(v4u v, __amdgpu_buffer_rsrc_t r, int vo, int so) { __builtin_amdgcn_raw_buffer_store_b128(v, r, vo, so, 16); }
template <int CTRL> __device__ __forceinline__ float dpp(float x) { return __builtin_bit_cast(float, __builtin_amdgcn_mov_dpp(__builtin_bit_cast(int, x), CTRL, 0xf, 0xf, true)); }
constexpr int XOR1 = 0xB1, XOR2 = 0x4E, HMIR = 0x141, ROR8 = 0x128;
__device__ __forceinline__ float x16_sum(float x) { auto s = __builtin_amdgcn_permlane16_swap(__float_as_uint(x), __float_as_uint(x), false, false); return __uint_as_float(s[0]) + __uint_as_float(s[1]); }
__device__ __forceinline__ float x32_sum(float x) { auto s = __builtin_amdgcn_permlane32_swap(__float_as_uint(x), __float_as_uint(x), false, false); return __uint_as_float(s[0]) + __uint_as_float(s[1]); }
__device__ __forceinline__ float xg_max(float x) {
  auto s = __builtin_amdgcn_permlane16_swap(__float_as_uint(x), __float_as_uint(x), false, false);
  x = fmaxf(__uint_as_float(s[0]), __uint_as_float(s[1]));
  auto t = __builtin_amdgcn_permlane32_swap(__float_as_uint(x), __float_as_uint(x), false, false);
  return fmaxf(__uint_as_float(t[0]), __uint_as_float(t[1]));
}
#define CBAR() asm volatile("" ::: "memory")
#define LDSP(T, off) ((LAS T*)(lds + (off)))
__device__ __forceinline__ unsigned lds_ld(LAS unsigned char* lds, int off) { return __builtin_amdgcn_readfirstlane(*(volatile LAS unsigned*)(lds + off)); }
__device__ __forceinline__ void lds_st(LAS unsigned char* lds, int off, unsigned v) { *(volatile LAS unsigned*)(lds + off) = v; }
template <int SLP> __device__ __forceinline__ unsigned lds_wait_ge(LAS unsigned char* lds, int off, unsigned want) {
  unsigned v = lds_ld(lds, off);
#pragma unroll 1
  for (unsigned sp = 0; v < want && sp < SPIN_CAP * 32 / (SLP + 1); ++sp) { if (SLP) __builtin_amdgcn_s_sleep(SLP); v = lds_ld(lds, off); }
  CBAR();
  return v;
}
template <int SLP> __device__ __forceinline__ void wait_slots(LAS unsigned char* lds, unsigned slots) { lds_wait_ge<SLP>(lds, L_FULL, 2 * slots); }
__device__ __forceinline__ void lds_inc(LAS unsigned char* lds, int off, int lane) {
  CBAR();
  if (lane == 0) __hip_atomic_fetch_add(LDSP(unsigned, off), 1u, __ATOMIC_RELAXED, __HIP_MEMORY_SCOPE_WORKGROUP);
  CBAR();
}

template <int N> __device__ __forceinline__ void dma4(unsigned m0v, unsigned v0, unsigned v1, unsigned v2, unsigned v3, v4i srd, unsigned so) {
  asm volatile("s_mov_b32 m0, %0\n\t"
               "s_waitcnt vmcnt(%7)\n\tbuffer_load_dwordx4 %1, %5, %6 offen nt lds\n\t"
               "s_waitcnt vmcnt(%7)\n\tbuffer_load_dwordx4 %2, %5, %6 offen offset:1024 nt lds\n\t"
               "s_waitcnt vmcnt(%7)\n\tbuffer_load_dwordx4 %3, %5, %6 offen offset:2048 nt lds\n\t"
               "s_waitcnt vmcnt(%7)\n\tbuffer_load_dwordx4 %4, %5, %6 offen offset:3072 nt lds"
               :: "s"(m0v), "v"(v0), "v"(v1), "v"(v2), "v"(v3), "s"(srd), "s"(so), "i"(N - 1) : "memory");
}

__global__ void __launch_bounds__(NTHR, 3) fused_decode(const u16* xa, const u16* ak, const u16* av, const u16* wqa, const u16* woa,
                                                        const u16* xb, const u16* bk, const u16* bv, const u16* wqb, const u16* wob,
                                                        u16* out, unsigned char* ws) {
  constexpr int NFL = C::NFL;
  {
    u64 pc; unsigned tv;
    asm volatile("s_getpc_b64 %0\n\tv_min_u32 %1, %3, %2\n\ts_movk_i32 m0, %4\n\tv_lshlrev_b32 %1, 7, %1\n\tglobal_load_lds_dword %1, %0"
                 : "=&s"(pc), "=&v"(tv) : "v"(threadIdx.x), "i"(C::CODE_LINES - 1), "i"(L_JUNK));
    __builtin_amdgcn_sched_barrier(0);
  }
  extern __shared__ __attribute__((aligned(16))) unsigned char lds_raw[];
  LAS unsigned char* lds = (LAS unsigned char*)lds_raw;
  const int wg = blockIdx.x, lane = threadIdx.x & 63, wave = __builtin_amdgcn_readfirstlane(threadIdx.x >> 6);
  const int xcd = wg & 7, jw = wg >> 3, b = jw >> 2, sp = jw & 3, h = 4 * xcd + sp;
  const int g = lane >> 4, n = lane & 15;
  *(volatile LAS unsigned*)(lds + 4 * n) = 0u;
  asm volatile("s_waitcnt lgkmcnt(0)" :: "s"(wqb), "s"(wob), "s"(out), "s"(ws), "s"(xa) : "memory");
  __builtin_amdgcn_s_barrier();
  const unsigned k0 = g * 256 + ((n ^ g) * 16);
#define SLOT_OFF(r_) (((((r_) & 0x70u) << 1) | ((r_) & 15u)) << 12)
  {
#define E4(m_, so_) "s_mov_b32 m0, " m_ "\n\ts_mov_b32 %0, " so_ "\n\t" \
               "buffer_load_dwordx4 %2, %6, %0 offen nt lds\n\tbuffer_load_dwordx4 %3, %6, %0 offen offset:1024 nt lds\n\t" \
               "buffer_load_dwordx4 %4, %6, %0 offen offset:2048 nt lds\n\tbuffer_load_dwordx4 %5, %6, %0 offen offset:3072 nt lds\n\t"
    unsigned tmp;
    asm volatile("s_cmp_lg_u32 %1, 0\n\ts_cbranch_scc1 1f\n\t"
                 E4("%7", "0") E4("%7+4096", "65536") E4("%7+8192", "%8") E4("%7+12288", "%8+65536") E4("%7+16384", "%9") E4("%7+20480", "%9+65536")
                 "1:"
                 : "=&s"(tmp) : "s"(wave), "v"(k0), "v"(k0 ^ 64), "v"(k0 ^ 128), "v"(k0 ^ 192), "s"(mksrd(ak + (size_t)(b * NH + h) * SK * DH, SK * 256)),
                   "i"(L_RING), "i"(SLOT_OFF(C::MUL)), "i"(SLOT_OFF(2 * C::MUL)) : "memory", "scc");
  }

  if (wave == 0) {
    __builtin_amdgcn_s_setprio(3);
    const size_t ua = (size_t)(b * NH + h) * SK * DH, ub = (size_t)(b * HKV + xcd) * SK * DH;
    const unsigned vv = lane * 16;
    unsigned slot = L_RING + 3 * SLOT;
    int limit = RING;
    u64 now = __builtin_amdgcn_s_memrealtime();
    unsigned base = (unsigned)now + C::PACE0, due = -C::PACE;
    now = base + due;
    int t = 3;
#pragma unroll 1
    for (int ph = 0; ph < 4; ++ph) {
      const v4i rs = mksrd(ph == 0 ? ak + ua : ph == 1 ? bk + ub : ph == 2 ? bv + ub : av + ua, SK * 256);
      const int tend = ph == 0 ? 128 : ph == 1 ? 160 : ph == 2 ? 192 : NSL;
      const unsigned v0 = ph < 2 ? k0 : vv, v1 = ph < 2 ? k0 ^ 64 : vv, v2 = ph < 2 ? k0 ^ 128 : vv, v3 = ph < 2 ? k0 ^ 192 : vv;
      unsigned r = (ph == 0 ? 3 * C::MUL : ph == 3 ? 0 : sp * 32 * C::MUL) & 127u;
#pragma unroll 1
      while (t < tend) {
        if (t + 3 >= limit) {
#pragma unroll 1
          for (unsigned spn = 0; spn < SPIN_CAP * 16; ++spn) {
            const v4u d = *(volatile LAS v4u*)(lds + L_DONE);
            const unsigned nb = lds_ld(lds, L_BASE); base = nb ? nb : base;
            const int c = min(min(4 * (int)d[0], 4 * (int)d[1] + 1), min(4 * (int)d[2] + 2, 4 * (int)d[3] + 3));
            limit = __builtin_amdgcn_readfirstlane(c) + RING;
            if (t + 3 < limit) break;
            __builtin_amdgcn_s_sleep(1);
          }
        }
#define RUN4(N_) do { static_assert(((N_) + 3) / 4 >= 5 && ((N_) + 3) / 4 <= 7, "the published count never goes down"); \
        _Pragma("unroll 1") for (const int te = (t | 3) + 1; t < te; ++t) { \
          const unsigned so = SLOT_OFF(r); \
          dma4<N_>(slot, v0, v1, v2, v3, rs, so); \
          dma4<N_>(slot + 4096, v0, v1, v2, v3, rs, so + 65536u); \
          asm volatile("s_waitcnt lgkmcnt(0)");     \
          lds_st(lds, L_FULL, (unsigned)(2 * t + 2 - ((N_) + 3) / 4)); \
          slot += SLOT; if (slot >= L_RING + RING * SLOT) slot = L_RING; \
          r = (r + C::MUL) & 127u; } } while (0)
        asm volatile("" : "+s"(now));
        lds_st(lds, L_IMPL, (unsigned)now - due);
        const int lagr = (int)((unsigned)now - due - base), lag = t >= C::T2 ? lagr * C::GAIN2 : lagr;
        due += ph == 0 ? C::PACE : C::PACE2;
        asm volatile("s_memrealtime %0" : "=s"(now));
        if (lag < C::U1) { if (lag >= -C::DN) RUN4(NFL); else RUN4(NFL - 1); }
        else if (lagr < C::U3) { if (lag < C::U2) RUN4(NFL + 1); else RUN4(NFL + 2); }
        else if (lagr < C::U4) RUN4(NFL + 3); else RUN4(NFL + 5);
      }
    }
    asm volatile("s_waitcnt vmcnt(16)" ::: "memory"); lds_st(lds, L_FULL, 2 * NSL - 4);
    asm volatile("s_waitcnt vmcnt(12)" ::: "memory"); lds_st(lds, L_FULL, 2 * NSL - 3);
    asm volatile("s_waitcnt vmcnt(8)" ::: "memory");  lds_st(lds, L_FULL, 2 * NSL - 2);
    asm volatile("s_waitcnt vmcnt(4)" ::: "memory");  lds_st(lds, L_FULL, 2 * NSL - 1);
    asm volatile("s_waitcnt vmcnt(0)" ::: "memory");  lds_st(lds, L_FULL, 2 * NSL);
  } else if (wave >= 2 && wave < 6) {
    const int w = wave - 2, jj = n >> 2;
    int slot = L_RING + w * SLOT; unsigned full = 0, cnt = 0; int t = w;
#define HALF_WAIT(k_, slp_) do { if (full < (unsigned)(2 * t + (k_))) full = lds_wait_ge<slp_>(lds, L_FULL, (unsigned)(2 * t + (k_))); CBAR(); } while (0)
#define SLOT_DONE() do { CBAR(); ++cnt; lds_st(lds, L_DONE + 4 * w, cnt);     \
        CBAR(); slot += 4 * SLOT; if (slot >= L_RING + RING * SLOT) slot -= RING * SLOT; t += 4; } while (0)
#define K_READ(T_) do { HALF_WAIT(2, 1); _Pragma("unroll") for (int m_ = 0; m_ < 8; ++m_) T_[m_] = *LDSP(v4u, ((slot + kb) ^ (64 * (m_ & 3))) + (m_ >> 2) * 4096); SLOT_DONE(); } while (0)
    const int sw = L_S + w * 4096;
    {
      const int kb = n * 256 + ((g ^ n) * 16);
      v4u q[4];
      float mx = -3.0e38f;
      const int sa0 = sw + ((n & 3) * 8 + g * 2 + (jj & 1)) * 16;
      int sa = sa0;
#define K_MATH(T_, act_, step_) do { f32x4 s0 = {0.f, 0.f, 0.f, 0.f}, s1 = {0.f, 0.f, 0.f, 0.f}; \
        _Pragma("unroll") for (int m = 0; m < 4; ++m) { s0 = mfma32(T_[m], q[m], s0); s1 = mfma32(T_[4 + m], q[m], s1); }       \
        mx = fmaxf(fmaxf(fmaxf(fmaxf(s0[0], s0[1]), fmaxf(s0[2], s0[3])), fmaxf(fmaxf(s1[0], s1[1]), fmaxf(s1[2], s1[3]))), mx); \
        if (act_) *LDSP(f32x4, sa) = (jj & 1) ? s1 : s0; \
        sa += (step_); } while (0)
      {
        v4u P[PARK][8];
#pragma unroll
        for (int i = 0; i < PARK; ++i) K_READ(P[i]);
        lds_wait_ge<1>(lds, L_QRDY, 1u);
#pragma unroll
        for (int m = 0; m < 4; ++m) q[m] = *LDSP(v4u, L_QIMG + g * 16 + 64 * m);
#pragma unroll
        for (int i = 0; i < PARK; ++i) K_MATH(P[i], (n & 11) == 0, 128);
      }
#pragma unroll 1
      for (int ph = 0; ph < 2; ++ph) {
        const bool act = ph == 1 ? n < 8 : (n & 11) == 0;
        const int sstep = ph == 1 ? 512 : 128, tend = ph == 0 ? 128 : 160;
#pragma unroll 1
        while (t < tend) { v4u T[8]; K_READ(T); K_MATH(T, act, sstep); }
        lds_wait_ge<1>(lds, L_QRDY + 4, 1u);
        const float Mh = xg_max(mx);
        const float M = __builtin_bit_cast(float, __builtin_amdgcn_ds_bpermute(((lane >> 3) & 3) * 4, __builtin_bit_cast(int, Mh)));
        const float mc = -M * SC2;
        float l = 0.f;
        CBAR();
#pragma unroll
        for (int j = 0; j < 4; ++j) {
          const f32x4 s = *LDSP(f32x4, sw + 16 * lane + 1024 * j);
          const float p0 = ex2(fmaf(s[0], SC2, mc)), p1 = ex2(fmaf(s[1], SC2, mc)), p2 = ex2(fmaf(s[2], SC2, mc)), p3 = ex2(fmaf(s[3], SC2, mc));
          l += (p0 + p1) + (p2 + p3);
          *LDSP(v2u, L_P + ph * 8192 + w * 2048 + 8 * lane + 512 * j) = v2u{cvtpk(p0, p1), cvtpk(p2, p3)};
        }
        l += dpp<XOR1>(l); l += dpp<XOR2>(l); l += dpp<HMIR>(l); l = x32_sum(l);
        const float la = x16_sum(l + dpp<ROR8>(l));
        if ((lane & 39) == 0) *LDSP(f32x2, L_ML + ph * 128 + w * 32 + lane) = f32x2{M, ph == 1 ? l : la};
        lds_inc(lds, L_MLC, lane);
        mx = -3.0e38f; sa = sa0;
#pragma unroll
        for (int m = 0; m < 4; ++m) q[m] = *LDSP(v4u, L_QIMG + 256 + (n & 3) * 256 + g * 16 + 64 * m);
      }
    }
    {
      const unsigned Z = 0x0C0C0C0Cu;
      const unsigned el0 = jj == 0 ? 0x0C0C0100u : jj == 1 ? 0x01000C0Cu : Z, el1 = jj == 2 ? 0x0C0C0100u : jj == 3 ? 0x01000C0Cu : Z;
      const unsigned eh0 = jj == 0 ? 0x0C0C0302u : jj == 1 ? 0x03020C0Cu : Z, eh1 = jj == 2 ? 0x0C0C0302u : jj == 3 ? 0x03020C0Cu : Z;
      const int vb = g * 1024 + n * 16;
#pragma unroll 1
      for (int ph = 1; ph >= 0; --ph) {
        if (ph == 0) {
          lds_wait_ge<1>(lds, L_MLC, 8u);
          f32x2 ml[4];
#pragma unroll
          for (int u = 0; u < 4; ++u) ml[u] = *LDSP(f32x2, L_ML + u * 32);
          const float M = fmaxf(fmaxf(ml[0][0], ml[1][0]), fmaxf(ml[2][0], ml[3][0]));
          float L = 0.f;
#pragma unroll
          for (int u = 0; u < 4; ++u) L = fmaf(ex2((ml[u][0] - M) * SC2), ml[u][1], L);
          *LDSP(float, L_WGT + 64 + 4 * w) = ex2(((*LDSP(f32x2, L_ML + w * 32))[0] - M) * SC2) / L;
        }
        const int tend = ph == 1 ? 192 : NSL, pstep = ph == 1 ? 256 : 64;
        int pa = L_P + ph * 8192 + w * 2048 + (ph == 1 ? (n & 3) * 64 : 0) + g * 16;
        f32x4 a0 = {0, 0, 0, 0}, a1 = {0, 0, 0, 0};
#pragma unroll 1
        while (t < tend) {
          const v4u pp = *LDSP(v4u, pa); pa += pstep;
          v2u bf[8];
#pragma unroll
          for (int i = 0; i < 8; ++i) { const unsigned pk = pp[i >> 1]; bf[i] = v2u{__builtin_amdgcn_perm(pk, pk, (i & 1) ? eh0 : el0), __builtin_amdgcn_perm(pk, pk, (i & 1) ? eh1 : el1)}; }
#pragma unroll
          for (int i = 0; i < 8; ++i) asm volatile("" : "+v"(bf[i]));
          v4u T[8];
          HALF_WAIT(1, 1);
#pragma unroll
          for (int i = 0; i < 4; ++i) T[i] = *LDSP(v4u, slot + vb + i * 256);
#pragma unroll
          for (int i = 0; i < 4; ++i) { a0 = mfma16(v2u{T[i][0], T[i][1]}, bf[i], a0); a1 = mfma16(v2u{T[i][2], T[i][3]}, bf[i], a1); }
          asm volatile("" : "+v"(a0), "+v"(a1));
          HALF_WAIT(2, 0);
#pragma unroll
          for (int i = 4; i < 8; ++i) T[i] = *LDSP(v4u, slot + vb + 4096 + (i & 3) * 256);
          SLOT_DONE();
#pragma unroll
          for (int i = 4; i < 8; ++i) { a0 = mfma16(v2u{T[i][0], T[i][1]}, bf[i], a0); a1 = mfma16(v2u{T[i][2], T[i][3]}, bf[i], a1); }
        }
        if ((n & 3) == 0) *LDSP(v4u, L_HREC + 4096 + w * 256 + jj * 64 + g * 16) = v4u{cvtpk(a0[0], a1[0]), cvtpk(a0[1], a1[1]), cvtpk(a0[2], a1[2]), cvtpk(a0[3], a1[3])};
        if (ph == 1) {
          const int ro = sw + (n & 3) * 512 + jj * 128 + g * 32;
          *LDSP(f32x4, ro) = f32x4{a0[0], a1[0], a0[1], a1[1]};
          *LDSP(f32x4, ro + 16) = f32x4{a0[2], a1[2], a0[3], a1[3]};
        }
        lds_inc(lds, ph == 1 ? L_BCNT : L_RCNT + 4, lane);
      }
    }
  } else if (wave >= 6) {
    const int w = wave - 6;
#pragma unroll 1
    for (int mat = 0; mat < 2; ++mat) {
      if (mat) wait_slots<16>(lds, 32u);
      const __amdgpu_buffer_rsrc_t rw = mk((mat ? wqb : wqa) + (size_t)(16 * jw + w) * 4096 + xcd * 512, 13 * 8192);
      v4u d[4];
#pragma unroll
      for (int i = 0; i < 4; ++i) d[i] = ld128(rw, lane * 16, ((i & 1) * 8 + (i >> 1) * 4) * 8192);
#pragma unroll
      for (int i = 0; i < 4; ++i) asm volatile("" :: "v"(d[i]));
    }
    const __amdgpu_buffer_rsrc_t ry = mk(ws + W_YA, W_END - W_YA);
    const int yo = ((b * 32 + h) * 512 + 128 * w + 8 * n + 2 * g) * 8;
    wait_slots<16>(lds, 72u);
#pragma unroll 1
    for (int pass = 0; pass < 2; ++pass) {
      const __amdgpu_buffer_rsrc_t rw = mk((pass == 0 ? wob : woa) + (size_t)xcd * 512 * 512, 512 * 1024);
      {
        v4u d[4];
#pragma unroll
        for (int i = 0; i < 4; ++i) d[i] = ld128(rw, (2 * b + (i >> 1) + 16 * (i & 1)) * 4096 + lane * 16, sp * 131072 + w * 1024);
#pragma unroll
        for (int i = 0; i < 4; ++i) asm volatile("" :: "v"(d[i]));
        lds_wait_ge<16>(lds, L_FULL, min(lds_ld(lds, L_FULL) + 24u, 2u * NSL));
      }
      const int wv = (pass == 0 ? w * 131072 + sp * 256 : sp * 131072 + w * 256) + g * 32768 + n * 16;
      v4u F[4][8];
#pragma unroll
      for (int s = 0; s < 4; ++s) {
#pragma unroll
        for (int jp = 0; jp < 4; ++jp) {
          const v4u w0 = ld128(rw, wv, (8 * jp + s) * 1024), w1 = ld128(rw, wv, (8 * jp + 4 + s) * 1024);
#pragma unroll
          for (int d = 0; d < 4; ++d) {
            unsigned lo = __builtin_amdgcn_perm(w1[d], w0[d], 0x05040100u), hi = __builtin_amdgcn_perm(w1[d], w0[d], 0x07060302u);
            asm volatile("" : "+v"(lo), "+v"(hi));
            F[s][2 * d][jp] = lo; F[s][2 * d + 1][jp] = hi;
          }
          __builtin_amdgcn_sched_barrier(0);
        }
#pragma unroll
        for (int c = 0; c < 8; ++c) asm volatile("" : "+v"(F[s][c]));
      }
      const int hr = pass == 0 ? w : 4;
      lds_wait_ge<4>(lds, L_RCNT + 4 * pass, 2u);
      lds_wait_ge<0>(lds, L_RCNT + 4 * pass, 4u);
      v4u of[4];
#pragma unroll
      for (int s = 0; s < 4; ++s) of[s] = *LDSP(v4u, L_HREC + hr * 1024 + (n & 3) * 256 + s * 64 + g * 16);
      const unsigned tag = *LDSP(unsigned, L_TAG);
      const f32x4 wt = *LDSP(f32x4, L_WGT + hr * 16);
      f32x4 da = {0, 0, 0, 0}, dc = {0, 0, 0, 0};
      __builtin_amdgcn_sched_barrier(0);
#pragma unroll
      for (int cp = 0; cp < 4; ++cp) {
        f32x4 d0 = {0, 0, 0, 0}, d1 = {0, 0, 0, 0};
#pragma unroll
        for (int s = 0; s < 4; ++s) { d0 = mfma32(of[s], F[s][2 * cp], d0); d1 = mfma32(of[s], F[s][2 * cp + 1], d1); }
#pragma unroll
        for (int r = 0; r < 4; ++r) { da[r] = (cp == 0 || g == cp) ? d0[r] : da[r]; dc[r] = (cp == 0 || g == cp) ? d1[r] : dc[r]; }
      }
#pragma unroll
      for (int i = 0; i < 18; ++i) __builtin_amdgcn_sched_group_barrier(0x8, 1, 0);
#pragma unroll
      for (int i = 0; i < 14; ++i) { __builtin_amdgcn_sched_group_barrier(0x8, 1, 0); __builtin_amdgcn_sched_group_barrier(0x2, 1, 0); }
      const float ya = fmaf(wt[0], da[0], wt[1] * da[1]) + fmaf(wt[2], da[2], wt[3] * da[3]), yc = fmaf(wt[0], dc[0], wt[1] * dc[1]) + fmaf(wt[2], dc[2], wt[3] * dc[3]);
      st_sc1(v4u{__float_as_uint(ya), tag, __float_as_uint(yc), tag}, ry, pass == 0 ? OOB : yo, 0);
      if (pass == 0) *LDSP(f32x2, L_YS + w * 512 + 8 * lane) = f32x2{ya, yc};
      lds_inc(lds, L_YPUB + 4 * pass, lane);
    }
  } else {
    const unsigned tag = __builtin_amdgcn_readfirstlane(__hip_atomic_load((GAS unsigned*)(ws + W_EPOCH), __ATOMIC_RELAXED, __HIP_MEMORY_SCOPE_AGENT)) + 1u;
    lds_st(lds, L_TAG, tag);
    const __amdgpu_buffer_rsrc_t rs = mk(ws, W_END);
#define AN_PUT(k_, at_) do { if (sp == 0) { wait_slots<16>(lds, at_); const unsigned im = lds_ld(lds, L_IMPL); \
      st_sc1(v4u{im, tag, im, tag}, rs, lane == 0 ? W_AN + (k_) * 1024 + (b * 8 + xcd) * 16 : OOB, 0); } } while (0)
#define AN_GET(k_, at_) do { { wait_slots<16>(lds, at_); const unsigned im = lds_ld(lds, L_IMPL); \
      const v4u a = ld_sc1(rs, W_AN + (k_) * 1024 + lane * 16, 0); \
      const bool okv = a[1] == tag && a[3] == tag; const int cntv = __builtin_popcountll(__builtin_amdgcn_ballot_w64(okv)); \
      float dv = okv ? (float)(int)(a[0] - im) : 0.f; \
      dv += dpp<XOR1>(dv); dv += dpp<XOR2>(dv); dv += dpp<HMIR>(dv); dv += dpp<ROR8>(dv); dv = x32_sum(x16_sum(dv)); \
      if (cntv >= 32) lds_st(lds, L_BASE, im + (int)(dv / (float)cntv) + C::MARGIN); } } while (0)
#pragma unroll 1
    for (int it = 0; it < 2; ++it) {
      if (it == 1) { AN_PUT(0, 30u); AN_GET(0, 46u); }
      if ((jw >> 4) == it) {
        const int cb = xcd * 32 + 2 * (jw & 15);
        v4u xf[16];
        const __amdgpu_buffer_rsrc_t rx = mk(it ? xb : xa, 8 * 512 * 2);
        if (it) wait_slots<16>(lds, 52u);
#pragma unroll
        for (int s = 0; s < 16; ++s) xf[s] = ld128(rx, n * 1024 + 16 * g + 64 * s, 0);
        if (!it) wait_slots<1>(lds, C::T_SLICE);
        const v4i rw = mksrd((it ? wqb : wqa) + cb * 16, 512 * 8192 - cb * 32);
        const unsigned vo = (lane >> 2) * 8192 + (lane & 3) * 16;
        f32x4 d0 = {0, 0, 0, 0}, d1 = {0, 0, 0, 0};
#pragma unroll
        for (int hf = 0; hf < 2; ++hf) {
          if (it || hf == 0) {
#pragma unroll 1
            for (int i = 0; i < (it ? 16 : 32); ++i) {
              asm volatile("s_mov_b32 m0, %0\n\ts_nop 0\n\tbuffer_load_dwordx4 %1, %2, %3 offen nt lds" :: "s"((it ? L_P : L_S) + i * 1024), "v"(vo), "s"(rw), "s"((hf * 16 + i) * 16 * 8192) : "memory");
              if (it) __builtin_amdgcn_s_sleep(6);
            }
            asm volatile("s_waitcnt vmcnt(0)" ::: "memory");
          }
          const int ta = ((it || hf) ? L_P : L_S) + (8 * g + (n >> 2)) * 64 + (n & 3) * 8;
          v2u t[8][4];
#pragma unroll
          for (int s = 0; s < 8; ++s)
#pragma unroll
            for (int u = 0; u < 4; ++u) t[s][u] = __builtin_bit_cast(v2u, __builtin_amdgcn_ds_read_tr16_b64_v4i16(LDSP(s16x4, ta + s * 2048 + (u & 1) * 256 + (u >> 1) * 32)));
          __builtin_amdgcn_sched_barrier(0);
#pragma unroll
          for (int s = 0; s < 8; ++s) {
            d0 = mfma32(v4u{t[s][0][0], t[s][0][1], t[s][1][0], t[s][1][1]}, xf[8 * hf + s], d0);
            d1 = mfma32(v4u{t[s][2][0], t[s][2][1], t[s][3][0], t[s][3][1]}, xf[8 * hf + s], d1);
          }
          asm volatile("" : "+v"(d0), "+v"(d1) :: "memory");
        }
        d1 = f32x4{dpp<ROR8>(d1[0]), dpp<ROR8>(d1[1]), dpp<ROR8>(d1[2]), dpp<ROR8>(d1[3])};
        const f32x4 dd = n < 8 ? d0 : d1;
        st_sc1(v4u{cvtpk(dd[0], dd[1]), tag, cvtpk(dd[2], dd[3]), tag}, rs, W_QG + (it * 256 + cb) * 512 + n * 64 + g * 16, 0);
      }
      wait_slots<4>(lds, it ? 104u : C::T_GATHER);
      const int go = it == 0 ? (lane < 32 ? W_QG + (h * 8 + (lane >> 2)) * 512 + b * 64 + (lane & 3) * 16 : OOB) : W_QG + (256 + xcd * 32 + (lane >> 2)) * 512 + b * 64 + (lane & 3) * 16;
      const u64 need = it == 0 ? 0xFFFFFFFFull : ~0ull;
#pragma unroll 1
      for (unsigned spn = 0; spn < SPIN_CAP; ++spn) {
        const v4u v = ld_sc1(rs, go, 0), u = ld_sc1(rs, it == 1 ? go : OOB, 16 * 512);
        if (it == 1 || lane < 32) *LDSP(v2u, L_QIMG + (it == 1 ? 256 : 0) + 8 * lane) = v2u{v[0], v[2]};
        if (it == 1) *LDSP(v2u, L_QIMG + 768 + 8 * lane) = v2u{u[0], u[2]};
        if ((__builtin_amdgcn_ballot_w64(v[1] != tag || v[3] != tag || (it == 1 && (u[1] != tag || u[3] != tag))) & need) == 0) break;
        __builtin_amdgcn_s_sleep(24);
      }
      CBAR();
      lds_st(lds, L_QRDY + 4 * it, 1u);
    }
    AN_PUT(1, 112u); AN_GET(1, 128u); AN_PUT(2, 176u); AN_GET(2, 190u);
    lds_wait_ge<16>(lds, L_BCNT, 4u);
    {
      f32x2 ml[4]; f32x4 ac[4][2];
#pragma unroll
      for (int u = 0; u < 4; ++u) { ml[u] = *LDSP(f32x2, L_ML + 128 + u * 32 + g * 8); ac[u][0] = *LDSP(f32x4, L_S + u * 4096 + 32 * lane); ac[u][1] = *LDSP(f32x4, L_S + u * 4096 + 32 * lane + 16); }
      const float M = fmaxf(fmaxf(ml[0][0], ml[1][0]), fmaxf(ml[2][0], ml[3][0]));
      float L = 0.f; f32x4 o0 = {0, 0, 0, 0}, o1 = {0, 0, 0, 0};
#pragma unroll
      for (int u = 0; u < 4; ++u) { const float f = ex2((ml[u][0] - M) * SC2); L = fmaf(f, ml[u][1], L); o0 += f * ac[u][0]; o1 += f * ac[u][1]; }
      const float inv = 1.f / L; o0 *= inv; o1 *= inv;
      st_sc1(v4u{cvtpk(o0[0], o0[1]), cvtpk(o0[2], o0[3]), cvtpk(o1[0], o1[1]), cvtpk(o1[2], o1[3])}, rs, W_PO + wg * 1024 + 16 * lane, 0);
      asm volatile("s_waitcnt vmcnt(0)" ::: "memory");
      st_sc1(v4u{__float_as_uint(M), tag, __float_as_uint(L), tag}, rs, n == 0 ? W_ML + wg * 128 + g * 16 : OOB, 0);
    }
    wait_slots<16>(lds, C::T_ML);
    v4u mlv = {0, 0, 0, 0};
#pragma unroll 1
    for (unsigned spn = 0; spn < SPIN_CAP; ++spn) {
      mlv = ld_sc1(rs, lane < 16 ? W_ML + (xcd + 8 * (4 * b + (lane & 3))) * 128 + (lane >> 2) * 16 : OOB, 0);
      if ((__builtin_amdgcn_ballot_w64(mlv[1] != tag || mlv[3] != tag) & 0xFFFFu) == 0u) break;
      __builtin_amdgcn_s_sleep(64);
    }
    __builtin_amdgcn_fence(__ATOMIC_ACQUIRE, "wavefront");
    {
      v4u p[4];
#pragma unroll
      for (int u = 0; u < 4; ++u) p[u] = ld_sc1(rs, W_PO + (xcd + 32 * b) * 1024 + 16 * lane, u * 8192);
      const float m = __uint_as_float(mlv[0]);
      float M = fmaxf(m, dpp<XOR1>(m)); M = fmaxf(M, dpp<XOR2>(M));
      const float e = ex2((m - M) * SC2) * __uint_as_float(mlv[2]);
      float L = e + dpp<XOR1>(e); L += dpp<XOR2>(L);
      if (lane < 16) *LDSP(float, L_WGT + 4 * lane) = e / L;
#pragma unroll
      for (int u = 0; u < 4; ++u) *LDSP(v4u, L_HREC + g * 1024 + u * 256 + n * 16) = p[u];
      CBAR(); lds_st(lds, L_RCNT, 4u);
    }
    lds_wait_ge<16>(lds, L_YPUB, 4u);
    {
      const f32x2 yb = (*LDSP(f32x2, L_YS + 8 * lane) + *LDSP(f32x2, L_YS + 512 + 8 * lane)) + (*LDSP(f32x2, L_YS + 1024 + 8 * lane) + *LDSP(f32x2, L_YS + 1536 + 8 * lane));
      AN_PUT(3, 244u); AN_GET(3, 258u);
      wait_slots<8>(lds, C::T_YB);
      st_sc1(v4u{__float_as_uint(yb[0]), tag, __float_as_uint(yb[1]), tag}, rs, W_YB + wg * 1024 + (8 * n + 2 * g) * 8, 0);
    }
#pragma unroll 1
    for (int pass = 0; pass < 2; ++pass) {
      if (pass == 0) wait_slots<8>(lds, NSL - 1); else lds_wait_ge<1>(lds, L_YPUB + 4, 4u);
      const int yo = pass == 0 ? W_YB + ((lane >> 3) + 8 * (4 * b + (h >> 3))) * 1024 + (h & 7) * 128 + (lane & 7) * 16
                               : W_YA + (b * 32 * 512 + (lane >> 3) * 512 + 16 * h + 2 * (lane & 7)) * 8;
      v4u y[4]; bool ok[4];
#pragma unroll
      for (int i = 0; i < 4; ++i) { y[i] = v4u{0, 0, 0, 0}; ok[i] = pass == 0 && i > 0; }
#pragma unroll 1
      for (unsigned spn = 0; spn < SPIN_CAP; ++spn) {
        v4u z[4];
#pragma unroll
        for (int i = 0; i < 4; ++i) z[i] = ld_sc1(rs, ok[i] ? OOB : yo, i * 8 * 512 * 8);
        bool all = true;
#pragma unroll
        for (int i = 0; i < 4; ++i) { if (!ok[i]) { y[i] = z[i]; ok[i] = z[i][1] == tag && z[i][3] == tag; } all &= ok[i]; }
        if (__builtin_amdgcn_ballot_w64(!all) == 0) break;
        __builtin_amdgcn_s_sleep(2);
      }
      float s0 = (__uint_as_float(y[0][0]) + __uint_as_float(y[1][0])) + (__uint_as_float(y[2][0]) + __uint_as_float(y[3][0]));
      float s1 = (__uint_as_float(y[0][2]) + __uint_as_float(y[1][2])) + (__uint_as_float(y[2][2]) + __uint_as_float(y[3][2]));
      s0 += dpp<ROR8>(s0); s1 += dpp<ROR8>(s1);
      s0 = x32_sum(x16_sum(s0)); s1 = x32_sum(x16_sum(s1));
      if (lane < 8) *(GAS unsigned*)(out + b * 1024 + (pass == 0 ? 512 : 0) + 16 * h + 2 * lane) = cvtpk(s0, s1);
    }
    if (wg == 0 && lane == 0) __hip_atomic_store((GAS unsigned*)(ws + W_EPOCH), tag, __ATOMIC_RELAXED, __HIP_MEMORY_SCOPE_AGENT);
  }
}

extern "C" void kernel_launch(void* const* d_in, const int* in_sizes, int n_in, void* d_out, int out_size, void* d_ws, size_t ws_size, hipStream_t stream) {
  (void)in_sizes; (void)n_in; (void)out_size; (void)ws_size;
  (void)hipFuncSetAttribute((const void*)fused_decode, hipFuncAttributeMaxDynamicSharedMemorySize, LDS_BYTES);
  hipLaunchKernelGGL(fused_decode, dim3(256), dim3(NTHR), LDS_BYTES, stream,
                     (const u16*)d_in[0], (const u16*)d_in[1], (const u16*)d_in[2], (const u16*)d_in[3], (const u16*)d_in[4],
                     (const u16*)d_in[5], (const u16*)d_in[6], (const u16*)d_in[7], (const u16*)d_in[8], (const u16*)d_in[9],
                     (u16*)d_out, (unsigned char*)d_ws);
}
```

```cpp
#include <hip/hip_runtime.h>
#include <hip/hip_bf16.h>
#include <stdint.h>

typedef unsigned v4u __attribute__((ext_vector_type(4)));
typedef unsigned v2u __attribute__((ext_vector_type(2)));
typedef int v4i __attribute__((ext_vector_type(4)));
typedef float f32x4 __attribute__((ext_vector_type(4)));
typedef float f32x2 __attribute__((ext_vector_type(2)));
typedef short s16x4 __attribute__((ext_vector_type(4)));
typedef __bf16 bf16x8 __attribute__((ext_vector_type(8)));
typedef __bf16 bf16x2 __attribute__((ext_vector_type(2)));
typedef unsigned short u16;
typedef unsigned long long u64;
#define GAS __attribute__((address_space(1)))
#define LAS __attribute__((address_space(3)))

struct C {
  static constexpr int NFL = 20;
  static constexpr int MUL = 11;
  static constexpr int PACE = 119, PACE2 = 112;
  static constexpr int PACE0 = 20, MARGIN = 20;
  static constexpr int DN = 40, U1 = 60, U2 = 110, U3 = 160, U4 = 220;
  static constexpr int T2 = 264, GAIN2 = 3;
  static constexpr int CODE_LINES = 145;
  static constexpr int T_SLICE = 3, T_GATHER = 17, T_ML = 216, T_YB = 314;
};

constexpr int SK = 4096, DH = 128, NH = 32, HKV = 8;
constexpr float SC2 = 0.088388347648318440f * 1.4426950408889634f;
constexpr int NWAVE = 10, NTHR = NWAVE * 64;
constexpr int NSL = 320;
constexpr int PARK = 4;
constexpr int RING = 15, SLOT = 8192;
constexpr int L_FULL = 0  , L_QRDY = 4  , L_BCNT = 12, L_DONE = 16  , L_RCNT = 32  , L_YPUB = 40  ,
              L_TAG = 48, L_MLC = 52, L_IMPL = 56, L_BASE = 60;
constexpr int L_QIMG = 64;
constexpr int L_WGT = L_QIMG + 1280;
constexpr int L_ML = L_WGT + 128;
constexpr int L_HREC = L_ML + 256;
constexpr int L_JUNK = 7936;
constexpr int L_S = 8192;
constexpr int L_P = L_S + 16384;
constexpr int L_YS = L_P + 8192;
constexpr int L_RING = L_P + 16384;
constexpr int LDS_BYTES = 160 * 1024;
static_assert(L_HREC % 16 == 0 && L_HREC + 5 * 1024 <= 7168 && L_RING + RING * SLOT <= LDS_BYTES, "lds map");
constexpr int W_EPOCH = 0;
constexpr int W_ML = 4096;
constexpr int W_AN = 49152;
constexpr int W_QG = 65536;
constexpr int W_PO = W_QG + 2 * 256 * 512;
constexpr int W_YB = W_PO + 256 * 1024;
constexpr int W_YA = W_YB + 256 * 1024;
constexpr int W_END = W_YA + 8 * 32 * 512 * 8;
constexpr unsigned SPIN_CAP = 1u << 20;
constexpr int OOB = 0x7FFFFF00;

__device__ __forceinline__ unsigned cvtpk(float lo, float hi) { return __builtin_bit_cast(unsigned, __builtin_convertvector(f32x2{lo, hi}, bf16x2)); }
__device__ __forceinline__ __amdgpu_buffer_rsrc_t mk(const void* p, unsigned bytes) { return __builtin_amdgcn_make_buffer_rsrc((void*)p, 0, bytes, 0x00020000); }
__device__ __forceinline__ v4i mksrd(const void* p, unsigned bytes) { return v4i{(int)(uintptr_t)p, (int)((uintptr_t)p >> 32), (int)bytes, 0x00020000}; }
__device__ __forceinline__ f32x4 mfma32(v4u a, v4u b, f32x4 c) { return __builtin_amdgcn_mfma_f32_16x16x32_bf16(__builtin_bit_cast(bf16x8, a), __builtin_bit_cast(bf16x8, b), c, 0, 0, 0); }
__device__ __forceinline__ f32x4 mfma16(v2u a, v2u b, f32x4 c) { return __builtin_amdgcn_mfma_f32_16x16x16bf16_1k(__builtin_bit_cast(s16x4, a), __builtin_bit_cast(s16x4, b), c, 0, 0, 0); }
__device__ __forceinline__ float ex2(float x) { return __builtin_amdgcn_exp2f(x); }
__device__ __forceinline__ v4u ld128(__amdgpu_buffer_rsrc_t r, int vo, int so) { return __builtin_amdgcn_raw_buffer_load_b128(r, vo, so, 0); }
__device__ __forceinline__ v4u ld_sc1(__amdgpu_buffer_rsrc_t r, int vo, int so) { return __builtin_amdgcn_raw_buffer_load_b128(r, vo, so, 16); }
__device__ __forceinline__ void st_sc1(v4u v, __amdgpu_buffer_rsrc_t r, int vo, int so) { __builtin_amdgcn_raw_buffer_store_b128(v, r, vo, so, 16); }
template <int CTRL> __device__ __forceinline__ float dpp(float x) { return __builtin_bit_cast(float, __builtin_amdgcn_mov_dpp(__builtin_bit_cast(int, x), CTRL, 0xf, 0xf, true)); }
constexpr int XOR1 = 0xB1, XOR2 = 0x4E, HMIR = 0x141, ROR8 = 0x128;
__device__ __forceinline__ float x16_sum(float x) { auto s = __builtin_amdgcn_permlane16_swap(__float_as_uint(x), __float_as_uint(x), false, false); return __uint_as_float(s[0]) + __uint_as_float(s[1]); }
__device__ __forceinline__ float x32_sum(float x) { auto s = __builtin_amdgcn_permlane32_swap(__float_as_uint(x), __float_as_uint(x), false, false); return __uint_as_float(s[0]) + __uint_as_float(s[1]); }
__device__ __forceinline__ float xg_max(float x) {
  auto s = __builtin_amdgcn_permlane16_swap(__float_as_uint(x), __float_as_uint(x), false, false);
  x = fmaxf(__uint_as_float(s[0]), __uint_as_float(s[1]));
  auto t = __builtin_amdgcn_permlane32_swap(__float_as_uint(x), __float_as_uint(x), false, false);
  return fmaxf(__uint_as_float(t[0]), __uint_as_float(t[1]));
}
#define CBAR() asm volatile("" ::: "memory")
#define LDSP(T, off) ((LAS T*)(lds + (off)))
__device__ __forceinline__ unsigned lds_ld(LAS unsigned char* lds, int off) { return __builtin_amdgcn_readfirstlane(*(volatile LAS unsigned*)(lds + off)); }
__device__ __forceinline__ void lds_st(LAS unsigned char* lds, int off, unsigned v) { *(volatile LAS unsigned*)(lds + off) = v; }
template <int SLP> __device__ __forceinline__ unsigned lds_wait_ge(LAS unsigned char* lds, int off, unsigned want) {
  unsigned v = lds_ld(lds, off);
#pragma unroll 1
  for (unsigned sp = 0; v < want && sp < SPIN_CAP * 32 / (SLP + 1); ++sp) { if (SLP) __builtin_amdgcn_s_sleep(SLP); v = lds_ld(lds, off); }
  CBAR();
  return v;
}
template <int SLP> __device__ __forceinline__ void wait_slots(LAS unsigned char* lds, unsigned slots) { lds_wait_ge<SLP>(lds, L_FULL, 2 * slots); }
__device__ __forceinline__ void lds_inc(LAS unsigned char* lds, int off, int lane) {
  CBAR();
  if (lane == 0) __hip_atomic_fetch_add(LDSP(unsigned, off), 1u, __ATOMIC_RELAXED, __HIP_MEMORY_SCOPE_WORKGROUP);
  CBAR();
}

template <int N> __device__ __forceinline__ void dma4(unsigned m0v, unsigned v0, unsigned v1, unsigned v2, unsigned v3, v4i srd, unsigned so) {
  asm volatile("s_mov_b32 m0, %0\n\t"
               "s_waitcnt vmcnt(%7)\n\tbuffer_load_dwordx4 %1, %5, %6 offen nt lds\n\t"
               "s_waitcnt vmcnt(%7)\n\tbuffer_load_dwordx4 %2, %5, %6 offen offset:1024 nt lds\n\t"
               "s_waitcnt vmcnt(%7)\n\tbuffer_load_dwordx4 %3, %5, %6 offen offset:2048 nt lds\n\t"
               "s_waitcnt vmcnt(%7)\n\tbuffer_load_dwordx4 %4, %5, %6 offen offset:3072 nt lds"
               :: "s"(m0v), "v"(v0), "v"(v1), "v"(v2), "v"(v3), "s"(srd), "s"(so), "i"(N - 1) : "memory");
}

__global__ void __launch_bounds__(NTHR, 3) fused_decode(const u16* xa, const u16* ak, const u16* av, const u16* wqa, const u16* woa,
                                                        const u16* xb, const u16* bk, const u16* bv, const u16* wqb, const u16* wob,
                                                        u16* out, unsigned char* ws) {
  constexpr int NFL = C::NFL;
  {
    u64 pc; unsigned tv;
    asm volatile("s_getpc_b64 %0\n\tv_min_u32 %1, %3, %2\n\ts_movk_i32 m0, %4\n\tv_lshlrev_b32 %1, 7, %1\n\tglobal_load_lds_dword %1, %0"
                 : "=&s"(pc), "=&v"(tv) : "v"(threadIdx.x), "i"(C::CODE_LINES - 1), "i"(L_JUNK));
    __builtin_amdgcn_sched_barrier(0);
  }
  extern __shared__ __attribute__((aligned(16))) unsigned char lds_raw[];
  LAS unsigned char* lds = (LAS unsigned char*)lds_raw;
  const int wg = blockIdx.x, lane = threadIdx.x & 63, wave = __builtin_amdgcn_readfirstlane(threadIdx.x >> 6);
  const int xcd = wg & 7, jw = wg >> 3, b = jw >> 2, sp = jw & 3, h = 4 * xcd + sp;
  const int g = lane >> 4, n = lane & 15;
  *(volatile LAS unsigned*)(lds + 4 * n) = 0u;
  asm volatile("s_waitcnt lgkmcnt(0)" :: "s"(wqb), "s"(wob), "s"(out), "s"(ws), "s"(xa) : "memory");
  __builtin_amdgcn_s_barrier();
  const unsigned k0 = g * 256 + ((n ^ g) * 16);
#define SLOT_OFF(r_) (((((r_) & 0x70u) << 1) | ((r_) & 15u)) << 12)
  {
#define E4(m_, so_) "s_mov_b32 m0, " m_ "\n\ts_mov_b32 %0, " so_ "\n\t" \
               "buffer_load_dwordx4 %2, %6, %0 offen nt lds\n\tbuffer_load_dwordx4 %3, %6, %0 offen offset:1024 nt lds\n\t" \
               "buffer_load_dwordx4 %4, %6, %0 offen offset:2048 nt lds\n\tbuffer_load_dwordx4 %5, %6, %0 offen offset:3072 nt lds\n\t"
    unsigned tmp;
    asm volatile("s_cmp_lg_u32 %1, 0\n\ts_cbranch_scc1 1f\n\t"
                 E4("%7", "0") E4("%7+4096", "65536") E4("%7+8192", "%8") E4("%7+12288", "%8+65536") E4("%7+16384", "%9") E4("%7+20480", "%9+65536")
                 "1:"
                 : "=&s"(tmp) : "s"(wave), "v"(k0), "v"(k0 ^ 64), "v"(k0 ^ 128), "v"(k0 ^ 192), "s"(mksrd(ak + (size_t)(b * NH + h) * SK * DH, SK * 256)),
                   "i"(L_RING), "i"(SLOT_OFF(C::MUL)), "i"(SLOT_OFF(2 * C::MUL)) : "memory", "scc");
  }
  asm volatile("s_cmp_lg_u32 %0, 2\n\ts_cbranch_scc1 1f\n\ts_movk_i32 m0, %1\n\ts_nop 0\n\t"
               "global_load_lds_dword %2, %6 nt\n\tglobal_load_lds_dword %5, %7 nt\n\tglobal_load_lds_dword %5, %8 nt\n\tglobal_load_lds_dword %2, %9 nt\n\t"
               "global_load_lds_dword %2, %10 nt\n\tglobal_load_lds_dword %2, %11 nt\n\tglobal_load_lds_dword %3, %12 nt\n\tglobal_load_lds_dword %3, %13 nt\n\t"
               "global_load_lds_dword %4, %14 nt\n\t1:"
               :: "s"(wave), "i"(L_JUNK), "v"((lane & 1) * 2097152 + wg * 128), "v"((b * HKV + xcd) << 20), "v"((b * NH + h) << 20), "v"(0),
                  "s"(wqa), "s"(ws + 2048), "s"(xa), "s"(wqb), "s"(wob), "s"(woa), "s"(bk), "s"(bv), "s"(av) : "scc");

  if (wave == 0) {
    __builtin_amdgcn_s_setprio(3);
    const size_t ua = (size_t)(b * NH + h) * SK * DH, ub = (size_t)(b * HKV + xcd) * SK * DH;
    const unsigned vv = lane * 16;
    unsigned slot = L_RING + 3 * SLOT;
    int limit = RING;
    u64 now = __builtin_amdgcn_s_memrealtime();
    unsigned base = (unsigned)now + C::PACE0, due = -C::PACE;
    now = base + due;
    int t = 3;
#pragma unroll 1
    for (int ph = 0; ph < 4; ++ph) {
      const v4i rs = mksrd(ph == 0 ? ak + ua : ph == 1 ? bk + ub : ph == 2 ? bv + ub : av + ua, SK * 256);
      const int tend = ph == 0 ? 128 : ph == 1 ? 160 : ph == 2 ? 192 : NSL;
      const unsigned v0 = ph < 2 ? k0 : vv, v1 = ph < 2 ? k0 ^ 64 : vv, v2 = ph < 2 ? k0 ^ 128 : vv, v3 = ph < 2 ? k0 ^ 192 : vv;
      unsigned r = (ph == 0 ? 3 * C::MUL : ph == 3 ? 0 : sp * 32 * C::MUL) & 127u;
#pragma unroll 1
      while (t < tend) {
        if (t + 3 >= limit) {
#pragma unroll 1
          for (unsigned spn = 0; spn < SPIN_CAP * 16; ++spn) {
            const v4u d = *(volatile LAS v4u*)(lds + L_DONE);
            const unsigned nb = lds_ld(lds, L_BASE); base = nb ? nb : base;
            const int c = min(min(4 * (int)d[0], 4 * (int)d[1] + 1), min(4 * (int)d[2] + 2, 4 * (int)d[3] + 3));
            limit = __builtin_amdgcn_readfirstlane(c) + RING;
            if (t + 3 < limit) break;
            __builtin_amdgcn_s_sleep(1);
          }
        }
#define RUN4(N_) do { static_assert(((N_) + 3) / 4 >= 5 && ((N_) + 3) / 4 <= 7, "the published count never goes down"); \
        _Pragma("unroll 1") for (const int te = (t | 3) + 1; t < te; ++t) { \
          const unsigned so = SLOT_OFF(r); \
          dma4<N_>(slot, v0, v1, v2, v3, rs, so); \
          dma4<N_>(slot + 4096, v0, v1, v2, v3, rs, so + 65536u); \
          asm volatile("s_waitcnt lgkmcnt(0)");     \
          lds_st(lds, L_FULL, (unsigned)(2 * t + 2 - ((N_) + 3) / 4)); \
          slot += SLOT; if (slot >= L_RING + RING * SLOT) slot = L_RING; \
          r = (r + C::MUL) & 127u; } } while (0)
        asm volatile("" : "+s"(now));
        lds_st(lds, L_IMPL, (unsigned)now - due);
        const int lagr = (int)((unsigned)now - due - base), lag = t >= C::T2 ? lagr * C::GAIN2 : lagr;
        due += ph == 0 ? C::PACE : C::PACE2;
        asm volatile("s_memrealtime %0" : "=s"(now));
        if (lag < C::U1) { if (lag >= -C::DN) RUN4(NFL); else RUN4(NFL - 1); }
        else if (lagr < C::U3) { if (lag < C::U2) RUN4(NFL + 1); else RUN4(NFL + 2); }
        else if (lagr < C::U4) RUN4(NFL + 3); else RUN4(NFL + 5);
      }
    }
    asm volatile("s_waitcnt vmcnt(16)" ::: "memory"); lds_st(lds, L_FULL, 2 * NSL - 4);
    asm volatile("s_waitcnt vmcnt(12)" ::: "memory"); lds_st(lds, L_FULL, 2 * NSL - 3);
    asm volatile("s_waitcnt vmcnt(8)" ::: "memory");  lds_st(lds, L_FULL, 2 * NSL - 2);
    asm volatile("s_waitcnt vmcnt(4)" ::: "memory");  lds_st(lds, L_FULL, 2 * NSL - 1);
    asm volatile("s_waitcnt vmcnt(0)" ::: "memory");  lds_st(lds, L_FULL, 2 * NSL);
  } else if (wave >= 2 && wave < 6) {
    const int w = wave - 2, jj = n >> 2;
    int slot = L_RING + w * SLOT; unsigned full = 0, cnt = 0; int t = w;
#define HALF_WAIT(k_, slp_) do { if (full < (unsigned)(2 * t + (k_))) full = lds_wait_ge<slp_>(lds, L_FULL, (unsigned)(2 * t + (k_))); CBAR(); } while (0)
#define SLOT_DONE() do { CBAR(); ++cnt; lds_st(lds, L_DONE + 4 * w, cnt);     \
        CBAR(); slot += 4 * SLOT; if (slot >= L_RING + RING * SLOT) slot -= RING * SLOT; t += 4; } while (0)
#define K_READ(T_) do { HALF_WAIT(2, 1); _Pragma("unroll") for (int m_ = 0; m_ < 8; ++m_) T_[m_] = *LDSP(v4u, ((slot + kb) ^ (64 * (m_ & 3))) + (m_ >> 2) * 4096); SLOT_DONE(); } while (0)
    const int sw = L_S + w * 4096;
    {
      const int kb = n * 256 + ((g ^ n) * 16);
      v4u q[4];
      float mx = -3.0e38f;
      const int sa0 = sw + ((n & 3) * 8 + g * 2 + (jj & 1)) * 16;
      int sa = sa0;
#define K_MATH(T_, act_, step_) do { f32x4 s0 = {0.f, 0.f, 0.f, 0.f}, s1 = {0.f, 0.f, 0.f, 0.f}; \
        _Pragma("unroll") for (int m = 0; m < 4; ++m) { s0 = mfma32(T_[m], q[m], s0); s1 = mfma32(T_[4 + m], q[m], s1); }       \
        mx = fmaxf(fmaxf(fmaxf(fmaxf(s0[0], s0[1]), fmaxf(s0[2], s0[3])), fmaxf(fmaxf(s1[0], s1[1]), fmaxf(s1[2], s1[3]))), mx); \
        if (act_) *LDSP(f32x4, sa) = (jj & 1) ? s1 : s0; \
        sa += (step_); } while (0)
      {
        v4u P[PARK][8];
#pragma unroll
        for (int i = 0; i < PARK; ++i) K_READ(P[i]);
        lds_wait_ge<1>(lds, L_QRDY, 1u);
#pragma unroll
        for (int m = 0; m < 4; ++m) q[m] = *LDSP(v4u, L_QIMG + g * 16 + 64 * m);
#pragma unroll
        for (int i = 0; i < PARK; ++i) K_MATH(P[i], (n & 11) == 0, 128);
      }
#pragma unroll 1
      for (int ph = 0; ph < 2; ++ph) {
        const bool act = ph == 1 ? n < 8 : (n & 11) == 0;
        const int sstep = ph == 1 ? 512 : 128, tend = ph == 0 ? 128 : 160;
#pragma unroll 1
        while (t < tend) { v4u T[8]; K_READ(T); K_MATH(T, act, sstep); }
        lds_wait_ge<1>(lds, L_QRDY + 4, 1u);
        const float Mh = xg_max(mx);
        const float M = __builtin_bit_cast(float, __builtin_amdgcn_ds_bpermute(((lane >> 3) & 3) * 4, __builtin_bit_cast(int, Mh)));
        const float mc = -M * SC2;
        float l = 0.f;
        CBAR();
#pragma unroll
        for (int j = 0; j < 4; ++j) {
          const f32x4 s = *LDSP(f32x4, sw + 16 * lane + 1024 * j);
          const float p0 = ex2(fmaf(s[0], SC2, mc)), p1 = ex2(fmaf(s[1], SC2, mc)), p2 = ex2(fmaf(s[2], SC2, mc)), p3 = ex2(fmaf(s[3], SC2, mc));
          l += (p0 + p1) + (p2 + p3);
          *LDSP(v2u, L_P + ph * 8192 + w * 2048 + 8 * lane + 512 * j) = v2u{cvtpk(p0, p1), cvtpk(p2, p3)};
        }
        l += dpp<XOR1>(l); l += dpp<XOR2>(l); l += dpp<HMIR>(l); l = x32_sum(l);
        const float la = x16_sum(l + dpp<ROR8>(l));
        if ((lane & 39) == 0) *LDSP(f32x2, L_ML + ph * 128 + w * 32 + lane) = f32x2{M, ph == 1 ? l : la};
        lds_inc(lds, L_MLC, lane);
        mx = -3.0e38f; sa = sa0;
#pragma unroll
        for (int m = 0; m < 4; ++m) q[m] = *LDSP(v4u, L_QIMG + 256 + (n & 3) * 256 + g * 16 + 64 * m);
      }
    }
    {
      const unsigned Z = 0x0C0C0C0Cu;
      const unsigned el0 = jj == 0 ? 0x0C0C0100u : jj == 1 ? 0x01000C0Cu : Z, el1 = jj == 2 ? 0x0C0C0100u : jj == 3 ? 0x01000C0Cu : Z;
      const unsigned eh0 = jj == 0 ? 0x0C0C0302u : jj == 1 ? 0x03020C0Cu : Z, eh1 = jj == 2 ? 0x0C0C0302u : jj == 3 ? 0x03020C0Cu : Z;
      const int vb = g * 1024 + n * 16;
#pragma unroll 1
      for (int ph = 1; ph >= 0; --ph) {
        if (ph == 0) {
          lds_wait_ge<1>(lds, L_MLC, 8u);
          f32x2 ml[4];
#pragma unroll
          for (int u = 0; u < 4; ++u) ml[u] = *LDSP(f32x2, L_ML + u * 32);
          const float M = fmaxf(fmaxf(ml[0][0], ml[1][0]), fmaxf(ml[2][0], ml[3][0]));
          float L = 0.f;
#pragma unroll
          for (int u = 0; u < 4; ++u) L = fmaf(ex2((ml[u][0] - M) * SC2), ml[u][1], L);
          *LDSP(float, L_WGT + 64 + 4 * w) = ex2(((*LDSP(f32x2, L_ML + w * 32))[0] - M) * SC2) / L;
        }
        const int tend = ph == 1 ? 192 : NSL, pstep = ph == 1 ? 256 : 64;
        int pa = L_P + ph * 8192 + w * 2048 + (ph == 1 ? (n & 3) * 64 : 0) + g * 16;
        f32x4 a0 = {0, 0, 0, 0}, a1 = {0, 0, 0, 0};
#pragma unroll 1
        while (t < tend) {
          const v4u pp = *LDSP(v4u, pa); pa += pstep;
          v2u bf[8];
#pragma unroll
          for (int i = 0; i < 8; ++i) { const unsigned pk = pp[i >> 1]; bf[i] = v2u{__builtin_amdgcn_perm(pk, pk, (i & 1) ? eh0 : el0), __builtin_amdgcn_perm(pk, pk, (i & 1) ? eh1 : el1)}; }
#pragma unroll
          for (int i = 0; i < 8; ++i) asm volatile("" : "+v"(bf[i]));
          v4u T[8];
          HALF_WAIT(1, 1);
#pragma unroll
          for (int i = 0; i < 4; ++i) T[i] = *LDSP(v4u, slot + vb + i * 256);
#pragma unroll
          for (int i = 0; i < 4; ++i) { a0 = mfma16(v2u{T[i][0], T[i][1]}, bf[i], a0); a1 = mfma16(v2u{T[i][2], T[i][3]}, bf[i], a1); }
          asm volatile("" : "+v"(a0), "+v"(a1));
          HALF_WAIT(2, 0);
#pragma unroll
          for (int i = 4; i < 8; ++i) T[i] = *LDSP(v4u, slot + vb + 4096 + (i & 3) * 256);
          SLOT_DONE();
#pragma unroll
          for (int i = 4; i < 8; ++i) { a0 = mfma16(v2u{T[i][0], T[i][1]}, bf[i], a0); a1 = mfma16(v2u{T[i][2], T[i][3]}, bf[i], a1); }
        }
        if ((n & 3) == 0) *LDSP(v4u, L_HREC + 4096 + w * 256 + jj * 64 + g * 16) = v4u{cvtpk(a0[0], a1[0]), cvtpk(a0[1], a1[1]), cvtpk(a0[2], a1[2]), cvtpk(a0[3], a1[3])};
        if (ph == 1) {
          const int ro = sw + (n & 3) * 512 + jj * 128 + g * 32;
          *LDSP(f32x4, ro) = f32x4{a0[0], a1[0], a0[1], a1[1]};
          *LDSP(f32x4, ro + 16) = f32x4{a0[2], a1[2], a0[3], a1[3]};
        }
        lds_inc(lds, ph == 1 ? L_BCNT : L_RCNT + 4, lane);
      }
    }
  } else if (wave >= 6) {
    const int w = wave - 6;
#pragma unroll 1
    for (int mat = 0; mat < 2; ++mat) {
      if (mat) wait_slots<16>(lds, 32u);
      const __amdgpu_buffer_rsrc_t rw = mk((mat ? wqb : wqa) + (size_t)(16 * jw + w) * 4096 + xcd * 512, 13 * 8192);
      v4u d[4];
#pragma unroll
      for (int i = 0; i < 4; ++i) d[i] = ld128(rw, lane * 16, ((i & 1) * 8 + (i >> 1) * 4) * 8192);
#pragma unroll
      for (int i = 0; i < 4; ++i) asm volatile("" :: "v"(d[i]));
    }
    const __amdgpu_buffer_rsrc_t ry = mk(ws + W_YA, W_END - W_YA);
    const int yo = ((b * 32 + h) * 512 + 128 * w + 8 * n + 2 * g) * 8;
    wait_slots<16>(lds, 72u);
#pragma unroll 1
    for (int pass = 0; pass < 2; ++pass) {
      const __amdgpu_buffer_rsrc_t rw = mk((pass == 0 ? wob : woa) + (size_t)xcd * 512 * 512, 512 * 1024);
      {
        v4u d[4];
#pragma unroll
        for (int i = 0; i < 4; ++i) d[i] = ld128(rw, (2 * b + (i >> 1) + 16 * (i & 1)) * 4096 + lane * 16, sp * 131072 + w * 1024);
#pragma unroll
        for (int i = 0; i < 4; ++i) asm volatile("" :: "v"(d[i]));
        lds_wait_ge<16>(lds, L_FULL, min(lds_ld(lds, L_FULL) + 24u, 2u * NSL));
      }
      const int wv = (pass == 0 ? w * 131072 + sp * 256 : sp * 131072 + w * 256) + g * 32768 + n * 16;
      v4u F[4][8];
#pragma unroll
      for (int s = 0; s < 4; ++s) {
#pragma unroll
        for (int jp = 0; jp < 4; ++jp) {
          const v4u w0 = ld128(rw, wv, (8 * jp + s) * 1024), w1 = ld128(rw, wv, (8 * jp + 4 + s) * 1024);
#pragma unroll
          for (int d = 0; d < 4; ++d) {
            unsigned lo = __builtin_amdgcn_perm(w1[d], w0[d], 0x05040100u), hi = __builtin_amdgcn_perm(w1[d], w0[d], 0x07060302u);
            asm volatile("" : "+v"(lo), "+v"(hi));
            F[s][2 * d][jp] = lo; F[s][2 * d + 1][jp] = hi;
          }
          __builtin_amdgcn_sched_barrier(0);
        }
#pragma unroll
        for (int c = 0; c < 8; ++c) asm volatile("" : "+v"(F[s][c]));
      }
      const int hr = pass == 0 ? w : 4;
      lds_wait_ge<4>(lds, L_RCNT + 4 * pass, 2u);
      lds_wait_ge<0>(lds, L_RCNT + 4 * pass, 4u);
      v4u of[4];
#pragma unroll
      for (int s = 0; s < 4; ++s) of[s] = *LDSP(v4u, L_HREC + hr * 1024 + (n & 3) * 256 + s * 64 + g * 16);
      const unsigned tag = *LDSP(unsigned, L_TAG);
      const f32x4 wt = *LDSP(f32x4, L_WGT + hr * 16);
      f32x4 da = {0, 0, 0, 0}, dc = {0, 0, 0, 0};
      __builtin_amdgcn_sched_barrier(0);
#pragma unroll
      for (int cp = 0; cp < 4; ++cp) {
        f32x4 d0 = {0, 0, 0, 0}, d1 = {0, 0, 0, 0};
#pragma unroll
        for (int s = 0; s < 4; ++s) { d0 = mfma32(of[s], F[s][2 * cp], d0); d1 = mfma32(of[s], F[s][2 * cp + 1], d1); }
#pragma unroll
        for (int r = 0; r < 4; ++r) { da[r] = (cp == 0 || g == cp) ? d0[r] : da[r]; dc[r] = (cp == 0 || g == cp) ? d1[r] : dc[r]; }
      }
#pragma unroll
      for (int i = 0; i < 18; ++i) __builtin_amdgcn_sched_group_barrier(0x8, 1, 0);
#pragma unroll
      for (int i = 0; i < 14; ++i) { __builtin_amdgcn_sched_group_barrier(0x8, 1, 0); __builtin_amdgcn_sched_group_barrier(0x2, 1, 0); }
      const float ya = fmaf(wt[0], da[0], wt[1] * da[1]) + fmaf(wt[2], da[2], wt[3] * da[3]), yc = fmaf(wt[0], dc[0], wt[1] * dc[1]) + fmaf(wt[2], dc[2], wt[3] * dc[3]);
      st_sc1(v4u{__float_as_uint(ya), tag, __float_as_uint(yc), tag}, ry, pass == 0 ? OOB : yo, 0);
      if (pass == 0) *LDSP(f32x2, L_YS + w * 512 + 8 * lane) = f32x2{ya, yc};
      lds_inc(lds, L_YPUB + 4 * pass, lane);
    }
  } else {
    const unsigned tag = __builtin_amdgcn_readfirstlane(__hip_atomic_load((GAS unsigned*)(ws + W_EPOCH), __ATOMIC_RELAXED, __HIP_MEMORY_SCOPE_AGENT)) + 1u;
    lds_st(lds, L_TAG, tag);
    const __amdgpu_buffer_rsrc_t rs = mk(ws, W_END);
#define AN_PUT(k_, at_) do { if (sp == 0) { wait_slots<16>(lds, at_); const unsigned im = lds_ld(lds, L_IMPL); \
      st_sc1(v4u{im, tag, im, tag}, rs, lane == 0 ? W_AN + (k_) * 1024 + (b * 8 + xcd) * 16 : OOB, 0); } } while (0)
#define AN_GET(k_, at_) do { { wait_slots<16>(lds, at_); const unsigned im = lds_ld(lds, L_IMPL); \
      const v4u a = ld_sc1(rs, W_AN + (k_) * 1024 + lane * 16, 0); \
      const bool okv = a[1] == tag && a[3] == tag; const int cntv = __builtin_popcountll(__builtin_amdgcn_ballot_w64(okv)); \
      float dv = okv ? (float)(int)(a[0] - im) : 0.f; \
      dv += dpp<XOR1>(dv); dv += dpp<XOR2>(dv); dv += dpp<HMIR>(dv); dv += dpp<ROR8>(dv); dv = x32_sum(x16_sum(dv)); \
      if (cntv >= 32) lds_st(lds, L_BASE, im + (int)(dv / (float)cntv) + C::MARGIN); } } while (0)
#pragma unroll 1
    for (int it = 0; it < 2; ++it) {
      if (it == 1) { AN_PUT(0, 30u); AN_GET(0, 46u); }
      if ((jw >> 4) == it) {
        const int cb = xcd * 32 + 2 * (jw & 15);
        v4u xf[16];
        const __amdgpu_buffer_rsrc_t rx = mk(it ? xb : xa, 8 * 512 * 2);
        if (it) wait_slots<16>(lds, 52u);
#pragma unroll
        for (int s = 0; s < 16; ++s) xf[s] = ld128(rx, n * 1024 + 16 * g + 64 * s, 0);
        if (!it) wait_slots<1>(lds, C::T_SLICE);
        const v4i rw = mksrd((it ? wqb : wqa) + cb * 16, 512 * 8192 - cb * 32);
        const unsigned vo = (lane >> 2) * 8192 + (lane & 3) * 16;
        f32x4 d0 = {0, 0, 0, 0}, d1 = {0, 0, 0, 0};
#pragma unroll
        for (int hf = 0; hf < 2; ++hf) {
          if (it || hf == 0) {
#pragma unroll 1
            for (int i = 0; i < (it ? 16 : 32); ++i) {
              asm volatile("s_mov_b32 m0, %0\n\ts_nop 0\n\tbuffer_load_dwordx4 %1, %2, %3 offen nt lds" :: "s"((it ? L_P : L_S) + i * 1024), "v"(vo), "s"(rw), "s"((hf * 16 + i) * 16 * 8192) : "memory");
              if (it) __builtin_amdgcn_s_sleep(6);
            }
            asm volatile("s_waitcnt vmcnt(0)" ::: "memory");
          }
          const int ta = ((it || hf) ? L_P : L_S) + (8 * g + (n >> 2)) * 64 + (n & 3) * 8;
          v2u t[8][4];
#pragma unroll
          for (int s = 0; s < 8; ++s)
#pragma unroll
            for (int u = 0; u < 4; ++u) t[s][u] = __builtin_bit_cast(v2u, __builtin_amdgcn_ds_read_tr16_b64_v4i16(LDSP(s16x4, ta + s * 2048 + (u & 1) * 256 + (u >> 1) * 32)));
          __builtin_amdgcn_sched_barrier(0);
#pragma unroll
          for (int s = 0; s < 8; ++s) {
            d0 = mfma32(v4u{t[s][0][0], t[s][0][1], t[s][1][0], t[s][1][1]}, xf[8 * hf + s], d0);
            d1 = mfma32(v4u{t[s][2][0], t[s][2][1], t[s][3][0], t[s][3][1]}, xf[8 * hf + s], d1);
          }
          asm volatile("" : "+v"(d0), "+v"(d1) :: "memory");
        }
        d1 = f32x4{dpp<ROR8>(d1[0]), dpp<ROR8>(d1[1]), dpp<ROR8>(d1[2]), dpp<ROR8>(d1[3])};
        const f32x4 dd = n < 8 ? d0 : d1;
        st_sc1(v4u{cvtpk(dd[0], dd[1]), tag, cvtpk(dd[2], dd[3]), tag}, rs, W_QG + (it * 256 + cb) * 512 + n * 64 + g * 16, 0);
      }
      wait_slots<4>(lds, it ? 104u : C::T_GATHER);
      const int go = it == 0 ? (lane < 32 ? W_QG + (h * 8 + (lane >> 2)) * 512 + b * 64 + (lane & 3) * 16 : OOB) : W_QG + (256 + xcd * 32 + (lane >> 2)) * 512 + b * 64 + (lane & 3) * 16;
      const u64 need = it == 0 ? 0xFFFFFFFFull : ~0ull;
#pragma unroll 1
      for (unsigned spn = 0; spn < SPIN_CAP; ++spn) {
        const v4u v = ld_sc1(rs, go, 0), u = ld_sc1(rs, it == 1 ? go : OOB, 16 * 512);
        if (it == 1 || lane < 32) *LDSP(v2u, L_QIMG + (it == 1 ? 256 : 0) + 8 * lane) = v2u{v[0], v[2]};
        if (it == 1) *LDSP(v2u, L_QIMG + 768 + 8 * lane) = v2u{u[0], u[2]};
        if ((__builtin_amdgcn_ballot_w64(v[1] != tag || v[3] != tag || (it == 1 && (u[1] != tag || u[3] != tag))) & need) == 0) break;
        __builtin_amdgcn_s_sleep(24);
      }
      CBAR();
      lds_st(lds, L_QRDY + 4 * it, 1u);
    }
    AN_PUT(1, 112u); AN_GET(1, 128u); AN_PUT(2, 176u); AN_GET(2, 190u);
    lds_wait_ge<16>(lds, L_BCNT, 4u);
    {
      f32x2 ml[4]; f32x4 ac[4][2];
#pragma unroll
      for (int u = 0; u < 4; ++u) { ml[u] = *LDSP(f32x2, L_ML + 128 + u * 32 + g * 8); ac[u][0] = *LDSP(f32x4, L_S + u * 4096 + 32 * lane); ac[u][1] = *LDSP(f32x4, L_S + u * 4096 + 32 * lane + 16); }
      const float M = fmaxf(fmaxf(ml[0][0], ml[1][0]), fmaxf(ml[2][0], ml[3][0]));
      float L = 0.f; f32x4 o0 = {0, 0, 0, 0}, o1 = {0, 0, 0, 0};
#pragma unroll
      for (int u = 0; u < 4; ++u) { const float f = ex2((ml[u][0] - M) * SC2); L = fmaf(f, ml[u][1], L); o0 += f * ac[u][0]; o1 += f * ac[u][1]; }
      const float inv = 1.f / L; o0 *= inv; o1 *= inv;
      st_sc1(v4u{cvtpk(o0[0], o0[1]), cvtpk(o0[2], o0[3]), cvtpk(o1[0], o1[1]), cvtpk(o1[2], o1[3])}, rs, W_PO + wg * 1024 + 16 * lane, 0);
      asm volatile("s_waitcnt vmcnt(0)" ::: "memory");
      st_sc1(v4u{__float_as_uint(M), tag, __float_as_uint(L), tag}, rs, n == 0 ? W_ML + wg * 128 + g * 16 : OOB, 0);
    }
    wait_slots<16>(lds, C::T_ML);
    v4u mlv = {0, 0, 0, 0};
#pragma unroll 1
    for (unsigned spn = 0; spn < SPIN_CAP; ++spn) {
      mlv = ld_sc1(rs, lane < 16 ? W_ML + (xcd + 8 * (4 * b + (lane & 3))) * 128 + (lane >> 2) * 16 : OOB, 0);
      if ((__builtin_amdgcn_ballot_w64(mlv[1] != tag || mlv[3] != tag) & 0xFFFFu) == 0u) break;
      __builtin_amdgcn_s_sleep(64);
    }
    __builtin_amdgcn_fence(__ATOMIC_ACQUIRE, "wavefront");
    {
      v4u p[4];
#pragma unroll
      for (int u = 0; u < 4; ++u) p[u] = ld_sc1(rs, W_PO + (xcd + 32 * b) * 1024 + 16 * lane, u * 8192);
      const float m = __uint_as_float(mlv[0]);
      float M = fmaxf(m, dpp<XOR1>(m)); M = fmaxf(M, dpp<XOR2>(M));
      const float e = ex2((m - M) * SC2) * __uint_as_float(mlv[2]);
      float L = e + dpp<XOR1>(e); L += dpp<XOR2>(L);
      if (lane < 16) *LDSP(float, L_WGT + 4 * lane) = e / L;
#pragma unroll
      for (int u = 0; u < 4; ++u) *LDSP(v4u, L_HREC + g * 1024 + u * 256 + n * 16) = p[u];
      CBAR(); lds_st(lds, L_RCNT, 4u);
    }
    lds_wait_ge<16>(lds, L_YPUB, 4u);
    {
      const f32x2 yb = (*LDSP(f32x2, L_YS + 8 * lane) + *LDSP(f32x2, L_YS + 512 + 8 * lane)) + (*LDSP(f32x2, L_YS + 1024 + 8 * lane) + *LDSP(f32x2, L_YS + 1536 + 8 * lane));
      AN_PUT(3, 244u); AN_GET(3, 258u);
      wait_slots<8>(lds, C::T_YB);
      st_sc1(v4u{__float_as_uint(yb[0]), tag, __float_as_uint(yb[1]), tag}, rs, W_YB + wg * 1024 + (8 * n + 2 * g) * 8, 0);
    }
#pragma unroll 1
    for (int pass = 0; pass < 2; ++pass) {
      if (pass == 0) wait_slots<8>(lds, NSL - 1); else lds_wait_ge<1>(lds, L_YPUB + 4, 4u);
      const int yo = pass == 0 ? W_YB + ((lane >> 3) + 8 * (4 * b + (h >> 3))) * 1024 + (h & 7) * 128 + (lane & 7) * 16
                               : W_YA + (b * 32 * 512 + (lane >> 3) * 512 + 16 * h + 2 * (lane & 7)) * 8;
      v4u y[4]; bool ok[4];
#pragma unroll
      for (int i = 0; i < 4; ++i) { y[i] = v4u{0, 0, 0, 0}; ok[i] = pass == 0 && i > 0; }
#pragma unroll 1
      for (unsigned spn = 0; spn < SPIN_CAP; ++spn) {
        v4u z[4];
#pragma unroll
        for (int i = 0; i < 4; ++i) z[i] = ld_sc1(rs, ok[i] ? OOB : yo, i * 8 * 512 * 8);
        bool all = true;
#pragma unroll
        for (int i = 0; i < 4; ++i) { if (!ok[i]) { y[i] = z[i]; ok[i] = z[i][1] == tag && z[i][3] == tag; } all &= ok[i]; }
        if (__builtin_amdgcn_ballot_w64(!all) == 0) break;
        __builtin_amdgcn_s_sleep(2);
      }
      float s0 = (__uint_as_float(y[0][0]) + __uint_as_float(y[1][0])) + (__uint_as_float(y[2][0]) + __uint_as_float(y[3][0]));
      float s1 = (__uint_as_float(y[0][2]) + __uint_as_float(y[1][2])) + (__uint_as_float(y[2][2]) + __uint_as_float(y[3][2]));
      s0 += dpp<ROR8>(s0); s1 += dpp<ROR8>(s1);
      s0 = x32_sum(x16_sum(s0)); s1 = x32_sum(x16_sum(s1));
      if (lane < 8) *(GAS unsigned*)(out + b * 1024 + (pass == 0 ? 512 : 0) + 16 * h + 2 * lane) = cvtpk(s0, s1);
    }
    if (wg == 0 && lane == 0) __hip_atomic_store((GAS unsigned*)(ws + W_EPOCH), tag, __ATOMIC_RELAXED, __HIP_MEMORY_SCOPE_AGENT);
  }
}

extern "C" void kernel_launch(void* const* d_in, const int* in_sizes, int n_in, void* d_out, int out_size, void* d_ws, size_t ws_size, hipStream_t stream) {
  (void)in_sizes; (void)n_in; (void)out_size; (void)ws_size;
  (void)hipFuncSetAttribute((const void*)fused_decode, hipFuncAttributeMaxDynamicSharedMemorySize, LDS_BYTES);
  hipLaunchKernelGGL(fused_decode, dim3(256), dim3(NTHR), LDS_BYTES, stream,
                     (const u16*)d_in[0], (const u16*)d_in[1], (const u16*)d_in[2], (const u16*)d_in[3], (const u16*)d_in[4],
                     (const u16*)d_in[5], (const u16*)d_in[6], (const u16*)d_in[7], (const u16*)d_in[8], (const u16*)d_in[9],
                     (u16*)d_out, (unsigned char*)d_ws);
}
```

```cpp
#include <hip/hip_runtime.h>
#include <hip/hip_bf16.h>
#include <stdint.h>

typedef unsigned v4u __attribute__((ext_vector_type(4)));
typedef unsigned v2u __attribute__((ext_vector_type(2)));
typedef int v4i __attribute__((ext_vector_type(4)));
typedef float f32x4 __attribute__((ext_vector_type(4)));
typedef float f32x2 __attribute__((ext_vector_type(2)));
typedef short s16x4 __attribute__((ext_vector_type(4)));
typedef __bf16 bf16x8 __attribute__((ext_vector_type(8)));
typedef __bf16 bf16x2 __attribute__((ext_vector_type(2)));
typedef unsigned short u16;
typedef unsigned long long u64;
#define GAS __attribute__((address_space(1)))
#define LAS __attribute__((address_space(3)))

struct C {
  static constexpr int NFL = 20;
  static constexpr int MUL = 11;
  static constexpr int PACE = 119, PACE2 = 112;
  static constexpr int PACE0 = 20, MARGIN = 20;
  static constexpr int DN = 40, U1 = 60, U2 = 110, U3 = 160, U4 = 220;
  static constexpr int T2 = 264, GAIN2 = 3;
  static constexpr int CODE_LINES = 158;
  static constexpr int T_SLICE = 2, T_GATHER = 17, T_ML = 216, T_YB = 314;
  static constexpr int EARLY2 = 1, DRY = 1, RETRY = 8, UNPARK = 1;
};

constexpr int SK = 4096, DH = 128, NH = 32, HKV = 8;
constexpr float SC2 = 0.088388347648318440f * 1.4426950408889634f;
constexpr int NWAVE = 10, NTHR = NWAVE * 64;
constexpr int NSL = 320;
constexpr int PARK = 4;
constexpr int RING = 15, SLOT = 8192;
constexpr int L_FULL = 0  , L_QRDY = 4  , L_BCNT = 12, L_DONE = 16  , L_RCNT = 32  , L_YPUB = 40  ,
              L_PFC = 48, L_MLC = 52, L_IMPL = 56, L_BASE = 60;
constexpr int L_QIMG = 64;
constexpr int L_WGT = L_QIMG + 1280;
constexpr int L_ML = L_WGT + 128;
constexpr int L_HREC = L_ML + 256;
constexpr int L_TAG = 7920;
constexpr int L_JUNK = 7936;
constexpr int L_S = 8192;
constexpr int L_P = L_S + 16384;
constexpr int L_YS = L_P + 8192;
constexpr int L_RING = L_P + 16384;
constexpr int LDS_BYTES = 160 * 1024;
static_assert(L_HREC % 16 == 0 && L_HREC + 5 * 1024 <= 7168 && L_RING + RING * SLOT <= LDS_BYTES, "lds map");
constexpr int W_EPOCH = 0;
constexpr int W_ML = 4096;
constexpr int W_AN = 49152;
constexpr int W_QG = 65536;
constexpr int W_PO = W_QG + 2 * 256 * 512;
constexpr int W_YB = W_PO + 256 * 1024;
constexpr int W_YA = W_YB + 256 * 1024;
constexpr int W_END = W_YA + 8 * 32 * 512 * 8;
constexpr unsigned SPIN_CAP = 1u << 20;
constexpr int OOB = 0x7FFFFF00;

__device__ __forceinline__ unsigned cvtpk(float lo, float hi) { return __builtin_bit_cast(unsigned, __builtin_convertvector(f32x2{lo, hi}, bf16x2)); }
__device__ __forceinline__ __amdgpu_buffer_rsrc_t mk(const void* p, unsigned bytes) { return __builtin_amdgcn_make_buffer_rsrc((void*)p, 0, bytes, 0x00020000); }
__device__ __forceinline__ v4i mksrd(const void* p, unsigned bytes) { return v4i{(int)(uintptr_t)p, (int)((uintptr_t)p >> 32), (int)bytes, 0x00020000}; }
__device__ __forceinline__ f32x4 mfma32(v4u a, v4u b, f32x4 c) { return __builtin_amdgcn_mfma_f32_16x16x32_bf16(__builtin_bit_cast(bf16x8, a), __builtin_bit_cast(bf16x8, b), c, 0, 0, 0); }
__device__ __forceinline__ f32x4 mfma16(v2u a, v2u b, f32x4 c) { return __builtin_amdgcn_mfma_f32_16x16x16bf16_1k(__builtin_bit_cast(s16x4, a), __builtin_bit_cast(s16x4, b), c, 0, 0, 0); }
__device__ __forceinline__ float ex2(float x) { return __builtin_amdgcn_exp2f(x); }
__device__ __forceinline__ v4u ld128(__amdgpu_buffer_rsrc_t r, int vo, int so) { return __builtin_amdgcn_raw_buffer_load_b128(r, vo, so, 0); }
__device__ __forceinline__ v4u ld_sc1(__amdgpu_buffer_rsrc_t r, int vo, int so) { return __builtin_amdgcn_raw_buffer_load_b128(r, vo, so, 16); }
__device__ __forceinline__ void st_sc1(v4u v, __amdgpu_buffer_rsrc_t r, int vo, int so) { __builtin_amdgcn_raw_buffer_store_b128(v, r, vo, so, 16); }
template <int CTRL> __device__ __forceinline__ float dpp(float x) { return __builtin_bit_cast(float, __builtin_amdgcn_mov_dpp(__builtin_bit_cast(int, x), CTRL, 0xf, 0xf, true)); }
constexpr int XOR1 = 0xB1, XOR2 = 0x4E, HMIR = 0x141, ROR8 = 0x128;
__device__ __forceinline__ float x16_sum(float x) { auto s = __builtin_amdgcn_permlane16_swap(__float_as_uint(x), __float_as_uint(x), false, false); return __uint_as_float(s[0]) + __uint_as_float(s[1]); }
__device__ __forceinline__ float x32_sum(float x) { auto s = __builtin_amdgcn_permlane32_swap(__float_as_uint(x), __float_as_uint(x), false, false); return __uint_as_float(s[0]) + __uint_as_float(s[1]); }
__device__ __forceinline__ float xg_max(float x) {
  auto s = __builtin_amdgcn_permlane16_swap(__float_as_uint(x), __float_as_uint(x), false, false);
  x = fmaxf(__uint_as_float(s[0]), __uint_as_float(s[1]));
  auto t = __builtin_amdgcn_permlane32_swap(__float_as_uint(x), __float_as_uint(x), false, false);
  return fmaxf(__uint_as_float(t[0]), __uint_as_float(t[1]));
}
__device__ __forceinline__ int fresh_lane() {
  int l; asm volatile("v_mbcnt_lo_u32_b32 %0, -1, 0\n\tv_mbcnt_hi_u32_b32 %0, -1, %0" : "=v"(l)); return l;
}
#define CBAR() asm volatile("" ::: "memory")
#define LDSP(T, off) ((LAS T*)(lds + (off)))
__device__ __forceinline__ unsigned lds_ld(LAS unsigned char* lds, int off) { return __builtin_amdgcn_readfirstlane(*(volatile LAS unsigned*)(lds + off)); }
__device__ __forceinline__ void lds_st(LAS unsigned char* lds, int off, unsigned v) { *(volatile LAS unsigned*)(lds + off) = v; }
template <int SLP> __device__ __forceinline__ unsigned lds_wait_ge(LAS unsigned char* lds, int off, unsigned want) {
  unsigned v = lds_ld(lds, off);
#pragma unroll 1
  for (unsigned sp = 0; v < want && sp < SPIN_CAP * 32 / (SLP + 1); ++sp) { if (SLP) __builtin_amdgcn_s_sleep(SLP); v = lds_ld(lds, off); }
  CBAR();
  return v;
}
template <int SLP> __device__ __forceinline__ void wait_slots(LAS unsigned char* lds, unsigned slots) { lds_wait_ge<SLP>(lds, L_FULL, 2 * slots); }
__device__ __forceinline__ void lds_inc(LAS unsigned char* lds, int off, int lane) {
  CBAR();
  if (lane == 0) __hip_atomic_fetch_add(LDSP(unsigned, off), 1u, __ATOMIC_RELAXED, __HIP_MEMORY_SCOPE_WORKGROUP);
  CBAR();
}

template <int N> __device__ __forceinline__ void dma4(unsigned m0v, unsigned v0, unsigned v1, unsigned v2, unsigned v3, v4i srd, unsigned so) {
  asm volatile("s_mov_b32 m0, %0\n\t"
               "s_waitcnt vmcnt(%7)\n\tbuffer_load_dwordx4 %1, %5, %6 offen nt lds\n\t"
               "s_waitcnt vmcnt(%7)\n\tbuffer_load_dwordx4 %2, %5, %6 offen offset:1024 nt lds\n\t"
               "s_waitcnt vmcnt(%7)\n\tbuffer_load_dwordx4 %3, %5, %6 offen offset:2048 nt lds\n\t"
               "s_waitcnt vmcnt(%7)\n\tbuffer_load_dwordx4 %4, %5, %6 offen offset:3072 nt lds"
               :: "s"(m0v), "v"(v0), "v"(v1), "v"(v2), "v"(v3), "s"(srd), "s"(so), "i"(N - 1) : "memory");
}

__global__ void __launch_bounds__(NTHR, 3) fused_decode(const u16* xa, const u16* ak, const u16* av, const u16* wqa, const u16* woa,
                                                        const u16* xb, const u16* bk, const u16* bv, const u16* wqb, const u16* wob,
                                                        u16* out, unsigned char* ws) {
  constexpr int NFL = C::NFL;
  {
    u64 pc; unsigned tv;
    asm volatile("s_getpc_b64 %0\n\tv_min_u32 %1, %3, %2\n\ts_movk_i32 m0, %4\n\tv_lshlrev_b32 %1, 7, %1\n\tglobal_load_lds_dword %1, %0"
                 : "=&s"(pc), "=&v"(tv) : "v"(threadIdx.x), "i"(C::CODE_LINES - 1), "i"(L_JUNK));
    __builtin_amdgcn_sched_barrier(0);
  }
  extern __shared__ __attribute__((aligned(16))) unsigned char lds_raw[];
  LAS unsigned char* lds = (LAS unsigned char*)lds_raw;
  const int wg = blockIdx.x, lane = threadIdx.x & 63, wave = __builtin_amdgcn_readfirstlane(threadIdx.x >> 6);
  const int xcd = wg & 7, jw = wg >> 3, b = jw >> 2, sp = jw & 3, h = 4 * xcd + sp;
  const int g = lane >> 4, n = lane & 15;
  *(volatile LAS unsigned*)(lds + 4 * n) = 0u;
  asm volatile("s_waitcnt lgkmcnt(0)" :: "s"(wqb), "s"(wob), "s"(out), "s"(ws), "s"(xa) : "memory");
  __builtin_amdgcn_s_barrier();
  const unsigned k0 = g * 256 + ((n ^ g) * 16);
#define SLOT_OFF(r_) (((((r_) & 0x70u) << 1) | ((r_) & 15u)) << 12)
  {
#define E4(m_, so_) "s_mov_b32 m0, " m_ "\n\ts_mov_b32 %0, " so_ "\n\t" \
               "buffer_load_dwordx4 %2, %6, %0 offen nt lds\n\tbuffer_load_dwordx4 %3, %6, %0 offen offset:1024 nt lds\n\t" \
               "buffer_load_dwordx4 %4, %6, %0 offen offset:2048 nt lds\n\tbuffer_load_dwordx4 %5, %6, %0 offen offset:3072 nt lds\n\t"
    unsigned tmp;
    asm volatile("s_cmp_lg_u32 %1, 0\n\ts_cbranch_scc1 1f\n\t"
                 E4("%7", "0") E4("%7+4096", "65536") E4("%7+8192", "%8") E4("%7+12288", "%8+65536") E4("%7+16384", "%9") E4("%7+20480", "%9+65536")
                 "1:"
                 : "=&s"(tmp) : "s"(wave), "v"(k0), "v"(k0 ^ 64), "v"(k0 ^ 128), "v"(k0 ^ 192), "s"(mksrd(ak + (size_t)(b * NH + h) * SK * DH, SK * 256)),
                   "i"(L_RING), "i"(SLOT_OFF(C::MUL)), "i"(SLOT_OFF(2 * C::MUL)) : "memory", "scc");
  }
  if (C::EARLY2) {
    unsigned tmp;
    asm volatile("s_cmp_lt_u32 %1, 6\n\ts_cbranch_scc1 1f\n\t"
                 "s_mov_b32 m0, %4\n\ts_mov_b32 %0, 0\n\tbuffer_load_dwordx4 %2, %3, %0 offen lds\n\t"
                 "s_mov_b32 %0, 65536\n\tbuffer_load_dwordx4 %2, %3, %0 offen lds\n\t"
                 "s_mov_b32 %0, 32768\n\tbuffer_load_dwordx4 %2, %3, %0 offen lds\n\t"
                 "s_mov_b32 %0, 98304\n\tbuffer_load_dwordx4 %2, %3, %0 offen lds\n\t"
                 "1:"
                 : "=&s"(tmp) : "s"(wave), "v"(lane * 16), "s"(mksrd(wqa + (size_t)(16 * jw + ((wave + 2) & 3)) * 4096 + xcd * 512, 13 * 8192)),
                   "i"(L_HREC) : "memory", "scc");
  }

  if (wave == 0) {
    __builtin_amdgcn_s_setprio(3);
    const size_t ua = (size_t)(b * NH + h) * SK * DH, ub = (size_t)(b * HKV + xcd) * SK * DH;
    const int ll = fresh_lane();
    const unsigned vv = ll * 16, k0 = (ll >> 4) * 256 + (((ll & 15) ^ (ll >> 4)) * 16);
    unsigned slot = L_RING + 3 * SLOT;
    int limit = RING;
    u64 now = __builtin_amdgcn_s_memrealtime();
    unsigned base = (unsigned)now + C::PACE0, due = -C::PACE;
    now = base + due;
    int t = 3;
#pragma unroll 1
    for (int ph = 0; ph < 4; ++ph) {
      const v4i rs = mksrd(ph == 0 ? ak + ua : ph == 1 ? bk + ub : ph == 2 ? bv + ub : av + ua, SK * 256);
      const int tend = ph == 0 ? 128 : ph == 1 ? 160 : ph == 2 ? 192 : NSL;
      const unsigned v0 = ph < 2 ? k0 : vv, v1 = ph < 2 ? k0 ^ 64 : vv, v2 = ph < 2 ? k0 ^ 128 : vv, v3 = ph < 2 ? k0 ^ 192 : vv;
      unsigned r = (ph == 0 ? 3 * C::MUL : ph == 3 ? 0 : sp * 32 * C::MUL) & 127u;
#pragma unroll 1
      while (t < tend) {
        if (t + 3 >= limit) {
#pragma unroll 1
          for (unsigned spn = 0; spn < SPIN_CAP * 16; ++spn) {
            const v4u d = *(volatile LAS v4u*)(lds + L_DONE);
            const unsigned nb = lds_ld(lds, L_BASE); base = nb ? nb : base;
            const int c = min(min(4 * (int)d[0], 4 * (int)d[1] + 1), min(4 * (int)d[2] + 2, 4 * (int)d[3] + 3));
            limit = __builtin_amdgcn_readfirstlane(c) + RING;
            if (t + 3 < limit) break;
            __builtin_amdgcn_s_sleep(1);
          }
        }
#define RUN4(N_) do { static_assert(((N_) + 3) / 4 >= 5 && ((N_) + 3) / 4 <= 7, "the published count never goes down"); \
        _Pragma("unroll 1") for (const int te = (t | 3) + 1; t < te; ++t) { \
          const unsigned so = SLOT_OFF(r); \
          dma4<N_>(slot, v0, v1, v2, v3, rs, so); \
          dma4<N_>(slot + 4096, v0, v1, v2, v3, rs, so + 65536u); \
          asm volatile("s_waitcnt lgkmcnt(0)");     \
          lds_st(lds, L_FULL, (unsigned)(2 * t + 2 - ((N_) + 3) / 4)); \
          slot += SLOT; if (slot >= L_RING + RING * SLOT) slot = L_RING; \
          r = (r + C::MUL) & 127u; } } while (0)
        asm volatile("" : "+s"(now));
        lds_st(lds, L_IMPL, (unsigned)now - due);
        const int lagr = (int)((unsigned)now - due - base), lag = t >= C::T2 ? lagr * C::GAIN2 : lagr;
        due += ph == 0 ? C::PACE : C::PACE2;
        asm volatile("s_memrealtime %0" : "=s"(now));
        if (lag < C::U1) { if (lag >= -C::DN) RUN4(NFL); else RUN4(NFL - 1); }
        else if (lagr < C::U3) { if (lag < C::U2) RUN4(NFL + 1); else RUN4(NFL + 2); }
        else if (lagr < C::U4) RUN4(NFL + 3); else RUN4(NFL + 5);
      }
    }
    asm volatile("s_waitcnt vmcnt(16)" ::: "memory"); lds_st(lds, L_FULL, 2 * NSL - 4);
    asm volatile("s_waitcnt vmcnt(12)" ::: "memory"); lds_st(lds, L_FULL, 2 * NSL - 3);
    asm volatile("s_waitcnt vmcnt(8)" ::: "memory");  lds_st(lds, L_FULL, 2 * NSL - 2);
    asm volatile("s_waitcnt vmcnt(4)" ::: "memory");  lds_st(lds, L_FULL, 2 * NSL - 1);
    asm volatile("s_waitcnt vmcnt(0)" ::: "memory");  lds_st(lds, L_FULL, 2 * NSL);
  } else if (wave >= 2 && wave < 6) {
    const int w = wave - 2, jj = n >> 2;
    int slot = L_RING + w * SLOT; unsigned full = 0, cnt = 0; int t = w;
#define HALF_WAIT(k_, slp_) do { if (full < (unsigned)(2 * t + (k_))) full = lds_wait_ge<slp_>(lds, L_FULL, (unsigned)(2 * t + (k_))); CBAR(); } while (0)
#define SLOT_DONE() do { CBAR(); ++cnt; lds_st(lds, L_DONE + 4 * w, cnt);     \
        CBAR(); slot += 4 * SLOT; if (slot >= L_RING + RING * SLOT) slot -= RING * SLOT; t += 4; } while (0)
#define K_READ(T_) do { HALF_WAIT(2, 1); _Pragma("unroll") for (int m_ = 0; m_ < 8; ++m_) T_[m_] = *LDSP(v4u, ((slot + kb) ^ (64 * (m_ & 3))) + (m_ >> 2) * 4096); SLOT_DONE(); } while (0)
    const int sw = L_S + w * 4096;
    {
      const int kb = n * 256 + ((g ^ n) * 16);
      v4u q[4];
      float mx = -3.0e38f;
      const int sa0 = sw + ((n & 3) * 8 + g * 2 + (jj & 1)) * 16;
      int sa = sa0;
#define K_MATH(T_, act_, step_) do { f32x4 s0 = {0.f, 0.f, 0.f, 0.f}, s1 = {0.f, 0.f, 0.f, 0.f}; \
        _Pragma("unroll") for (int m = 0; m < 4; ++m) { s0 = mfma32(T_[m], q[m], s0); s1 = mfma32(T_[4 + m], q[m], s1); }       \
        mx = fmaxf(fmaxf(fmaxf(fmaxf(s0[0], s0[1]), fmaxf(s0[2], s0[3])), fmaxf(fmaxf(s1[0], s1[1]), fmaxf(s1[2], s1[3]))), mx); \
        if (act_) *LDSP(f32x4, sa) = (jj & 1) ? s1 : s0; \
        sa += (step_); } while (0)
      {
        v4u P[PARK][8];
#pragma unroll
        for (int i = 0; i < PARK; ++i) K_READ(P[i]);
        lds_wait_ge<1>(lds, L_QRDY, 1u);
#pragma unroll
        for (int m = 0; m < 4; ++m) q[m] = *LDSP(v4u, L_QIMG + g * 16 + 64 * m);
#pragma unroll
        for (int i = 0; i < PARK; ++i) { K_MATH(P[i], (n & 11) == 0, 128); if (C::UNPARK && i < 2) K_READ(P[i]); }
        if (C::UNPARK) { K_MATH(P[0], (n & 11) == 0, 128); K_MATH(P[1], (n & 11) == 0, 128); }
      }
#pragma unroll 1
      for (int ph = 0; ph < 2; ++ph) {
        const bool act = ph == 1 ? n < 8 : (n & 11) == 0;
        const int sstep = ph == 1 ? 512 : 128, tend = ph == 0 ? 128 : 160;
#pragma unroll 1
        while (t < tend) { v4u T[8]; K_READ(T); K_MATH(T, act, sstep); }
        lds_wait_ge<1>(lds, L_QRDY + 4, 1u);
        const float Mh = xg_max(mx);
        const float M = __builtin_bit_cast(float, __builtin_amdgcn_ds_bpermute(((lane >> 3) & 3) * 4, __builtin_bit_cast(int, Mh)));
        const float mc = -M * SC2;
        float l = 0.f;
        CBAR();
#pragma unroll
        for (int j = 0; j < 4; ++j) {
          const f32x4 s = *LDSP(f32x4, sw + 16 * lane + 1024 * j);
          const float p0 = ex2(fmaf(s[0], SC2, mc)), p1 = ex2(fmaf(s[1], SC2, mc)), p2 = ex2(fmaf(s[2], SC2, mc)), p3 = ex2(fmaf(s[3], SC2, mc));
          l += (p0 + p1) + (p2 + p3);
          *LDSP(v2u, L_P + ph * 8192 + w * 2048 + 8 * lane + 512 * j) = v2u{cvtpk(p0, p1), cvtpk(p2, p3)};
        }
        l += dpp<XOR1>(l); l += dpp<XOR2>(l); l += dpp<HMIR>(l); l = x32_sum(l);
        const float la = x16_sum(l + dpp<ROR8>(l));
        if ((lane & 39) == 0) *LDSP(f32x2, L_ML + ph * 128 + w * 32 + lane) = f32x2{M, ph == 1 ? l : la};
        lds_inc(lds, L_MLC, lane);
        mx = -3.0e38f; sa = sa0;
#pragma unroll
        for (int m = 0; m < 4; ++m) q[m] = *LDSP(v4u, L_QIMG + 256 + (n & 3) * 256 + g * 16 + 64 * m);
      }
    }
    {
      const unsigned Z = 0x0C0C0C0Cu;
      const unsigned el0 = jj == 0 ? 0x0C0C0100u : jj == 1 ? 0x01000C0Cu : Z, el1 = jj == 2 ? 0x0C0C0100u : jj == 3 ? 0x01000C0Cu : Z;
      const unsigned eh0 = jj == 0 ? 0x0C0C0302u : jj == 1 ? 0x03020C0Cu : Z, eh1 = jj == 2 ? 0x0C0C0302u : jj == 3 ? 0x03020C0Cu : Z;
      const int vb = g * 1024 + n * 16;
#pragma unroll 1
      for (int ph = 1; ph >= 0; --ph) {
        if (ph == 0) {
          lds_wait_ge<1>(lds, L_MLC, 8u);
          f32x2 ml[4];
#pragma unroll
          for (int u = 0; u < 4; ++u) ml[u] = *LDSP(f32x2, L_ML + u * 32);
          const float M = fmaxf(fmaxf(ml[0][0], ml[1][0]), fmaxf(ml[2][0], ml[3][0]));
          float L = 0.f;
#pragma unroll
          for (int u = 0; u < 4; ++u) L = fmaf(ex2((ml[u][0] - M) * SC2), ml[u][1], L);
          *LDSP(float, L_WGT + 64 + 4 * w) = ex2(((*LDSP(f32x2, L_ML + w * 32))[0] - M) * SC2) / L;
        }
        const int tend = ph == 1 ? 192 : NSL, pstep = ph == 1 ? 256 : 64;
        int pa = L_P + ph * 8192 + w * 2048 + (ph == 1 ? (n & 3) * 64 : 0) + g * 16;
        f32x4 a0 = {0, 0, 0, 0}, a1 = {0, 0, 0, 0};
#pragma unroll 1
        while (t < tend) {
          const v4u pp = *LDSP(v4u, pa); pa += pstep;
          v2u bf[8];
#pragma unroll
          for (int i = 0; i < 8; ++i) { const unsigned pk = pp[i >> 1]; bf[i] = v2u{__builtin_amdgcn_perm(pk, pk, (i & 1) ? eh0 : el0), __builtin_amdgcn_perm(pk, pk, (i & 1) ? eh1 : el1)}; }
#pragma unroll
          for (int i = 0; i < 8; ++i) asm volatile("" : "+v"(bf[i]));
          v4u T[8];
          HALF_WAIT(1, 1);
#pragma unroll
          for (int i = 0; i < 4; ++i) T[i] = *LDSP(v4u, slot + vb + i * 256);
#pragma unroll
          for (int i = 0; i < 4; ++i) { a0 = mfma16(v2u{T[i][0], T[i][1]}, bf[i], a0); a1 = mfma16(v2u{T[i][2], T[i][3]}, bf[i], a1); }
          asm volatile("" : "+v"(a0), "+v"(a1));
          HALF_WAIT(2, 0);
#pragma unroll
          for (int i = 4; i < 8; ++i) T[i] = *LDSP(v4u, slot + vb + 4096 + (i & 3) * 256);
          SLOT_DONE();
#pragma unroll
          for (int i = 4; i < 8; ++i) { a0 = mfma16(v2u{T[i][0], T[i][1]}, bf[i], a0); a1 = mfma16(v2u{T[i][2], T[i][3]}, bf[i], a1); }
        }
        if ((n & 3) == 0) *LDSP(v4u, L_HREC + 4096 + w * 256 + jj * 64 + g * 16) = v4u{cvtpk(a0[0], a1[0]), cvtpk(a0[1], a1[1]), cvtpk(a0[2], a1[2]), cvtpk(a0[3], a1[3])};
        if (ph == 1) {
          const int ro = sw + (n & 3) * 512 + jj * 128 + g * 32;
          *LDSP(f32x4, ro) = f32x4{a0[0], a1[0], a0[1], a1[1]};
          *LDSP(f32x4, ro + 16) = f32x4{a0[2], a1[2], a0[3], a1[3]};
        }
        lds_inc(lds, ph == 1 ? L_BCNT : L_RCNT + 4, lane);
      }
    }
  } else if (wave >= 6) {
    const int w = wave - 6;
    if (C::EARLY2) { asm volatile("s_waitcnt vmcnt(0)" ::: "memory"); lds_inc(lds, L_PFC, lane); }
#pragma unroll 1
    for (int mat = C::EARLY2; mat < 2; ++mat) {
      if (mat) wait_slots<16>(lds, 32u);
      const __amdgpu_buffer_rsrc_t rw = mk((mat ? wqb : wqa) + (size_t)(16 * jw + w) * 4096 + xcd * 512, 13 * 8192);
      v4u d[4];
#pragma unroll
      for (int i = 0; i < 4; ++i) d[i] = ld128(rw, lane * 16, ((i & 1) * 8 + (i >> 1) * 4) * 8192);
#pragma unroll
      for (int i = 0; i < 4; ++i) asm volatile("" :: "v"(d[i]));
    }
    const __amdgpu_buffer_rsrc_t ry = mk(ws + W_YA, W_END - W_YA);
    const int yo = ((b * 32 + h) * 512 + 128 * w + 8 * n + 2 * g) * 8;
    wait_slots<16>(lds, 72u);
#pragma unroll 1
    for (int pass = 0; pass < 2; ++pass) {
      const __amdgpu_buffer_rsrc_t rw = mk((pass == 0 ? wob : woa) + (size_t)xcd * 512 * 512, 512 * 1024);
      {
        v4u d[4];
#pragma unroll
        for (int i = 0; i < 4; ++i) d[i] = ld128(rw, (2 * b + (i >> 1) + 16 * (i & 1)) * 4096 + lane * 16, sp * 131072 + w * 1024);
#pragma unroll
        for (int i = 0; i < 4; ++i) asm volatile("" :: "v"(d[i]));
        lds_wait_ge<16>(lds, L_FULL, min(lds_ld(lds, L_FULL) + 24u, 2u * NSL));
      }
      const int wv = (pass == 0 ? w * 131072 + sp * 256 : sp * 131072 + w * 256) + g * 32768 + n * 16;
      v4u F[4][8];
#pragma unroll
      for (int s = 0; s < 4; ++s) {
#pragma unroll
        for (int jp = 0; jp < 4; ++jp) {
          const v4u w0 = ld128(rw, wv, (8 * jp + s) * 1024), w1 = ld128(rw, wv, (8 * jp + 4 + s) * 1024);
#pragma unroll
          for (int d = 0; d < 4; ++d) {
            unsigned lo = __builtin_amdgcn_perm(w1[d], w0[d], 0x05040100u), hi = __builtin_amdgcn_perm(w1[d], w0[d], 0x07060302u);
            asm volatile("" : "+v"(lo), "+v"(hi));
            F[s][2 * d][jp] = lo; F[s][2 * d + 1][jp] = hi;
          }
          __builtin_amdgcn_sched_barrier(0);
        }
#pragma unroll
        for (int c = 0; c < 8; ++c) asm volatile("" : "+v"(F[s][c]));
      }
      const int hr = pass == 0 ? w : 4;
      lds_wait_ge<4>(lds, L_RCNT + 4 * pass, 2u);
      lds_wait_ge<0>(lds, L_RCNT + 4 * pass, 4u);
      v4u of[4];
#pragma unroll
      for (int s = 0; s < 4; ++s) of[s] = *LDSP(v4u, L_HREC + hr * 1024 + (n & 3) * 256 + s * 64 + g * 16);
      const unsigned tag = *LDSP(unsigned, L_TAG);
      const f32x4 wt = *LDSP(f32x4, L_WGT + hr * 16);
      f32x4 da = {0, 0, 0, 0}, dc = {0, 0, 0, 0};
      __builtin_amdgcn_sched_barrier(0);
#pragma unroll
      for (int cp = 0; cp < 4; ++cp) {
        f32x4 d0 = {0, 0, 0, 0}, d1 = {0, 0, 0, 0};
#pragma unroll
        for (int s = 0; s < 4; ++s) { d0 = mfma32(of[s], F[s][2 * cp], d0); d1 = mfma32(of[s], F[s][2 * cp + 1], d1); }
#pragma unroll
        for (int r = 0; r < 4; ++r) { da[r] = (cp == 0 || g == cp) ? d0[r] : da[r]; dc[r] = (cp == 0 || g == cp) ? d1[r] : dc[r]; }
      }
#pragma unroll
      for (int i = 0; i < 18; ++i) __builtin_amdgcn_sched_group_barrier(0x8, 1, 0);
#pragma unroll
      for (int i = 0; i < 14; ++i) { __builtin_amdgcn_sched_group_barrier(0x8, 1, 0); __builtin_amdgcn_sched_group_barrier(0x2, 1, 0); }
      const float ya = fmaf(wt[0], da[0], wt[1] * da[1]) + fmaf(wt[2], da[2], wt[3] * da[3]), yc = fmaf(wt[0], dc[0], wt[1] * dc[1]) + fmaf(wt[2], dc[2], wt[3] * dc[3]);
      st_sc1(v4u{__float_as_uint(ya), tag, __float_as_uint(yc), tag}, ry, pass == 0 ? OOB : yo, 0);
      if (pass == 0) *LDSP(f32x2, L_YS + w * 512 + 8 * lane) = f32x2{ya, yc};
      lds_inc(lds, L_YPUB + 4 * pass, lane);
    }
  } else {
    unsigned epoch, tag = 0;
    asm volatile("global_load_dword %0, %1, %2 sc1" : "=v"(epoch) : "v"(W_EPOCH), "s"(ws) : "memory");
#define TAKE_TAG() do { asm volatile("s_waitcnt vmcnt(0)\n\tv_readfirstlane_b32 %0, %1\n\ts_nop 1" : "=s"(tag) : "v"(epoch) : "memory"); tag += 1u; } while (0)
    const __amdgpu_buffer_rsrc_t rs = mk(ws, W_END);
#define AN_PUT(k_, at_) do { if (sp == 0) { wait_slots<16>(lds, at_); const unsigned im = lds_ld(lds, L_IMPL); \
      st_sc1(v4u{im, tag, im, tag}, rs, lane == 0 ? W_AN + (k_) * 1024 + (b * 8 + xcd) * 16 : OOB, 0); } } while (0)
#define AN_GET(k_, at_) do { { wait_slots<16>(lds, at_); const unsigned im = lds_ld(lds, L_IMPL); \
      const v4u a = ld_sc1(rs, W_AN + (k_) * 1024 + lane * 16, 0); \
      const bool okv = a[1] == tag && a[3] == tag; const int cntv = __builtin_popcountll(__builtin_amdgcn_ballot_w64(okv)); \
      float dv = okv ? (float)(int)(a[0] - im) : 0.f; \
      dv += dpp<XOR1>(dv); dv += dpp<XOR2>(dv); dv += dpp<HMIR>(dv); dv += dpp<ROR8>(dv); dv = x32_sum(x16_sum(dv)); \
      if (cntv >= 32) lds_st(lds, L_BASE, im + (int)(dv / (float)cntv) + C::MARGIN); } } while (0)
    int it = C::DRY ? -1 : 0; asm volatile("" : "+s"(it));
#pragma unroll 1
    for (; it < 2; ++it) {
      if (it == 1) { AN_PUT(0, 30u); AN_GET(0, 46u); }
      if ((jw >> 4) == max(it, 0)) {
        const int cb = xcd * 32 + 2 * (jw & 15);
        if (it == 1) wait_slots<16>(lds, 52u);
        const __amdgpu_buffer_rsrc_t rx = mk(it == 1 ? xb : xa, 8 * 512 * 2);
        v4u xf[16];
#pragma unroll
        for (int s = 0; s < 16; ++s) xf[s] = v4u{0, 0, 0, 0};
        if (it == 0) { if (C::EARLY2) lds_wait_ge<1>(lds, L_PFC, 4u); wait_slots<1>(lds, C::T_SLICE); }
        const v4i rw = mksrd((it == 1 ? wqb : wqa) + cb * 16, 512 * 8192 - cb * 32);
        const unsigned vo = (lane >> 2) * 8192 + (lane & 3) * 16;
        f32x4 d0 = {0, 0, 0, 0}, d1 = {0, 0, 0, 0};
#pragma unroll
        for (int hf = 0; hf < 2; ++hf) {
          if (it == 1 || (it == 0 && hf == 0)) {
#pragma unroll 1
            for (int i = 0; i < (it ? 16 : 32); ++i) {
              asm volatile("s_mov_b32 m0, %0\n\ts_nop 0\n\tbuffer_load_dwordx4 %1, %2, %3 offen nt lds" :: "s"((it ? L_P : L_S) + i * 1024), "v"(vo), "s"(rw), "s"((hf * 16 + i) * 16 * 8192) : "memory");
              if (it) __builtin_amdgcn_s_sleep(6);
            }
            if (hf == 0) {
#pragma unroll
              for (int s = 0; s < 16; ++s) xf[s] = ld128(rx, n * 1024 + 16 * g + 64 * s, 0);
            }
            TAKE_TAG();
#pragma unroll
            for (int s = 0; s < 16; ++s) asm volatile("" : "+v"(xf[s]));
          }
          const int ta = ((it == 1 || hf) ? L_P : L_S) + (8 * g + (n >> 2)) * 64 + (n & 3) * 8;
          v2u t[8][4];
#pragma unroll
          for (int s = 0; s < 8; ++s)
#pragma unroll
            for (int u = 0; u < 4; ++u) t[s][u] = __builtin_bit_cast(v2u, __builtin_amdgcn_ds_read_tr16_b64_v4i16(LDSP(s16x4, ta + s * 2048 + (u & 1) * 256 + (u >> 1) * 32)));
          __builtin_amdgcn_sched_barrier(0);
#pragma unroll
          for (int s = 0; s < 8; ++s) {
            d0 = mfma32(v4u{t[s][0][0], t[s][0][1], t[s][1][0], t[s][1][1]}, xf[8 * hf + s], d0);
            d1 = mfma32(v4u{t[s][2][0], t[s][2][1], t[s][3][0], t[s][3][1]}, xf[8 * hf + s], d1);
          }
          asm volatile("" : "+v"(d0), "+v"(d1) :: "memory");
        }
        d1 = f32x4{dpp<ROR8>(d1[0]), dpp<ROR8>(d1[1]), dpp<ROR8>(d1[2]), dpp<ROR8>(d1[3])};
        const f32x4 dd = n < 8 ? d0 : d1;
        st_sc1(v4u{cvtpk(dd[0], dd[1]), tag, cvtpk(dd[2], dd[3]), tag}, rs, it >= 0 ? W_QG + (it * 256 + cb) * 512 + n * 64 + g * 16 : OOB, 0);
      }
      if (it < 0) continue;
      wait_slots<4>(lds, it ? 104u : C::T_GATHER);
      TAKE_TAG();
      const int go = it == 0 ? (lane < 32 ? W_QG + (h * 8 + (lane >> 2)) * 512 + b * 64 + (lane & 3) * 16 : OOB) : W_QG + (256 + xcd * 32 + (lane >> 2)) * 512 + b * 64 + (lane & 3) * 16;
      const u64 need = it == 0 ? 0xFFFFFFFFull : ~0ull;
#pragma unroll 1
      for (unsigned spn = 0; spn < SPIN_CAP; ++spn) {
        const v4u v = ld_sc1(rs, go, 0), u = ld_sc1(rs, it == 1 ? go : OOB, 16 * 512);
        if (it == 1 || lane < 32) *LDSP(v2u, L_QIMG + (it == 1 ? 256 : 0) + 8 * lane) = v2u{v[0], v[2]};
        if (it == 1) *LDSP(v2u, L_QIMG + 768 + 8 * lane) = v2u{u[0], u[2]};
        if ((__builtin_amdgcn_ballot_w64(v[1] != tag || v[3] != tag || (it == 1 && (u[1] != tag || u[3] != tag))) & need) == 0) break;
        __builtin_amdgcn_s_sleep(C::RETRY);
      }
      CBAR();
      lds_st(lds, L_QRDY + 4 * it, 1u);
    }
    lds_st(lds, L_TAG, tag);
    AN_PUT(1, 112u); AN_GET(1, 128u); AN_PUT(2, 176u); AN_GET(2, 190u);
    lds_wait_ge<16>(lds, L_BCNT, 4u);
    {
      f32x2 ml[4]; f32x4 ac[4][2];
#pragma unroll
      for (int u = 0; u < 4; ++u) { ml[u] = *LDSP(f32x2, L_ML + 128 + u * 32 + g * 8); ac[u][0] = *LDSP(f32x4, L_S + u * 4096 + 32 * lane); ac[u][1] = *LDSP(f32x4, L_S + u * 4096 + 32 * lane + 16); }
      const float M = fmaxf(fmaxf(ml[0][0], ml[1][0]), fmaxf(ml[2][0], ml[3][0]));
      float L = 0.f; f32x4 o0 = {0, 0, 0, 0}, o1 = {0, 0, 0, 0};
#pragma unroll
      for (int u = 0; u < 4; ++u) { const float f = ex2((ml[u][0] - M) * SC2); L = fmaf(f, ml[u][1], L); o0 += f * ac[u][0]; o1 += f * ac[u][1]; }
      const float inv = 1.f / L; o0 *= inv; o1 *= inv;
      st_sc1(v4u{cvtpk(o0[0], o0[1]), cvtpk(o0[2], o0[3]), cvtpk(o1[0], o1[1]), cvtpk(o1[2], o1[3])}, rs, W_PO + wg * 1024 + 16 * lane, 0);
      asm volatile("s_waitcnt vmcnt(0)" ::: "memory");
      st_sc1(v4u{__float_as_uint(M), tag, __float_as_uint(L), tag}, rs, n == 0 ? W_ML + wg * 128 + g * 16 : OOB, 0);
    }
    wait_slots<16>(lds, C::T_ML);
    if (C::EARLY2) lds_wait_ge<1>(lds, L_PFC, 4u);
    v4u mlv = {0, 0, 0, 0};
#pragma unroll 1
    for (unsigned spn = 0; spn < SPIN_CAP; ++spn) {
      mlv = ld_sc1(rs, lane < 16 ? W_ML + (xcd + 8 * (4 * b + (lane & 3))) * 128 + (lane >> 2) * 16 : OOB, 0);
      if ((__builtin_amdgcn_ballot_w64(mlv[1] != tag || mlv[3] != tag) & 0xFFFFu) == 0u) break;
      __builtin_amdgcn_s_sleep(64);
    }
    __builtin_amdgcn_fence(__ATOMIC_ACQUIRE, "wavefront");
    {
      v4u p[4];
#pragma unroll
      for (int u = 0; u < 4; ++u) p[u] = ld_sc1(rs, W_PO + (xcd + 32 * b) * 1024 + 16 * lane, u * 8192);
      const float m = __uint_as_float(mlv[0]);
      float M = fmaxf(m, dpp<XOR1>(m)); M = fmaxf(M, dpp<XOR2>(M));
      const float e = ex2((m - M) * SC2) * __uint_as_float(mlv[2]);
      float L = e + dpp<XOR1>(e); L += dpp<XOR2>(L);
      if (lane < 16) *LDSP(float, L_WGT + 4 * lane) = e / L;
#pragma unroll
      for (int u = 0; u < 4; ++u) *LDSP(v4u, L_HREC + g * 1024 + u * 256 + n * 16) = p[u];
      CBAR(); lds_st(lds, L_RCNT, 4u);
    }
    lds_wait_ge<16>(lds, L_YPUB, 4u);
    {
      const f32x2 yb = (*LDSP(f32x2, L_YS + 8 * lane) + *LDSP(f32x2, L_YS + 512 + 8 * lane)) + (*LDSP(f32x2, L_YS + 1024 + 8 * lane) + *LDSP(f32x2, L_YS + 1536 + 8 * lane));
      AN_PUT(3, 244u); AN_GET(3, 258u);
      wait_slots<8>(lds, C::T_YB);
      st_sc1(v4u{__float_as_uint(yb[0]), tag, __float_as_uint(yb[1]), tag}, rs, W_YB + wg * 1024 + (8 * n + 2 * g) * 8, 0);
    }
#pragma unroll 1
    for (int pass = 0; pass < 2; ++pass) {
      if (pass == 0) wait_slots<8>(lds, NSL - 1); else lds_wait_ge<1>(lds, L_YPUB + 4, 4u);
      const int yo = pass == 0 ? W_YB + ((lane >> 3) + 8 * (4 * b + (h >> 3))) * 1024 + (h & 7) * 128 + (lane & 7) * 16
                               : W_YA + (b * 32 * 512 + (lane >> 3) * 512 + 16 * h + 2 * (lane & 7)) * 8;
      v4u y[4]; bool ok[4];
#pragma unroll
      for (int i = 0; i < 4; ++i) { y[i] = v4u{0, 0, 0, 0}; ok[i] = pass == 0 && i > 0; }
#pragma unroll 1
      for (unsigned spn = 0; spn < SPIN_CAP; ++spn) {
        v4u z[4];
#pragma unroll
        for (int i = 0; i < 4; ++i) z[i] = ld_sc1(rs, ok[i] ? OOB : yo, i * 8 * 512 * 8);
        bool all = true;
#pragma unroll
        for (int i = 0; i < 4; ++i) { if (!ok[i]) { y[i] = z[i]; ok[i] = z[i][1] == tag && z[i][3] == tag; } all &= ok[i]; }
        if (__builtin_amdgcn_ballot_w64(!all) == 0) break;
        __builtin_amdgcn_s_sleep(2);
      }
      float s0 = (__uint_as_float(y[0][0]) + __uint_as_float(y[1][0])) + (__uint_as_float(y[2][0]) + __uint_as_float(y[3][0]));
      float s1 = (__uint_as_float(y[0][2]) + __uint_as_float(y[1][2])) + (__uint_as_float(y[2][2]) + __uint_as_float(y[3][2]));
      s0 += dpp<ROR8>(s0); s1 += dpp<ROR8>(s1);
      s0 = x32_sum(x16_sum(s0)); s1 = x32_sum(x16_sum(s1));
      if (lane < 8) *(GAS unsigned*)(out + b * 1024 + (pass == 0 ? 512 : 0) + 16 * h + 2 * lane) = cvtpk(s0, s1);
    }
    if (wg == 0 && lane == 0) __hip_atomic_store((GAS unsigned*)(ws + W_EPOCH), tag, __ATOMIC_RELAXED, __HIP_MEMORY_SCOPE_AGENT);
  }
}

extern "C" void kernel_launch(void* const* d_in, const int* in_sizes, int n_in, void* d_out, int out_size, void* d_ws, size_t ws_size, hipStream_t stream) {
  (void)in_sizes; (void)n_in; (void)out_size; (void)ws_size;
  (void)hipFuncSetAttribute((const void*)fused_decode, hipFuncAttributeMaxDynamicSharedMemorySize, LDS_BYTES);
  hipLaunchKernelGGL(fused_decode, dim3(256), dim3(NTHR), LDS_BYTES, stream,
                     (const u16*)d_in[0], (const u16*)d_in[1], (const u16*)d_in[2], (const u16*)d_in[3], (const u16*)d_in[4],
                     (const u16*)d_in[5], (const u16*)d_in[6], (const u16*)d_in[7], (const u16*)d_in[8], (const u16*)d_in[9],
                     (u16*)d_out, (unsigned char*)d_ws);
}
```

```cpp
#include <hip/hip_runtime.h>
#include <hip/hip_bf16.h>
#include <stdint.h>

typedef unsigned v4u __attribute__((ext_vector_type(4)));
typedef unsigned v2u __attribute__((ext_vector_type(2)));
typedef int v4i __attribute__((ext_vector_type(4)));
typedef float f32x4 __attribute__((ext_vector_type(4)));
typedef float f32x2 __attribute__((ext_vector_type(2)));
typedef short s16x4 __attribute__((ext_vector_type(4)));
typedef __bf16 bf16x8 __attribute__((ext_vector_type(8)));
typedef __bf16 bf16x2 __attribute__((ext_vector_type(2)));
typedef unsigned short u16;
typedef unsigned long long u64;
#define GAS __attribute__((address_space(1)))
#define LAS __attribute__((address_space(3)))

struct C {
  static constexpr int NFL = 20;
  static constexpr int MUL = 11;
  static constexpr int PACE = 119, PACE2 = 112;
  static constexpr int PACE0 = 20, MARGIN = 20;
  static constexpr int DN = 40, U1 = 60, U2 = 110, U3 = 160, U4 = 220;
  static constexpr int T2 = 264, GAIN2 = 3;
  static constexpr int CODE_LINES = 152;
  static constexpr int T_SLICE = 3, T_GATHER = 18, T_ML = 216, T_YB = 314;
};

constexpr int SK = 4096, DH = 128, NH = 32, HKV = 8;
constexpr float SC2 = 0.088388347648318440f * 1.4426950408889634f;
constexpr int NWAVE = 10, NTHR = NWAVE * 64;
constexpr int NSL = 320;
constexpr int PARK = 4;
constexpr int RING = 15, SLOT = 8192;
constexpr int L_FULL = 0  , L_QRDY = 4  , L_BCNT = 12, L_DONE = 16  , L_RCNT = 32  , L_YPUB = 40  ,
              L_TAG = 48, L_MLC = 52, L_IMPL = 56, L_BASE = 60;
constexpr int L_QIMG = 64;
constexpr int L_WGT = L_QIMG + 1280;
constexpr int L_ML = L_WGT + 128;
constexpr int L_HREC = L_ML + 256;
constexpr int L_JUNK = 7936;
constexpr int L_S = 8192;
constexpr int L_P = L_S + 16384;
constexpr int L_YS = L_P + 8192;
constexpr int L_RING = L_P + 16384;
constexpr int LDS_BYTES = 160 * 1024;
static_assert(L_HREC % 16 == 0 && L_HREC + 5 * 1024 <= 7168 && L_RING + RING * SLOT <= LDS_BYTES, "lds map");
constexpr int W_EPOCH = 0;
constexpr int W_ML = 4096;
constexpr int W_AN = 49152;
constexpr int W_QG = 65536;
constexpr int W_PO = W_QG + 2 * 256 * 512;
constexpr int W_YB = W_PO + 256 * 1024;
constexpr int W_YA = W_YB + 256 * 1024;
constexpr int W_END = W_YA + 8 * 32 * 512 * 8;
constexpr unsigned SPIN_CAP = 1u << 20;
constexpr int OOB = 0x7FFFFF00;

__device__ __forceinline__ unsigned cvtpk(float lo, float hi) { return __builtin_bit_cast(unsigned, __builtin_convertvector(f32x2{lo, hi}, bf16x2)); }
__device__ __forceinline__ __amdgpu_buffer_rsrc_t mk(const void* p, unsigned bytes) { return __builtin_amdgcn_make_buffer_rsrc((void*)p, 0, bytes, 0x00020000); }
__device__ __forceinline__ v4i mksrd(const void* p, unsigned bytes) { return v4i{(int)(uintptr_t)p, (int)((uintptr_t)p >> 32), (int)bytes, 0x00020000}; }
__device__ __forceinline__ f32x4 mfma32(v4u a, v4u b, f32x4 c) { return __builtin_amdgcn_mfma_f32_16x16x32_bf16(__builtin_bit_cast(bf16x8, a), __builtin_bit_cast(bf16x8, b), c, 0, 0, 0); }
__device__ __forceinline__ f32x4 mfma16(v2u a, v2u b, f32x4 c) { return __builtin_amdgcn_mfma_f32_16x16x16bf16_1k(__builtin_bit_cast(s16x4, a), __builtin_bit_cast(s16x4, b), c, 0, 0, 0); }
__device__ __forceinline__ float ex2(float x) { return __builtin_amdgcn_exp2f(x); }
__device__ __forceinline__ v4u ld128(__amdgpu_buffer_rsrc_t r, int vo, int so) { return __builtin_amdgcn_raw_buffer_load_b128(r, vo, so, 0); }
__device__ __forceinline__ v4u ld_sc1(__amdgpu_buffer_rsrc_t r, int vo, int so) { return __builtin_amdgcn_raw_buffer_load_b128(r, vo, so, 16); }
__device__ __forceinline__ void st_sc1(v4u v, __amdgpu_buffer_rsrc_t r, int vo, int so) { __builtin_amdgcn_raw_buffer_store_b128(v, r, vo, so, 16); }
template <int CTRL> __device__ __forceinline__ float dpp(float x) { return __builtin_bit_cast(float, __builtin_amdgcn_mov_dpp(__builtin_bit_cast(int, x), CTRL, 0xf, 0xf, true)); }
constexpr int XOR1 = 0xB1, XOR2 = 0x4E, HMIR = 0x141, ROR8 = 0x128;
__device__ __forceinline__ float x16_sum(float x) { auto s = __builtin_amdgcn_permlane16_swap(__float_as_uint(x), __float_as_uint(x), false, false); return __uint_as_float(s[0]) + __uint_as_float(s[1]); }
__device__ __forceinline__ float x32_sum(float x) { auto s = __builtin_amdgcn_permlane32_swap(__float_as_uint(x), __float_as_uint(x), false, false); return __uint_as_float(s[0]) + __uint_as_float(s[1]); }
__device__ __forceinline__ float xg_max(float x) {
  auto s = __builtin_amdgcn_permlane16_swap(__float_as_uint(x), __float_as_uint(x), false, false);
  x = fmaxf(__uint_as_float(s[0]), __uint_as_float(s[1]));
  auto t = __builtin_amdgcn_permlane32_swap(__float_as_uint(x), __float_as_uint(x), false, false);
  return fmaxf(__uint_as_float(t[0]), __uint_as_float(t[1]));
}
__device__ __forceinline__ int fresh_lane() {
  int l; asm volatile("v_mbcnt_lo_u32_b32 %0, -1, 0\n\tv_mbcnt_hi_u32_b32 %0, -1, %0" : "=v"(l)); return l;
}
#define CBAR() asm volatile("" ::: "memory")
#define LDSP(T, off) ((LAS T*)(lds + (off)))
__device__ __forceinline__ unsigned lds_ld(LAS unsigned char* lds, int off) { return __builtin_amdgcn_readfirstlane(*(volatile LAS unsigned*)(lds + off)); }
__device__ __forceinline__ void lds_st(LAS unsigned char* lds, int off, unsigned v) { *(volatile LAS unsigned*)(lds + off) = v; }
template <int SLP> __device__ __forceinline__ unsigned lds_wait_ge(LAS unsigned char* lds, int off, unsigned want) {
  unsigned v = lds_ld(lds, off);
#pragma unroll 1
  for (unsigned sp = 0; v < want && sp < SPIN_CAP * 32 / (SLP + 1); ++sp) { if (SLP) __builtin_amdgcn_s_sleep(SLP); v = lds_ld(lds, off); }
  CBAR();
  return v;
}
template <int SLP> __device__ __forceinline__ void wait_slots(LAS unsigned char* lds, unsigned slots) { lds_wait_ge<SLP>(lds, L_FULL, 2 * slots); }
__device__ __forceinline__ void lds_inc(LAS unsigned char* lds, int off, int lane) {
  CBAR();
  if (lane == 0) __hip_atomic_fetch_add(LDSP(unsigned, off), 1u, __ATOMIC_RELAXED, __HIP_MEMORY_SCOPE_WORKGROUP);
  CBAR();
}

template <int N> __device__ __forceinline__ void dma4(unsigned m0v, unsigned v0, unsigned v1, unsigned v2, unsigned v3, v4i srd, unsigned so) {
  asm volatile("s_mov_b32 m0, %0\n\t"
               "s_waitcnt vmcnt(%7)\n\tbuffer_load_dwordx4 %1, %5, %6 offen nt lds\n\t"
               "s_waitcnt vmcnt(%7)\n\tbuffer_load_dwordx4 %2, %5, %6 offen offset:1024 nt lds\n\t"
               "s_waitcnt vmcnt(%7)\n\tbuffer_load_dwordx4 %3, %5, %6 offen offset:2048 nt lds\n\t"
               "s_waitcnt vmcnt(%7)\n\tbuffer_load_dwordx4 %4, %5, %6 offen offset:3072 nt lds"
               :: "s"(m0v), "v"(v0), "v"(v1), "v"(v2), "v"(v3), "s"(srd), "s"(so), "i"(N - 1) : "memory");
}

__global__ void __launch_bounds__(NTHR, 3) fused_decode(const u16* xa, const u16* ak, const u16* av, const u16* wqa, const u16* woa,
                                                        const u16* xb, const u16* bk, const u16* bv, const u16* wqb, const u16* wob,
                                                        u16* out, unsigned char* ws) {
  constexpr int NFL = C::NFL;
  {
    u64 pc; unsigned tv;
    asm volatile("s_getpc_b64 %0\n\tv_min_u32 %1, %3, %2\n\ts_movk_i32 m0, %4\n\tv_lshlrev_b32 %1, 7, %1\n\tglobal_load_lds_dword %1, %0"
                 : "=&s"(pc), "=&v"(tv) : "v"(threadIdx.x), "i"(C::CODE_LINES - 1), "i"(L_JUNK));
    __builtin_amdgcn_sched_barrier(0);
  }
  extern __shared__ __attribute__((aligned(16))) unsigned char lds_raw[];
  LAS unsigned char* lds = (LAS unsigned char*)lds_raw;
  const int wg = blockIdx.x, lane = threadIdx.x & 63, wave = __builtin_amdgcn_readfirstlane(threadIdx.x >> 6);
  const int xcd = wg & 7, jw = wg >> 3, b = jw >> 2, sp = jw & 3, h = 4 * xcd + sp;
  const int g = lane >> 4, n = lane & 15;
  *(volatile LAS unsigned*)(lds + 4 * n) = 0u;
  asm volatile("s_waitcnt lgkmcnt(0)" :: "s"(wqb), "s"(wob), "s"(out), "s"(ws), "s"(xa) : "memory");
  __builtin_amdgcn_s_barrier();
  const unsigned k0 = g * 256 + ((n ^ g) * 16);
#define SLOT_OFF(r_) (((((r_) & 0x70u) << 1) | ((r_) & 15u)) << 12)
  {
#define E4(m_, so_) "s_mov_b32 m0, " m_ "\n\ts_mov_b32 %0, " so_ "\n\t" \
               "buffer_load_dwordx4 %2, %6, %0 offen nt lds\n\tbuffer_load_dwordx4 %3, %6, %0 offen offset:1024 nt lds\n\t" \
               "buffer_load_dwordx4 %4, %6, %0 offen offset:2048 nt lds\n\tbuffer_load_dwordx4 %5, %6, %0 offen offset:3072 nt lds\n\t"
    unsigned tmp;
    asm volatile("s_cmp_lg_u32 %1, 0\n\ts_cbranch_scc1 1f\n\t"
                 E4("%7", "0") E4("%7+4096", "65536") E4("%7+8192", "%8") E4("%7+12288", "%8+65536") E4("%7+16384", "%9") E4("%7+20480", "%9+65536")
                 "1:"
                 : "=&s"(tmp) : "s"(wave), "v"(k0), "v"(k0 ^ 64), "v"(k0 ^ 128), "v"(k0 ^ 192), "s"(mksrd(ak + (size_t)(b * NH + h) * SK * DH, SK * 256)),
                   "i"(L_RING), "i"(SLOT_OFF(C::MUL)), "i"(SLOT_OFF(2 * C::MUL)) : "memory", "scc");
  }

  if (wave == 0) {
    __builtin_amdgcn_s_setprio(3);
    const size_t ua = (size_t)(b * NH + h) * SK * DH, ub = (size_t)(b * HKV + xcd) * SK * DH;
    const int ll = fresh_lane();
    const unsigned vv = ll * 16, k0 = (ll >> 4) * 256 + (((ll & 15) ^ (ll >> 4)) * 16);
    unsigned slot = L_RING + 3 * SLOT;
    int limit = RING;
    u64 now = __builtin_amdgcn_s_memrealtime();
    unsigned base = (unsigned)now + C::PACE0, due = -C::PACE;
    now = base + due;
    int t = 3;
#pragma unroll 1
    for (int ph = 0; ph < 4; ++ph) {
      const v4i rs = mksrd(ph == 0 ? ak + ua : ph == 1 ? bk + ub : ph == 2 ? bv + ub : av + ua, SK * 256);
      const int tend = ph == 0 ? 128 : ph == 1 ? 160 : ph == 2 ? 192 : NSL;
      const unsigned v0 = ph < 2 ? k0 : vv, v1 = ph < 2 ? k0 ^ 64 : vv, v2 = ph < 2 ? k0 ^ 128 : vv, v3 = ph < 2 ? k0 ^ 192 : vv;
      unsigned r = (ph == 0 ? 3 * C::MUL : ph == 3 ? 0 : sp * 32 * C::MUL) & 127u;
#pragma unroll 1
      while (t < tend) {
        if (t + 3 >= limit) {
#pragma unroll 1
          for (unsigned spn = 0; spn < SPIN_CAP * 16; ++spn) {
            const v4u d = *(volatile LAS v4u*)(lds + L_DONE);
            const unsigned nb = lds_ld(lds, L_BASE); base = nb ? nb : base;
            const int c = min(min(4 * (int)d[0], 4 * (int)d[1] + 1), min(4 * (int)d[2] + 2, 4 * (int)d[3] + 3));
            limit = __builtin_amdgcn_readfirstlane(c) + RING;
            if (t + 3 < limit) break;
            __builtin_amdgcn_s_sleep(1);
          }
        }
#define RUN4(N_) do { static_assert(((N_) + 3) / 4 >= 5 && ((N_) + 3) / 4 <= 7, "the published count never goes down"); \
        _Pragma("unroll 1") for (const int te = (t | 3) + 1; t < te; ++t) { \
          const unsigned so = SLOT_OFF(r); \
          dma4<N_>(slot, v0, v1, v2, v3, rs, so); \
          dma4<N_>(slot + 4096, v0, v1, v2, v3, rs, so + 65536u); \
          asm volatile("s_waitcnt lgkmcnt(0)");     \
          lds_st(lds, L_FULL, (unsigned)(2 * t + 2 - ((N_) + 3) / 4)); \
          slot += SLOT; if (slot >= L_RING + RING * SLOT) slot = L_RING; \
          r = (r + C::MUL) & 127u; } } while (0)
        asm volatile("" : "+s"(now));
        lds_st(lds, L_IMPL, (unsigned)now - due);
        const int lagr = (int)((unsigned)now - due - base), lag = t >= C::T2 ? lagr * C::GAIN2 : lagr;
        due += ph == 0 ? C::PACE : C::PACE2;
        asm volatile("s_memrealtime %0" : "=s"(now));
        if (lag < C::U1) { if (lag >= -C::DN) RUN4(NFL); else RUN4(NFL - 1); }
        else if (lagr < C::U3) { if (lag < C::U2) RUN4(NFL + 1); else RUN4(NFL + 2); }
        else if (lagr < C::U4) RUN4(NFL + 3); else RUN4(NFL + 5);
      }
    }
    asm volatile("s_waitcnt vmcnt(16)" ::: "memory"); lds_st(lds, L_FULL, 2 * NSL - 4);
    asm volatile("s_waitcnt vmcnt(12)" ::: "memory"); lds_st(lds, L_FULL, 2 * NSL - 3);
    asm volatile("s_waitcnt vmcnt(8)" ::: "memory");  lds_st(lds, L_FULL, 2 * NSL - 2);
    asm volatile("s_waitcnt vmcnt(4)" ::: "memory");  lds_st(lds, L_FULL, 2 * NSL - 1);
    asm volatile("s_waitcnt vmcnt(0)" ::: "memory");  lds_st(lds, L_FULL, 2 * NSL);
  } else if (wave >= 2 && wave < 6) {
    const int w = wave - 2, jj = n >> 2;
    int slot = L_RING + w * SLOT; unsigned full = 0, cnt = 0; int t = w;
#define HALF_WAIT(k_, slp_) do { if (full < (unsigned)(2 * t + (k_))) full = lds_wait_ge<slp_>(lds, L_FULL, (unsigned)(2 * t + (k_))); CBAR(); } while (0)
#define SLOT_DONE() do { CBAR(); ++cnt; lds_st(lds, L_DONE + 4 * w, cnt);     \
        CBAR(); slot += 4 * SLOT; if (slot >= L_RING + RING * SLOT) slot -= RING * SLOT; t += 4; } while (0)
#define K_READ(T_) do { HALF_WAIT(2, 1); _Pragma("unroll") for (int m_ = 0; m_ < 8; ++m_) T_[m_] = *LDSP(v4u, ((slot + kb) ^ (64 * (m_ & 3))) + (m_ >> 2) * 4096); SLOT_DONE(); } while (0)
    const int sw = L_S + w * 4096;
    {
      const int kb = n * 256 + ((g ^ n) * 16);
      v4u q[4];
      float mx = -3.0e38f;
      const int sa0 = sw + ((n & 3) * 8 + g * 2 + (jj & 1)) * 16;
      int sa = sa0;
#define K_MATH(T_, act_, step_) do { f32x4 s0 = {0.f, 0.f, 0.f, 0.f}, s1 = {0.f, 0.f, 0.f, 0.f}; \
        _Pragma("unroll") for (int m = 0; m < 4; ++m) { s0 = mfma32(T_[m], q[m], s0); s1 = mfma32(T_[4 + m], q[m], s1); }       \
        mx = fmaxf(fmaxf(fmaxf(fmaxf(s0[0], s0[1]), fmaxf(s0[2], s0[3])), fmaxf(fmaxf(s1[0], s1[1]), fmaxf(s1[2], s1[3]))), mx); \
        if (act_) *LDSP(f32x4, sa) = (jj & 1) ? s1 : s0; \
        sa += (step_); } while (0)
      {
        v4u P[PARK][8];
#pragma unroll
        for (int i = 0; i < PARK; ++i) K_READ(P[i]);
        lds_wait_ge<1>(lds, L_QRDY, 1u);
#pragma unroll
        for (int m = 0; m < 4; ++m) q[m] = *LDSP(v4u, L_QIMG + g * 16 + 64 * m);
#pragma unroll
        for (int i = 0; i < PARK; ++i) { K_MATH(P[i], (n & 11) == 0, 128); if (i < 2) K_READ(P[i]); }
        K_MATH(P[0], (n & 11) == 0, 128); K_MATH(P[1], (n & 11) == 0, 128);
      }
#pragma unroll 1
      for (int ph = 0; ph < 2; ++ph) {
        const bool act = ph == 1 ? n < 8 : (n & 11) == 0;
        const int sstep = ph == 1 ? 512 : 128, tend = ph == 0 ? 128 : 160;
#pragma unroll 1
        while (t < tend) { v4u T[8]; K_READ(T); K_MATH(T, act, sstep); }
        lds_wait_ge<1>(lds, L_QRDY + 4, 1u);
        const float Mh = xg_max(mx);
        const float M = __builtin_bit_cast(float, __builtin_amdgcn_ds_bpermute(((lane >> 3) & 3) * 4, __builtin_bit_cast(int, Mh)));
        const float mc = -M * SC2;
        float l = 0.f;
        CBAR();
#pragma unroll
        for (int j = 0; j < 4; ++j) {
          const f32x4 s = *LDSP(f32x4, sw + 16 * lane + 1024 * j);
          const float p0 = ex2(fmaf(s[0], SC2, mc)), p1 = ex2(fmaf(s[1], SC2, mc)), p2 = ex2(fmaf(s[2], SC2, mc)), p3 = ex2(fmaf(s[3], SC2, mc));
          l += (p0 + p1) + (p2 + p3);
          *LDSP(v2u, L_P + ph * 8192 + w * 2048 + 8 * lane + 512 * j) = v2u{cvtpk(p0, p1), cvtpk(p2, p3)};
        }
        l += dpp<XOR1>(l); l += dpp<XOR2>(l); l += dpp<HMIR>(l); l = x32_sum(l);
        const float la = x16_sum(l + dpp<ROR8>(l));
        if ((lane & 39) == 0) *LDSP(f32x2, L_ML + ph * 128 + w * 32 + lane) = f32x2{M, ph == 1 ? l : la};
        lds_inc(lds, L_MLC, lane);
        mx = -3.0e38f; sa = sa0;
#pragma unroll
        for (int m = 0; m < 4; ++m) q[m] = *LDSP(v4u, L_QIMG + 256 + (n & 3) * 256 + g * 16 + 64 * m);
      }
    }
    {
      const unsigned Z = 0x0C0C0C0Cu;
      const unsigned el0 = jj == 0 ? 0x0C0C0100u : jj == 1 ? 0x01000C0Cu : Z, el1 = jj == 2 ? 0x0C0C0100u : jj == 3 ? 0x01000C0Cu : Z;
      const unsigned eh0 = jj == 0 ? 0x0C0C0302u : jj == 1 ? 0x03020C0Cu : Z, eh1 = jj == 2 ? 0x0C0C0302u : jj == 3 ? 0x03020C0Cu : Z;
      const int vb = g * 1024 + n * 16;
#pragma unroll 1
      for (int ph = 1; ph >= 0; --ph) {
        if (ph == 0) {
          lds_wait_ge<1>(lds, L_MLC, 8u);
          f32x2 ml[4];
#pragma unroll
          for (int u = 0; u < 4; ++u) ml[u] = *LDSP(f32x2, L_ML + u * 32);
          const float M = fmaxf(fmaxf(ml[0][0], ml[1][0]), fmaxf(ml[2][0], ml[3][0]));
          float L = 0.f;
#pragma unroll
          for (int u = 0; u < 4; ++u) L = fmaf(ex2((ml[u][0] - M) * SC2), ml[u][1], L);
          *LDSP(float, L_WGT + 64 + 4 * w) = ex2(((*LDSP(f32x2, L_ML + w * 32))[0] - M) * SC2) / L;
        }
        const int tend = ph == 1 ? 192 : NSL, pstep = ph == 1 ? 256 : 64;
        int pa = L_P + ph * 8192 + w * 2048 + (ph == 1 ? (n & 3) * 64 : 0) + g * 16;
        f32x4 a0 = {0, 0, 0, 0}, a1 = {0, 0, 0, 0};
#pragma unroll 1
        while (t < tend) {
          const v4u pp = *LDSP(v4u, pa); pa += pstep;
          v2u bf[8];
#pragma unroll
          for (int i = 0; i < 8; ++i) { const unsigned pk = pp[i >> 1]; bf[i] = v2u{__builtin_amdgcn_perm(pk, pk, (i & 1) ? eh0 : el0), __builtin_amdgcn_perm(pk, pk, (i & 1) ? eh1 : el1)}; }
#pragma unroll
          for (int i = 0; i < 8; ++i) asm volatile("" : "+v"(bf[i]));
          v4u T[8];
          HALF_WAIT(1, 1);
#pragma unroll
          for (int i = 0; i < 4; ++i) T[i] = *LDSP(v4u, slot + vb + i * 256);
#pragma unroll
          for (int i = 0; i < 4; ++i) { a0 = mfma16(v2u{T[i][0], T[i][1]}, bf[i], a0); a1 = mfma16(v2u{T[i][2], T[i][3]}, bf[i], a1); }
          asm volatile("" : "+v"(a0), "+v"(a1));
          HALF_WAIT(2, 0);
#pragma unroll
          for (int i = 4; i < 8; ++i) T[i] = *LDSP(v4u, slot + vb + 4096 + (i & 3) * 256);
          SLOT_DONE();
#pragma unroll
          for (int i = 4; i < 8; ++i) { a0 = mfma16(v2u{T[i][0], T[i][1]}, bf[i], a0); a1 = mfma16(v2u{T[i][2], T[i][3]}, bf[i], a1); }
        }
        if ((n & 3) == 0) *LDSP(v4u, L_HREC + 4096 + w * 256 + jj * 64 + g * 16) = v4u{cvtpk(a0[0], a1[0]), cvtpk(a0[1], a1[1]), cvtpk(a0[2], a1[2]), cvtpk(a0[3], a1[3])};
        if (ph == 1) {
          const int ro = sw + (n & 3) * 512 + jj * 128 + g * 32;
          *LDSP(f32x4, ro) = f32x4{a0[0], a1[0], a0[1], a1[1]};
          *LDSP(f32x4, ro + 16) = f32x4{a0[2], a1[2], a0[3], a1[3]};
        }
        lds_inc(lds, ph == 1 ? L_BCNT : L_RCNT + 4, lane);
      }
    }
  } else if (wave >= 6) {
    const int w = wave - 6;
#pragma unroll 1
    for (int mat = 0; mat < 2; ++mat) {
      if (mat) wait_slots<16>(lds, 32u);
      const __amdgpu_buffer_rsrc_t rw = mk((mat ? wqb : wqa) + (size_t)(16 * jw + w) * 4096 + xcd * 512, 13 * 8192);
      v4u d[4];
#pragma unroll
      for (int i = 0; i < 4; ++i) d[i] = ld128(rw, lane * 16, ((i & 1) * 8 + (i >> 1) * 4) * 8192);
#pragma unroll
      for (int i = 0; i < 4; ++i) asm volatile("" :: "v"(d[i]));
    }
    const __amdgpu_buffer_rsrc_t ry = mk(ws + W_YA, W_END - W_YA);
    const int yo = ((b * 32 + h) * 512 + 128 * w + 8 * n + 2 * g) * 8;
    wait_slots<16>(lds, 72u);
#pragma unroll 1
    for (int pass = 0; pass < 2; ++pass) {
      const __amdgpu_buffer_rsrc_t rw = mk((pass == 0 ? wob : woa) + (size_t)xcd * 512 * 512, 512 * 1024);
      {
        v4u d[4];
#pragma unroll
        for (int i = 0; i < 4; ++i) d[i] = ld128(rw, (2 * b + (i >> 1) + 16 * (i & 1)) * 4096 + lane * 16, sp * 131072 + w * 1024);
#pragma unroll
        for (int i = 0; i < 4; ++i) asm volatile("" :: "v"(d[i]));
        lds_wait_ge<16>(lds, L_FULL, min(lds_ld(lds, L_FULL) + 24u, 2u * NSL));
      }
      const int wv = (pass == 0 ? w * 131072 + sp * 256 : sp * 131072 + w * 256) + g * 32768 + n * 16;
      v4u F[4][8];
#pragma unroll
      for (int s = 0; s < 4; ++s) {
#pragma unroll
        for (int jp = 0; jp < 4; ++jp) {
          const v4u w0 = ld128(rw, wv, (8 * jp + s) * 1024), w1 = ld128(rw, wv, (8 * jp + 4 + s) * 1024);
#pragma unroll
          for (int d = 0; d < 4; ++d) {
            unsigned lo = __builtin_amdgcn_perm(w1[d], w0[d], 0x05040100u), hi = __builtin_amdgcn_perm(w1[d], w0[d], 0x07060302u);
            asm volatile("" : "+v"(lo), "+v"(hi));
            F[s][2 * d][jp] = lo; F[s][2 * d + 1][jp] = hi;
          }
          __builtin_amdgcn_sched_barrier(0);
        }
#pragma unroll
        for (int c = 0; c < 8; ++c) asm volatile("" : "+v"(F[s][c]));
      }
      const int hr = pass == 0 ? w : 4;
      lds_wait_ge<4>(lds, L_RCNT + 4 * pass, 2u);
      lds_wait_ge<0>(lds, L_RCNT + 4 * pass, 4u);
      v4u of[4];
#pragma unroll
      for (int s = 0; s < 4; ++s) of[s] = *LDSP(v4u, L_HREC + hr * 1024 + (n & 3) * 256 + s * 64 + g * 16);
      const unsigned tag = *LDSP(unsigned, L_TAG);
      const f32x4 wt = *LDSP(f32x4, L_WGT + hr * 16);
      f32x4 da = {0, 0, 0, 0}, dc = {0, 0, 0, 0};
      __builtin_amdgcn_sched_barrier(0);
#pragma unroll
      for (int cp = 0; cp < 4; ++cp) {
        f32x4 d0 = {0, 0, 0, 0}, d1 = {0, 0, 0, 0};
#pragma unroll
        for (int s = 0; s < 4; ++s) { d0 = mfma32(of[s], F[s][2 * cp], d0); d1 = mfma32(of[s], F[s][2 * cp + 1], d1); }
#pragma unroll
        for (int r = 0; r < 4; ++r) { da[r] = (cp == 0 || g == cp) ? d0[r] : da[r]; dc[r] = (cp == 0 || g == cp) ? d1[r] : dc[r]; }
      }
#pragma unroll
      for (int i = 0; i < 18; ++i) __builtin_amdgcn_sched_group_barrier(0x8, 1, 0);
#pragma unroll
      for (int i = 0; i < 14; ++i) { __builtin_amdgcn_sched_group_barrier(0x8, 1, 0); __builtin_amdgcn_sched_group_barrier(0x2, 1, 0); }
      const float ya = fmaf(wt[0], da[0], wt[1] * da[1]) + fmaf(wt[2], da[2], wt[3] * da[3]), yc = fmaf(wt[0], dc[0], wt[1] * dc[1]) + fmaf(wt[2], dc[2], wt[3] * dc[3]);
      st_sc1(v4u{__float_as_uint(ya), tag, __float_as_uint(yc), tag}, ry, pass == 0 ? OOB : yo, 0);
      if (pass == 0) *LDSP(f32x2, L_YS + w * 512 + 8 * lane) = f32x2{ya, yc};
      lds_inc(lds, L_YPUB + 4 * pass, lane);
    }
  } else {
    const unsigned tag = __builtin_amdgcn_readfirstlane(__hip_atomic_load((GAS unsigned*)(ws + W_EPOCH), __ATOMIC_RELAXED, __HIP_MEMORY_SCOPE_AGENT)) + 1u;
    lds_st(lds, L_TAG, tag);
    const __amdgpu_buffer_rsrc_t rs = mk(ws, W_END);
#define AN_PUT(k_, at_) do { if (sp == 0) { wait_slots<16>(lds, at_); const unsigned im = lds_ld(lds, L_IMPL); \
      st_sc1(v4u{im, tag, im, tag}, rs, lane == 0 ? W_AN + (k_) * 1024 + (b * 8 + xcd) * 16 : OOB, 0); } } while (0)
#define AN_GET(k_, at_) do { { wait_slots<16>(lds, at_); const unsigned im = lds_ld(lds, L_IMPL); \
      const v4u a = ld_sc1(rs, W_AN + (k_) * 1024 + lane * 16, 0); \
      const bool okv = a[1] == tag && a[3] == tag; const int cntv = __builtin_popcountll(__builtin_amdgcn_ballot_w64(okv)); \
      float dv = okv ? (float)(int)(a[0] - im) : 0.f; \
      dv += dpp<XOR1>(dv); dv += dpp<XOR2>(dv); dv += dpp<HMIR>(dv); dv += dpp<ROR8>(dv); dv = x32_sum(x16_sum(dv)); \
      if (cntv >= 32) lds_st(lds, L_BASE, im + (int)(dv / (float)cntv) + C::MARGIN); } } while (0)
#pragma unroll 1
    for (int it = 0; it < 2; ++it) {
      if (it == 1) { AN_PUT(0, 30u); AN_GET(0, 46u); }
      if ((jw >> 4) == it) {
        const int cb = xcd * 32 + 2 * (jw & 15);
        v4u xf[16];
        const __amdgpu_buffer_rsrc_t rx = mk(it ? xb : xa, 8 * 512 * 2);
        if (it) wait_slots<16>(lds, 52u);
#pragma unroll
        for (int s = 0; s < 16; ++s) xf[s] = ld128(rx, n * 1024 + 16 * g + 64 * s, 0);
        if (!it) wait_slots<1>(lds, C::T_SLICE);
        const v4i rw = mksrd((it ? wqb : wqa) + cb * 16, 512 * 8192 - cb * 32);
        const unsigned vo = (lane >> 2) * 8192 + (lane & 3) * 16;
        f32x4 d0 = {0, 0, 0, 0}, d1 = {0, 0, 0, 0};
#pragma unroll
        for (int hf = 0; hf < 2; ++hf) {
          if (it || hf == 0) {
#pragma unroll 1
            for (int i = 0; i < (it ? 16 : 32); ++i) {
              asm volatile("s_mov_b32 m0, %0\n\ts_nop 0\n\tbuffer_load_dwordx4 %1, %2, %3 offen nt lds" :: "s"((it ? L_P : L_S) + i * 1024), "v"(vo), "s"(rw), "s"((hf * 16 + i) * 16 * 8192) : "memory");
              if (it) __builtin_amdgcn_s_sleep(6);
            }
            asm volatile("s_waitcnt vmcnt(0)" ::: "memory");
          }
          const int ta = ((it || hf) ? L_P : L_S) + (8 * g + (n >> 2)) * 64 + (n & 3) * 8;
          v2u t[8][4];
#pragma unroll
          for (int s = 0; s < 8; ++s)
#pragma unroll
            for (int u = 0; u < 4; ++u) t[s][u] = __builtin_bit_cast(v2u, __builtin_amdgcn_ds_read_tr16_b64_v4i16(LDSP(s16x4, ta + s * 2048 + (u & 1) * 256 + (u >> 1) * 32)));
          __builtin_amdgcn_sched_barrier(0);
#pragma unroll
          for (int s = 0; s < 8; ++s) {
            d0 = mfma32(v4u{t[s][0][0], t[s][0][1], t[s][1][0], t[s][1][1]}, xf[8 * hf + s], d0);
            d1 = mfma32(v4u{t[s][2][0], t[s][2][1], t[s][3][0], t[s][3][1]}, xf[8 * hf + s], d1);
          }
          asm volatile("" : "+v"(d0), "+v"(d1) :: "memory");
        }
        d1 = f32x4{dpp<ROR8>(d1[0]), dpp<ROR8>(d1[1]), dpp<ROR8>(d1[2]), dpp<ROR8>(d1[3])};
        const f32x4 dd = n < 8 ? d0 : d1;
        st_sc1(v4u{cvtpk(dd[0], dd[1]), tag, cvtpk(dd[2], dd[3]), tag}, rs, W_QG + (it * 256 + cb) * 512 + n * 64 + g * 16, 0);
      }
      wait_slots<4>(lds, it ? 104u : C::T_GATHER);
      const int go = it == 0 ? (lane < 32 ? W_QG + (h * 8 + (lane >> 2)) * 512 + b * 64 + (lane & 3) * 16 : OOB) : W_QG + (256 + xcd * 32 + (lane >> 2)) * 512 + b * 64 + (lane & 3) * 16;
      const u64 need = it == 0 ? 0xFFFFFFFFull : ~0ull;
#pragma unroll 1
      for (unsigned spn = 0; spn < SPIN_CAP; ++spn) {
        const v4u v = ld_sc1(rs, go, 0), u = ld_sc1(rs, it == 1 ? go : OOB, 16 * 512);
        if (it == 1 || lane < 32) *LDSP(v2u, L_QIMG + (it == 1 ? 256 : 0) + 8 * lane) = v2u{v[0], v[2]};
        if (it == 1) *LDSP(v2u, L_QIMG + 768 + 8 * lane) = v2u{u[0], u[2]};
        if ((__builtin_amdgcn_ballot_w64(v[1] != tag || v[3] != tag || (it == 1 && (u[1] != tag || u[3] != tag))) & need) == 0) break;
        __builtin_amdgcn_s_sleep(8);
      }
      CBAR();
      lds_st(lds, L_QRDY + 4 * it, 1u);
    }
    AN_PUT(1, 112u); AN_GET(1, 128u); AN_PUT(2, 176u); AN_GET(2, 190u);
    lds_wait_ge<16>(lds, L_BCNT, 4u);
    {
      f32x2 ml[4]; f32x4 ac[4][2];
#pragma unroll
      for (int u = 0; u < 4; ++u) { ml[u] = *LDSP(f32x2, L_ML + 128 + u * 32 + g * 8); ac[u][0] = *LDSP(f32x4, L_S + u * 4096 + 32 * lane); ac[u][1] = *LDSP(f32x4, L_S + u * 4096 + 32 * lane + 16); }
      const float M = fmaxf(fmaxf(ml[0][0], ml[1][0]), fmaxf(ml[2][0], ml[3][0]));
      float L = 0.f; f32x4 o0 = {0, 0, 0, 0}, o1 = {0, 0, 0, 0};
#pragma unroll
      for (int u = 0; u < 4; ++u) { const float f = ex2((ml[u][0] - M) * SC2); L = fmaf(f, ml[u][1], L); o0 += f * ac[u][0]; o1 += f * ac[u][1]; }
      const float inv = 1.f / L; o0 *= inv; o1 *= inv;
      st_sc1(v4u{cvtpk(o0[0], o0[1]), cvtpk(o0[2], o0[3]), cvtpk(o1[0], o1[1]), cvtpk(o1[2], o1[3])}, rs, W_PO + wg * 1024 + 16 * lane, 0);
      asm volatile("s_waitcnt vmcnt(0)" ::: "memory");
      st_sc1(v4u{__float_as_uint(M), tag, __float_as_uint(L), tag}, rs, n == 0 ? W_ML + wg * 128 + g * 16 : OOB, 0);
    }
    wait_slots<16>(lds, C::T_ML);
    v4u mlv = {0, 0, 0, 0};
#pragma unroll 1
    for (unsigned spn = 0; spn < SPIN_CAP; ++spn) {
      mlv = ld_sc1(rs, lane < 16 ? W_ML + (xcd + 8 * (4 * b + (lane & 3))) * 128 + (lane >> 2) * 16 : OOB, 0);
      if ((__builtin_amdgcn_ballot_w64(mlv[1] != tag || mlv[3] != tag) & 0xFFFFu) == 0u) break;
      __builtin_amdgcn_s_sleep(64);
    }
    __builtin_amdgcn_fence(__ATOMIC_ACQUIRE, "wavefront");
    {
      v4u p[4];
#pragma unroll
      for (int u = 0; u < 4; ++u) p[u] = ld_sc1(rs, W_PO + (xcd + 32 * b) * 1024 + 16 * lane, u * 8192);
      const float m = __uint_as_float(mlv[0]);
      float M = fmaxf(m, dpp<XOR1>(m)); M = fmaxf(M, dpp<XOR2>(M));
      const float e = ex2((m - M) * SC2) * __uint_as_float(mlv[2]);
      float L = e + dpp<XOR1>(e); L += dpp<XOR2>(L);
      if (lane < 16) *LDSP(float, L_WGT + 4 * lane) = e / L;
#pragma unroll
      for (int u = 0; u < 4; ++u) *LDSP(v4u, L_HREC + g * 1024 + u * 256 + n * 16) = p[u];
      CBAR(); lds_st(lds, L_RCNT, 4u);
    }
    lds_wait_ge<16>(lds, L_YPUB, 4u);
    {
      const f32x2 yb = (*LDSP(f32x2, L_YS + 8 * lane) + *LDSP(f32x2, L_YS + 512 + 8 * lane)) + (*LDSP(f32x2, L_YS + 1024 + 8 * lane) + *LDSP(f32x2, L_YS + 1536 + 8 * lane));
      AN_PUT(3, 244u); AN_GET(3, 258u);
      wait_slots<8>(lds, C::T_YB);
      st_sc1(v4u{__float_as_uint(yb[0]), tag, __float_as_uint(yb[1]), tag}, rs, W_YB + wg * 1024 + (8 * n + 2 * g) * 8, 0);
    }
#pragma unroll 1
    for (int pass = 0; pass < 2; ++pass) {
      if (pass == 0) wait_slots<8>(lds, NSL - 1); else lds_wait_ge<1>(lds, L_YPUB + 4, 4u);
      const int yo = pass == 0 ? W_YB + ((lane >> 3) + 8 * (4 * b + (h >> 3))) * 1024 + (h & 7) * 128 + (lane & 7) * 16
                               : W_YA + (b * 32 * 512 + (lane >> 3) * 512 + 16 * h + 2 * (lane & 7)) * 8;
      v4u y[4]; bool ok[4];
#pragma unroll
      for (int i = 0; i < 4; ++i) { y[i] = v4u{0, 0, 0, 0}; ok[i] = pass == 0 && i > 0; }
#pragma unroll 1
      for (unsigned spn = 0; spn < SPIN_CAP; ++spn) {
        v4u z[4];
#pragma unroll
        for (int i = 0; i < 4; ++i) z[i] = ld_sc1(rs, ok[i] ? OOB : yo, i * 8 * 512 * 8);
        bool all = true;
#pragma unroll
        for (int i = 0; i < 4; ++i) { if (!ok[i]) { y[i] = z[i]; ok[i] = z[i][1] == tag && z[i][3] == tag; } all &= ok[i]; }
        if (__builtin_amdgcn_ballot_w64(!all) == 0) break;
        __builtin_amdgcn_s_sleep(2);
      }
      float s0 = (__uint_as_float(y[0][0]) + __uint_as_float(y[1][0])) + (__uint_as_float(y[2][0]) + __uint_as_float(y[3][0]));
      float s1 = (__uint_as_float(y[0][2]) + __uint_as_float(y[1][2])) + (__uint_as_float(y[2][2]) + __uint_as_float(y[3][2]));
      s0 += dpp<ROR8>(s0); s1 += dpp<ROR8>(s1);
      s0 = x32_sum(x16_sum(s0)); s1 = x32_sum(x16_sum(s1));
      if (lane < 8) *(GAS unsigned*)(out + b * 1024 + (pass == 0 ? 512 : 0) + 16 * h + 2 * lane) = cvtpk(s0, s1);
    }
    if (wg == 0 && lane == 0) __hip_atomic_store((GAS unsigned*)(ws + W_EPOCH), tag, __ATOMIC_RELAXED, __HIP_MEMORY_SCOPE_AGENT);
  }
}

extern "C" void kernel_launch(void* const* d_in, const int* in_sizes, int n_in, void* d_out, int out_size, void* d_ws, size_t ws_size, hipStream_t stream) {
  (void)in_sizes; (void)n_in; (void)out_size; (void)ws_size;
  (void)hipFuncSetAttribute((const void*)fused_decode, hipFuncAttributeMaxDynamicSharedMemorySize, LDS_BYTES);
  hipLaunchKernelGGL(fused_decode, dim3(256), dim3(NTHR), LDS_BYTES, stream,
                     (const u16*)d_in[0], (const u16*)d_in[1], (const u16*)d_in[2], (const u16*)d_in[3], (const u16*)d_in[4],
                     (const u16*)d_in[5], (const u16*)d_in[6], (const u16*)d_in[7], (const u16*)d_in[8], (const u16*)d_in[9],
                     (u16*)d_out, (unsigned char*)d_ws);
}
```
